```python
import torch

def _softmax(x, axis=-1):
    return torch.softmax(x, dim=axis)
def _gelu(x):
    return torch.nn.functional.gelu(x, approximate="tanh")
def _silu(x):
    return torch.nn.functional.silu(x)
def _sigmoid(x):
    return torch.sigmoid(x)
def _layernorm(x, axis=-1, eps=1e-5):
    m = x.mean(dim=axis, keepdim=True)
    v = x.var(dim=axis, keepdim=True, unbiased=False)
    return (x - m) / torch.sqrt(v + eps)
def _scatter_add(tgt, idx, src, axis=0):
    return tgt.clone().index_add_(axis, idx, src)
def _segment_sum(src, seg, n):
    out = torch.zeros((n,) + src.shape[1:], dtype=src.dtype)
    return out.index_add_(0, seg, src)
def _pad(x, pw):
    flat = tuple(v for pair in reversed(pw) for v in pair)
    return torch.nn.functional.pad(x, flat)

kernel_name = 'gated_mix__spectral_gfnet__swiglu_ffn__prompt'

def setup_inputs():

    _w = (lambda a: (lambda t: t.to(torch.bfloat16) if t.is_floating_point() else t)(torch.as_tensor(a)))
    def _A_setup():
        B, S, D = 1, 2048, 576
        Df = D // 2 + 1
        return {
            "x": _w((lambda *s: torch.randn(*s, dtype=torch.bfloat16) * 0.05)(B, S, D)),
            "w_r": _w((lambda *s: torch.randn(*s, dtype=torch.bfloat16) * 1.6)(S, Df) * 0.02),
            "w_i": _w((lambda *s: torch.randn(*s, dtype=torch.bfloat16) * 1.6)(S, Df) * 0.02),
        }
    _A_in = _A_setup()

    def _B_setup():
        B, S, D, F = 1, 2048, 576, 4608
        return {
            "x": _w((lambda *s: torch.randn(*s, dtype=torch.bfloat16) * 0.05)(B, S, D)),
            "w_gate": _w((lambda *s: torch.randn(*s, dtype=torch.bfloat16) * 0.05)(D, F)),
            "w_up": _w((lambda *s: torch.randn(*s, dtype=torch.bfloat16) * 0.05)(D, F)),
            "w_down": _w((lambda *s: torch.randn(*s, dtype=torch.bfloat16) * 0.05)(F, D)),
        }
    _B_in = _B_setup()

    return {
        "A_x": _A_in["x"],
        "A_w_r": _A_in["w_r"],
        "A_w_i": _A_in["w_i"],
        "B_x": _B_in["x"],
        "B_w_gate": _B_in["w_gate"],
        "B_w_up": _B_in["w_up"],
        "B_w_down": _B_in["w_down"],
        "gate_w": _w((lambda *s: torch.randn(*s, dtype=torch.bfloat16) * 0.05)(576) * 16.0),
    }

def reference(A_x, A_w_r, A_w_i, B_x, B_w_gate, B_w_up, B_w_down, gate_w):
    def _A_ref(x, w_r, w_i):
        B, S, D = x.shape
        X = (lambda x, axes=(-2,-1): torch.fft.rfft2(x.float(), dim=axes))(x, axes=(-2, -1))
        W = w_r + 1j * w_i
        X = X * W
        y = (lambda x, s, axes=(-2,-1): torch.fft.irfft2(x, s=s, dim=axes).to(torch.bfloat16))(X, (S, D), axes=(-2, -1))
        return y

    def _B_ref(x, w_gate, w_up, w_down):
        g = _silu(x @ w_gate)
        u = x @ w_up
        return (g * u) @ w_down

    x = A_x
    g = _sigmoid((x * gate_w).sum(-1, keepdims=True))
    a = _A_ref(x, A_w_r, A_w_i)
    b = _B_ref(x, B_w_gate, B_w_up, B_w_down)
    return g * a + (1 - g) * b
```

```cpp
#define L1MAP 1

#include <hip/hip_runtime.h>
#include <hip/hip_bf16.h>
typedef unsigned short u16;
typedef float f32x16 __attribute__((ext_vector_type(16)));
typedef float f32x4 __attribute__((ext_vector_type(4)));
typedef __bf16 bf16x8 __attribute__((ext_vector_type(8)));
typedef short s16x4 __attribute__((ext_vector_type(4)));
typedef unsigned u32x4 __attribute__((ext_vector_type(4)));
typedef unsigned u32x2 __attribute__((ext_vector_type(2)));
constexpr int KS=163;
constexpr size_t WS_H  = 0;
constexpr size_t WS_WD = 22u<<20;
constexpr size_t WS_XB = 28u<<20;
constexpr size_t WS_X1 = 38u<<20;
constexpr size_t WS_WT = 41u<<20;
constexpr size_t WS_G  = 44u<<20;
constexpr size_t WS_FD = 45u<<20;
constexpr size_t WS_TB = 46u<<20;
constexpr size_t WS_TM = 47u<<20;
__device__ __forceinline__ u16 f2bf(float f){ unsigned u=__builtin_bit_cast(unsigned,f); u=(u+0x7fffu+((u>>16)&1u))>>16; return (u16)u; }
__device__ __forceinline__ float bf2f(u16 v){ return __builtin_bit_cast(float,((unsigned)v)<<16); }

#if defined(__HIP_DEVICE_COMPILE__)
namespace ct {
constexpr double PI = 3.14159265358979323846264338327950288;
constexpr double ccos(double x){ double x2=x*x, t=1.0, s=1.0; for(int i=1;i<=16;++i){ t = -t*x2/((2*i-1)*(2*i)); s+=t; } return s; }
constexpr unsigned long long f2bf(double v){ float f=(float)v; unsigned u=__builtin_bit_cast(unsigned,f); u=(u+0x7fffu+((u>>16)&1u))>>16; return u; }
struct Base{ unsigned long long v[576]; constexpr Base():v(){ for(int i=0;i<576;++i){ int j=i>288?i-576:i; double c=ccos(2*PI*j/576.0); if(i==144||i==432) c=0.0; v[i]=f2bf(c);} } };
constexpr Base base{};
struct Blk{ unsigned long long v[9*1024]; };
constexpr Blk mk(int f,int part){
  Blk s{};
  for(int i=0;i<9*1024;++i){
    const int k=(i>>4)&63, c=f*128+part*64+((((i&15)>>2)^(((k>>1)&1)|(((k>>3)&1)<<1)))*16)+(i&3)*4, d=(i>>10)*64+k, kd=c>>1, a=(kd*d)%576, b=(a+d)%576;
    s.v[i] = (kd<=288 ? (base.v[a] | ((kd==0||kd==288) ? 0ull : base.v[(a+144)%576]<<16)) : 0ull) | (kd+1<=288 ? ((base.v[b]<<32) | (kd+1==288 ? 0ull : base.v[(b+144)%576]<<48)) : 0ull);
  }
  return s;
}
struct Tb{ unsigned long long v[36*128]; };
constexpr unsigned long long c64(int i){ return base.v[((i%64)*9)%576]; }
constexpr unsigned long long s64(int i){ return base.v[(((i%64)*9)+432)%576]; }
constexpr unsigned long long n64(int i){ return base.v[(((i%64)*9)+144)%576]; }
constexpr Tb mktb(){
  Tb t{};
  for(int i=0;i<36*128*4;++i){
    const int fr=i>>9, lane=(i>>3)&63, j=i&7, row=lane&31, h=lane>>5; unsigned long long v=0;
    if(fr<6){ const int a=16*(fr&1)+8*h+j; v = fr<2? c64(2*a*row) : fr<4? s64(2*a*row) : n64(2*a*row); }
    else if(fr<12){ const int c=16*(fr&1)+8*(j>>2)+4*h+(j&3); v = fr<8? c64(2*c*row) : fr<10? s64(2*c*row) : n64(2*c*row); }
    else { const int t8=(fr-12)&7, b=16*(t8&3)+8*(j>>2)+4*h+(j&3), e=32*(t8>>2)+row; v = fr<20? c64(b*e) : fr<28? s64(b*e) : n64(b*e); }
    t.v[i>>2] |= v<<(16*(i&3));
  }
  return t;
}
struct C2048{ unsigned long long v[2048]; constexpr C2048():v(){ for(int i=0;i<2048;++i){ int j=i>1024?i-2048:i; double c=ccos(2*PI*j/2048.0); if(i==512||i==1536) c=0.0; v[i]=__builtin_bit_cast(unsigned,(float)c);} } };
constexpr C2048 c2048{};
struct Tw{ unsigned long long v[3*8*64*2]; };
constexpr Tw mktw(){
  Tw t{};
  for(int i=0;i<3*8*64*2;++i){
    const int u=i&1, lane=(i>>1)&63, rho=4*((i>>8)&3)+2*((i>>7)&1)+u, blk=i>>10, row=(rho&3)+8*(rho>>2)+4*(lane>>5), c=lane&31;
    const int k=(blk==0? row*c : blk==1? (32+row)*c : row*(32+c))%2048;
    t.v[i] = c2048.v[k] | (c2048.v[(k+1536)%2048]<<32);
  }
  return t;
}
struct Gt{ unsigned long long v[19*9*128]; };
constexpr unsigned long long dbl(unsigned long long b){ return (b&0x7fffull)? b+0x80ull : b; }
struct Base2{ unsigned long long v[576]; constexpr Base2():v(){ for(int i=0;i<576;++i) v[i]=dbl(base.v[i]); } };
constexpr Base2 base2{};
constexpr Gt mkg(int nt){
  Gt g{};
  for(int i=0;i<19*9*128;++i){
    const int l=(i>>1)&63, fr=i>>7, ks=fr/9, d=nt*144+(fr-ks*9)*16+(l&15), kd=16*ks+4*(l>>4)+2*(i&1), a=(kd*d)%576, b=(a+d)%576;
    g.v[i] = (kd>288 ? 0ull : (kd==0||kd==288) ? base.v[a] : (base2.v[a] | base2.v[(a+144)%576]<<16)) | (kd+1>=288 ? 0ull : (base2.v[b]<<32 | base2.v[(b+144)%576]<<48));
  }
  return g;
}
}
namespace ct { constexpr Blk f0=mk(0,0), f1=mk(0,1), f2=mk(1,0), f3=mk(1,1), f4=mk(2,0), f5=mk(2,1), f6=mk(3,0), f7=mk(3,1), f8=mk(4,0), f9=mk(4,1);
               constexpr Gt g0=mkg(0), g1=mkg(1), g2=mkg(2), g3=mkg(3); }
__device__ __attribute__((aligned(256))) static constexpr ct::Blk FDT[10]={ct::f0,ct::f1,ct::f2,ct::f3,ct::f4,ct::f5,ct::f6,ct::f7,ct::f8,ct::f9};
__device__ __attribute__((aligned(256))) static constexpr ct::Gt GTT[4]={ct::g0,ct::g1,ct::g2,ct::g3};
struct F2T{ ct::Tb tb; ct::Tw tw; };
__device__ __attribute__((aligned(256))) static constexpr F2T F2TT={ct::mktb(),ct::mktw()};
#endif

typedef __attribute__((address_space(3))) s16x4* ldsp4;
typedef short v4i16 __attribute__((ext_vector_type(4)));
__device__ __forceinline__ u32x2 trd(const char* p){ return __builtin_bit_cast(u32x2,__builtin_amdgcn_ds_read_tr16_b64_v4i16((__attribute__((address_space(3))) v4i16*)p)); }
__device__ __forceinline__ void lds_dma(const char* g, char* l){ __builtin_amdgcn_global_load_lds((const __attribute__((address_space(1))) unsigned*)g,(__attribute__((address_space(3))) unsigned*)l,16,0,0); }
__device__ __forceinline__ void wd_block(const char* img, int ks, int w, unsigned lane, __amdgpu_buffer_rsrc_t rs){
  const int q=lane>>4, aa=(lane&15)>>2, p4=lane&3;
  const char* base=img+(4*q+aa)*1152+8*p4;
  u32x2 lo[5], hi[5];
  #pragma unroll
  for(int i=0;i<5;++i){ const int fr=min(w+8*i,35); lo[i]=trd(base+fr*32); hi[i]=trd(base+fr*32+16*1152); }
  #pragma unroll
  for(int i=0;i<5;++i){ const int fr=w+8*i; if(fr<36){ const int nt=fr/9, blk=fr-nt*9; const u32x4 o={lo[i][0],lo[i][1],hi[i][0],hi[i][1]};
      __builtin_amdgcn_raw_buffer_store_b128(o,rs,((nt*KS+ks)*9+blk)*1024+lane*16,0,17); } }
}
__device__ __forceinline__ void wt_block(const char* ta, int e, int w, unsigned lane, __amdgpu_buffer_rsrc_t rs){
  const int c4=lane&7; const char* p=ta+(4*c4)*578+(lane>>3)*2;
  u32x4 v[5];
  #pragma unroll
  for(int i=0;i<5;++i){ const int g=min(w+8*i,36);
    #pragma unroll
    for(int u=0;u<4;++u) v[i][u]=(unsigned)*(const u16*)(p+u*578+g*16) | ((unsigned)*(const u16*)(p+19456+u*578+g*16)<<16); }
  #pragma unroll
  for(int i=0;i<5;++i){ const int g=w+8*i; if(g<38){ const int kd=8*g+(lane>>3); u32x4 o;
      #pragma unroll
      for(int u=0;u<4;++u) o[u]= kd<289 ? min(v[i][u],0xfffffffeu) : 0u;
      __builtin_amdgcn_raw_buffer_store_b128(o,rs,(kd*2048+e*32+4*c4)*4,0,17); } }
}
#define SST(i)
__device__ __forceinline__ void side_jobs(int sid, int w, const u16* x, const u16* gw, const u16* wr, const u16* wi, const u16* wd, char* ws, const char* tptr, int trec){
  extern __shared__ __attribute__((aligned(16))) char smem[];
  typedef const __attribute__((address_space(1))) u32x4* g4; typedef const __attribute__((address_space(1))) unsigned* g1;
  unsigned lane; asm volatile("v_mbcnt_lo_u32_b32 %0, -1, 0\n v_mbcnt_hi_u32_b32 %0, -1, %0":"=v"(lane));
  const int tid=w*64+lane;
  const __amdgpu_buffer_rsrc_t rsW=__builtin_amdgcn_make_buffer_rsrc(ws+WS_WD,0,4*KS*9216,0x20000);
  const __amdgpu_buffer_rsrc_t rsT=__builtin_amdgcn_make_buffer_rsrc(ws+WS_WT,0,304*8192,0x20000);
  const __amdgpu_buffer_rsrc_t rsG=__builtin_amdgcn_make_buffer_rsrc(ws+WS_G,0,2*2048*4,0x20000);
  SST(2);
  const char* wdp=(const char*)wd+(size_t)sid*110592;
  for(int i=w;i<108;i+=8) lds_dma(wdp+i*1024+lane*16,smem+i*1024);
  char* ta=smem+110592;
  for(int i=w;i<38;i+=8){ const int arr=i>=19, ii=i-19*arr, off=ii*1024+lane*16; if(off+16<=18496) lds_dma((const char*)(arr?wi:wr)+(size_t)sid*18496+off,ta+arr*19456+ii*1024); }
  u32x4 w2[5];
  const int e2=min(48+sid,63);
  #pragma unroll
  for(int i=0;i<5;++i){ const int pc=min(tid+512*i,2311), arr=pc>=1156; w2[i]=*(g4)((const char*)(arr?wi:wr)+(size_t)e2*18496+(pc-1156*arr)*16); }
  unsigned xv[6][5], gv[5];
  #pragma unroll
  for(int k=0;k<5;++k){ const int dd=min((int)lane+64*k,287); gv[k]=((g1)gw)[dd]; }
  #pragma unroll
  for(int i=0;i<6;++i){ const int r=min(sid*43+w+8*i,2047);
    #pragma unroll
    for(int k=0;k<5;++k){ const int dd=min((int)lane+64*k,287); xv[i][k]=((g1)x)[r*288+dd]; } }
  unsigned tv=0; if(tid*128<trec) tv=*(g1)(tptr+tid*128);
  asm volatile("":"+v"(w2[0]),"+v"(w2[1]),"+v"(w2[2]),"+v"(w2[3]),"+v"(w2[4]),"+v"(tv));
  asm volatile("s_waitcnt vmcnt(0) lgkmcnt(0)\n s_barrier":::"memory");
  SST(3);
  wd_block(smem,sid*3,w,lane,rsW);
  wd_block(smem+36864,sid*3+1,w,lane,rsW);
  wd_block(smem+73728,sid*3+2,w,lane,rsW);
  wt_block(ta,sid,w,lane,rsT);
  if(sid<16){
    asm volatile("s_waitcnt lgkmcnt(0)\n s_barrier":::"memory");
    #pragma unroll
    for(int i=0;i<5;++i){ const int pc=tid+512*i, arr=pc>=1156; if(pc<2312) *(u32x4*)(smem+arr*19456+(pc-1156*arr)*16)=w2[i]; }
    asm volatile("s_waitcnt lgkmcnt(0)\n s_barrier":::"memory");
    wt_block(smem,48+sid,w,lane,rsT);
  }
  SST(4);
  #pragma unroll
  for(int i=0;i<6;++i){ float s=0.f;
    #pragma unroll
    for(int k=0;k<5;++k){ const float p0=bf2f(f2bf(bf2f(xv[i][k]&0xffff)*bf2f(gv[k]&0xffff))), p1=bf2f(f2bf(bf2f(xv[i][k]>>16)*bf2f(gv[k]>>16))); if(k<4 || lane<32) s+=p0+p1; }
    for(int o=32;o>0;o>>=1) s+=__shfl_xor(s,o);
    const int r=sid*43+w+8*i;
    if(lane==0 && w+8*i<43 && r<2048){ const float l=bf2f(f2bf(s)); const float gg=bf2f(f2bf(1.f/(1.f+__expf(-l))));
      __builtin_amdgcn_raw_buffer_store_b32(__builtin_bit_cast(unsigned,gg),rsG,r*4,0,17);
      __builtin_amdgcn_raw_buffer_store_b32(__builtin_bit_cast(unsigned,gg*(1.f/(576.f*2048.f))),rsG,8192+r*4,0,17); } }
  asm volatile("s_waitcnt lgkmcnt(0)\n s_barrier":::"memory");
}

__device__ __forceinline__ unsigned ringpos(unsigned p0, unsigned t){ unsigned p=p0+t*16384u; return p>=163840u ? p-163840u : p; }
__device__ __forceinline__ void wd_job(unsigned p0, int ks, int w, unsigned lane, char* WD){
  extern __shared__ __attribute__((aligned(16))) char smem[];
  const __amdgpu_buffer_rsrc_t rs=__builtin_amdgcn_make_buffer_rsrc(WD,0,4*KS*9216,0x20000);
  const int q=lane>>4, aa=(lane&15)>>2, p4=lane&3;
  const unsigned e0=ringpos(p0,0), e1=ringpos(p0,1), e2=ringpos(p0,2);
  u32x2 lo[5], hi[5];
  #pragma unroll
  for(int i=0;i<5;++i){ const unsigned fr=min(w+8*i,35); const unsigned o=(4*q+aa)*1152+8*p4+fr*32, o2=o+16*1152;
    const unsigned a=(o<16384u? e0 : e1)+(o&16383u), a2=(o2<16384u? e0 : o2<32768u? e1 : e2)+(o2&16383u);
    lo[i]=trd(smem+a); hi[i]=trd(smem+a2); }
  #pragma unroll
  for(int i=0;i<5;++i){ const int fr=w+8*i; if(fr<36){ const int nt=fr/9, blk=fr-nt*9; const u32x4 o={lo[i][0],lo[i][1],hi[i][0],hi[i][1]};
      __builtin_amdgcn_raw_buffer_store_b128(o,rs,((nt*KS+ks)*9+blk)*1024+lane*16,0,17); } }
}
__device__ __forceinline__ void wt_job(unsigned p0, int e, int w, unsigned lane, char* WT){
  extern __shared__ __attribute__((aligned(16))) char smem[];
  const __amdgpu_buffer_rsrc_t rs=__builtin_amdgcn_make_buffer_rsrc(WT,0,304*8192,0x20000);
  const unsigned e0=ringpos(p0,0), e1=ringpos(p0,1), e2=ringpos(p0,2);
  const int c4=lane&7;
  u32x4 v[5];
  #pragma unroll
  for(int i=0;i<5;++i){ const unsigned g=min(w+8*i,36);
    #pragma unroll
    for(int u=0;u<4;++u){ const unsigned o=(4*c4+u)*578+(lane>>3)*2+g*16; const unsigned a=(o<8192u? e0 : o<16384u? e1 : e2)+(o&8191u);
      v[i][u]=(unsigned)*(const u16*)(smem+a) | ((unsigned)*(const u16*)(smem+a+8192)<<16); } }
  #pragma unroll
  for(int i=0;i<5;++i){ const int g=w+8*i; if(g<38){ const int kd=8*g+(lane>>3); u32x4 o;
      #pragma unroll
      for(int u=0;u<4;++u) o[u]= kd<289 ? min(v[i][u],0xfffffffeu) : 0u;
      __builtin_amdgcn_raw_buffer_store_b128(o,rs,(kd*2048+e*32+4*c4)*4,0,17); } }
}
__device__ __forceinline__ float wave_sum63(float s){
  #define DPPADD(ctrl,rm) s+=__builtin_bit_cast(float,__builtin_amdgcn_update_dpp(0,__builtin_bit_cast(int,s),ctrl,rm,0xf,true))
  DPPADD(0x111,0xf); DPPADD(0x112,0xf); DPPADD(0x114,0xf); DPPADD(0x118,0xf); DPPADD(0x142,0xa); DPPADD(0x143,0xc);
  #undef DPPADD
  return s;
}
__device__ __forceinline__ void gate_job(int jb, int w, unsigned lane, const u16* x, const u16* gw, char* G){
  typedef const __attribute__((address_space(1))) unsigned* g1;
  const __amdgpu_buffer_rsrc_t rs=__builtin_amdgcn_make_buffer_rsrc(G,0,2*2048*4,0x20000);
  unsigned xv[6][5], gv[5];
  #pragma unroll
  for(int k=0;k<5;++k){ const int dd=min((int)lane+64*k,287); gv[k]=((g1)gw)[dd]; }
  #pragma unroll
  for(int i=0;i<6;++i){ const int r=min(jb*43+w+8*i,2047);
    #pragma unroll
    for(int k=0;k<5;++k){ const int dd=min((int)lane+64*k,287); xv[i][k]=((g1)x)[r*288+dd]; } }
  float mine=0.f;
  #pragma unroll
  for(int i=0;i<6;++i){ float s=0.f;
    #pragma unroll
    for(int k=0;k<5;++k){ const float p0=bf2f(f2bf(bf2f(xv[i][k]&0xffff)*bf2f(gv[k]&0xffff))), p1=bf2f(f2bf(bf2f(xv[i][k]>>16)*bf2f(gv[k]>>16))); if(k<4 || lane<32) s+=p0+p1; }
    s=wave_sum63(s);
    const float t=__builtin_bit_cast(float,__builtin_amdgcn_readlane(__builtin_bit_cast(int,s),63));
    if(lane==i) mine=t; }
  const int r=jb*43+w+8*(int)lane;
  if(lane<6 && w+8*(int)lane<43 && r<2048){ const float l=bf2f(f2bf(mine)); const float gg=bf2f(f2bf(1.f/(1.f+__expf(-l))));
    __builtin_amdgcn_raw_buffer_store_b32(__builtin_bit_cast(unsigned,gg),rs,r*4,0,17);
    __builtin_amdgcn_raw_buffer_store_b32(__builtin_bit_cast(unsigned,gg*(1.f/(576.f*2048.f))),rs,8192+r*4,0,17); }
}

struct L1P{ int mt,f,type,j0,nj; };
__device__ __forceinline__ L1P l1_params(int b){
  const int xcd=b&7, idx=b>>3; L1P p; p.mt=idx&15; const int half=idx>>4;
  p.f=(xcd-p.mt)&7; p.type=(half==1 && p.f<5)?1:0; p.j0=9*xcd+(half?5:0); p.nj=half? (p.type?5:4) : 5; return p;
}
extern "C" __global__ __launch_bounds__(512,2) void l1_kernel(const u16* x, const u16* wg, const u16* wu, const u16* wd, const u16* wr, const u16* wi, char* ws, const u16* gw){

  const int b=blockIdx.x;
  int w=__builtin_amdgcn_readfirstlane(threadIdx.x>>6);
  asm volatile("":"+s"(w));
  const L1P p=l1_params(b);
  int mt=p.mt, f=p.f, type=p.type, j0=p.j0, nj=p.nj;
  const char* fda=nullptr; const char* fdb=nullptr;
  const char* sg; const char* su; int srec, sstr, srec2;
  if(b<144){ sg=(const char*)wd+(size_t)b*36864; su=sg+8192; srec=36864; srec2=36864-8192; sstr=16384; }
  else { const int e=min(b-144,63); sg=(const char*)wr+(size_t)e*18496; su=(const char*)wi+(size_t)e*18496; srec= b<208? 18496:0; srec2=srec; sstr=8192; }
  const int sid=mt*3+(f-5);
  const char* tptr=(const char*)x; int trec=0;
#if defined(__HIP_DEVICE_COMPILE__)
  fda=(const char*)FDT+(size_t)min(f,4)*147456; fdb=fda+73728;
  if(nj==4){
    if(sid<22){ tptr=(const char*)GTT+sid*32768; trec=min(32768,700416-sid*32768); }
    else if(sid<24){ tptr=(const char*)&F2TT+(sid-22)*32768; trec=min(32768,61440-(sid-22)*32768); }
    else if(sid<47){ tptr=(const char*)FDT+(sid-24)*32768; trec=min(32768,737280-(sid-24)*32768); } }
#endif
  int bb=b, p0;
  trec=__builtin_amdgcn_readfirstlane(trec);
  type=__builtin_amdgcn_readfirstlane(type); nj=__builtin_amdgcn_readfirstlane(nj); j0=__builtin_amdgcn_readfirstlane(j0); f=__builtin_amdgcn_readfirstlane(f); mt=__builtin_amdgcn_readfirstlane(mt);
  srec=__builtin_amdgcn_readfirstlane(srec); srec2=__builtin_amdgcn_readfirstlane(srec2); sstr=__builtin_amdgcn_readfirstlane(sstr);
  asm volatile("":"+s"(type),"+s"(nj),"+s"(j0),"+s"(f),"+s"(mt),"+s"(fda),"+s"(fdb),"+s"(bb),"+s"(sg),"+s"(su),"+s"(srec),"+s"(sstr),"+s"(srec2),"+s"(tptr),"+s"(trec));
  asm volatile(
    "s_cmp_lt_u32 %13, 8\n"
    "s_cbranch_scc0 L1NOCPF%=\n"
    "v_mbcnt_lo_u32_b32 v208, -1, 0\n"
    "v_mbcnt_hi_u32_b32 v208, -1, v208\n"
    "s_lshl_b32 s67, %8, 6\n"
    "v_add_u32 v208, s67, v208\n"
    "v_lshlrev_b32 v208, 7, v208\n"
    "s_getpc_b64 s[88:89]\n"
    "L1PC0:\n"
    "s_sub_u32 s88, s88, L1PC0-l1_kernel\n"
    "s_subb_u32 s89, s89, 0\n"
    "s_and_b32 s89, s89, 0xffff\n"
    "s_mov_b32 s90, L2_CODE_END-l1_kernel+512\n"
    "s_mov_b32 s91, 0x20000\n"
    "buffer_load_dword v135, v208, s[88:91], 0 offen\n"
    "L1NOCPF%=:\n"
    "s_mov_b32 s64, %8\n"
    "s_and_b32 s65, s64, 3\n"
    "s_lshr_b32 s66, s64, 2\n"
    "s_mov_b32 s76, %9\n"
    "s_mov_b32 s77, %10\n"
    "s_add_u32 s98, s77, 1\n"
    "s_mul_i32 s67, %7, 147456\n"
    "s_mov_b64 s[36:37], %1\n"
    "s_add_u32 s36, s36, s67\n"
    "s_addc_u32 s37, s37, 0\n"
    "s_and_b32 s37, s37, 0xffff\n"
    "s_mov_b32 s38, 147456\n"
    "s_mov_b32 s39, 0x20000\n"
    "s_mov_b64 s[40:41], %2\n"
    "s_and_b32 s41, s41, 0xffff\n"
    "s_mov_b32 s42, 5308416\n"
    "s_mov_b32 s43, 0x20000\n"
    "s_mov_b64 s[44:45], %3\n"
    "s_and_b32 s45, s45, 0xffff\n"
    "s_mov_b32 s46, 5308416\n"
    "s_mov_b32 s47, 0x20000\n"
    "s_mov_b64 s[56:57], %5\n"
    "s_and_b32 s57, s57, 0xffff\n"
    "s_mov_b32 s58, 21364736\n"
    "s_mov_b32 s59, 0x20000\n"
    "s_mov_b32 s67, 39845888\n"
    "s_mov_b64 s[60:61], %5\n"
    "s_add_u32 s60, s60, s67\n"
    "s_addc_u32 s61, s61, 0\n"
    "s_and_b32 s61, s61, 0xffff\n"
    "s_mov_b32 s62, 2621440\n"
    "s_mov_b32 s63, 0x20000\n"
    "s_lshl_b32 s73, s64, 10\n"
    "s_lshl_b32 s74, s64, 11\n"
    "s_mov_b32 s72, 0\n"
    "v_mbcnt_lo_u32_b32 v254, -1, 0\n"
    "v_mbcnt_hi_u32_b32 v254, -1, v254\n"
    "v_lshrrev_b32 v208, 3, v254\n"
    "v_bfe_u32 v209, v254, 4, 1\n"
    "s_and_b32 s67, s64, 1\n"
    "s_lshl_b32 s67, s67, 1\n"
    "v_or_b32 v209, s67, v209\n"
    "v_bfe_u32 v210, v254, 1, 2\n"
    "v_xor_b32 v210, v210, v209\n"
    "v_lshlrev_b32 v210, 5, v210\n"
    "v_and_b32 v211, 1, v254\n"
    "v_lshlrev_b32 v211, 4, v211\n"
    "v_add_u32 v210, v210, v211\n"
    "s_lshl_b32 s67, s64, 3\n"
    "v_add_u32 v208, s67, v208\n"
    "s_mov_b32 s67, 9216\n"
    "v_mul_lo_u32 v208, v208, s67\n"
    "v_add_u32 v242, v208, v210\n"
    "v_lshrrev_b32 v208, 3, v254\n"
    "v_lshrrev_b32 v209, 4, v254\n"
    "v_and_b32 v210, 7, v254\n"
    "v_xor_b32 v210, v210, v209\n"
    "s_lshl_b32 s67, s64, 4\n"
    "v_add_u32 v208, s67, v208\n"
    "s_movk_i32 s67, 1152\n"
    "v_mul_lo_u32 v208, v208, s67\n"
    "v_lshlrev_b32 v211, 4, v210\n"
    "v_add_u32 v244, v208, v211\n"
    "v_xor_b32 v210, 4, v210\n"
    "v_lshlrev_b32 v211, 4, v210\n"
    "v_add_u32 v245, v208, v211\n"
    "v_add_u32 v245, 9216, v245\n"
    "s_lshl_b32 s69, %11, 7\n"
    "s_add_u32 s78, s69, 256\n"
    "s_sub_u32 s76, s76, 1\n"
    "s_nop 4\n"
    "s_add_u32 m0, s74, 0\n"
    "s_nop 0\n"
    "buffer_load_dwordx4 v244, s[36:39], s72 offen lds\n"
    "s_add_u32 m0, m0, 1024\n"
    "s_nop 0\n"
    "buffer_load_dwordx4 v245, s[36:39], s72 offen lds\n"
    "s_add_u32 s72, s72, 128\n"
    "s_add_u32 m0, s73, 16384\n"
    "s_nop 0\n"
    "buffer_load_dwordx4 v242, s[40:43], s69 offen lds\n"
    "s_add_u32 m0, m0, 8192\n"
    "s_nop 0\n"
    "buffer_load_dwordx4 v242, s[44:47], s69 offen lds\n"
    "s_barrier\n"
    "s_add_u32 s70, s69, 128\n"
    "s_add_u32 m0, s73, 32768\n"
    "s_nop 0\n"
    "buffer_load_dwordx4 v242, s[40:43], s70 offen lds\n"
    "s_add_u32 m0, m0, 8192\n"
    "s_nop 0\n"
    "buffer_load_dwordx4 v242, s[44:47], s70 offen lds\n"
    "s_add_u32 s69, s69, 589824\n"
    "s_add_u32 m0, s74, 49152\n"
    "s_nop 0\n"
    "buffer_load_dwordx4 v244, s[36:39], s72 offen lds\n"
    "s_add_u32 m0, m0, 1024\n"
    "s_nop 0\n"
    "buffer_load_dwordx4 v245, s[36:39], s72 offen lds\n"
    "s_add_u32 s72, s72, 128\n"
    "s_barrier\n"
    "s_add_u32 m0, s73, 65536\n"
    "s_nop 0\n"
    "buffer_load_dwordx4 v242, s[40:43], s69 offen lds\n"
    "s_add_u32 m0, m0, 8192\n"
    "s_nop 0\n"
    "buffer_load_dwordx4 v242, s[44:47], s69 offen lds\n"
    "s_add_u32 s70, s69, 128\n"
    "s_add_u32 m0, s73, 81920\n"
    "s_nop 0\n"
    "buffer_load_dwordx4 v242, s[40:43], s70 offen lds\n"
    "s_add_u32 m0, m0, 8192\n"
    "s_nop 0\n"
    "buffer_load_dwordx4 v242, s[44:47], s70 offen lds\n"
    "s_add_u32 s69, s69, 589824\n"
    "v_lshrrev_b32 v208, 4, v254\n"
    "v_bfe_u32 v209, v254, 2, 2\n"
    "v_lshlrev_b32 v210, 3, v208\n"
    "v_add_u32 v210, v210, v209\n"
    "v_lshlrev_b32 v210, 7, v210\n"
    "v_and_b32 v211, 3, v254\n"
    "v_lshlrev_b32 v211, 3, v211\n"
    "v_add_u32 v210, v210, v211\n"
    "v_bfe_u32 v209, v254, 3, 1\n"
    "v_and_b32 v208, 1, v208\n"
    "v_lshlrev_b32 v208, 1, v208\n"
    "v_or_b32 v209, v209, v208\n"
    "s_lshl_b32 s67, s66, 1\n"
    "s_add_u32 s68, s67, 0\n"
    "v_xor_b32 v211, s68, v209\n"
    "v_lshlrev_b32 v211, 5, v211\n"
    "v_add_u32 v240, v210, v211\n"
    "s_add_u32 s68, s67, 1\n"
    "v_xor_b32 v211, s68, v209\n"
    "v_lshlrev_b32 v211, 5, v211\n"
    "v_add_u32 v241, v210, v211\n"
    "v_and_b32 v208, 15, v254\n"
    "v_lshrrev_b32 v209, 1, v208\n"
    "v_lshrrev_b32 v210, 4, v254\n"
    "v_xor_b32 v209, v209, v210\n"
    "v_lshlrev_b32 v209, 4, v209\n"
    "s_lshl_b32 s67, s65, 5\n"
    "v_add_u32 v208, s67, v208\n"
    "v_lshlrev_b32 v208, 7, v208\n"
    "v_add_u32 v246, v208, v209\n"
    "v_xor_b32 v247, 64, v246\n"
    "v_lshlrev_b32 v252, 4, v254\n"
    "s_lshl_b32 s67, s66, 13\n"
    "s_lshl_b32 s68, s65, 11\n"
    "s_add_u32 s67, s67, s68\n"
    "v_add_u32 v252, s67, v252\n"
    "s_mul_i32 s75, %7, 163\n"
    "s_lshl_b32 s67, %11, 1\n"
    "s_add_u32 s75, s75, s67\n"
    "s_lshl_b32 s75, s75, 13\n"
    "v_and_b32 v208, 15, v254\n"
    "v_lshlrev_b32 v208, 12, v208\n"
    "v_lshrrev_b32 v209, 4, v254\n"
    "v_lshlrev_b32 v209, 4, v209\n"
    "v_add_u32 v253, v208, v209\n"
    "s_lshl_b32 s67, s66, 17\n"
    "s_lshl_b32 s68, s65, 6\n"
    "s_add_u32 s67, s67, s68\n"
    "v_add_u32 v253, s67, v253\n"
    "s_lshl_b32 s79, %12, 19\n"
    "s_lshl_b32 s67, %7, 8\n"
    "s_add_u32 s79, s79, s67\n"
    "s_mov_b64 s[88:89], %19\n"
    "s_and_b32 s89, s89, 0xffff\n"
    "s_mov_b32 s90, %20\n"
    "s_mov_b32 s91, 0x20000\n"
    "v_mbcnt_lo_u32_b32 v208, -1, 0\n"
    "v_mbcnt_hi_u32_b32 v208, -1, v208\n"
    "s_lshl_b32 s67, s64, 6\n"
    "v_add_u32 v208, s67, v208\n"
    "v_lshlrev_b32 v208, 7, v208\n"
    "buffer_load_dword v134, v208, s[88:91], 0 offen\n"
    "s_lshr_b32 s67, %13, 3\n"
    "s_mul_i32 s88, s67, 18\n"
    "s_and_b32 s67, %13, 7\n"
    "s_mul_i32 s89, s67, 1152\n"
    "s_lshr_b32 s67, %13, 7\n"
    "s_lshl_b32 s90, s67, 6\n"
    "v_mbcnt_lo_u32_b32 v208, -1, 0\n"
    "v_mbcnt_hi_u32_b32 v208, -1, v208\n"
    "s_lshl_b32 s67, s64, 6\n"
    "v_add_u32 v208, s67, v208\n"
    "v_mov_b32 v212, 0x40000000\n"
    "s_mov_b32 s67, 116509\n"
    "v_mul_u32_u24 v209, s67, v208\n"
    "v_lshrrev_b32 v209, 20, v209\n"
    "v_mul_u32_u24 v210, 9, v209\n"
    "v_sub_u32 v210, v208, v210\n"
    "v_lshlrev_b32 v210, 7, v210\n"
    "v_cmp_gt_u32 vcc, 18, v209\n"
    "v_add_u32 v211, s88, v209\n"
    "s_movk_i32 s67, 9216\n"
    "v_mul_lo_u32 v211, v211, s67\n"
    "v_add_u32 v210, v210, v211\n"
    "v_add_u32 v210, s89, v210\n"
    "v_cndmask_b32 v132, v212, v210, vcc\n"
    "v_lshrrev_b32 v209, 2, v208\n"
    "v_add_u32 v209, s90, v209\n"
    "s_movk_i32 s67, 1152\n"
    "v_mul_lo_u32 v209, v209, s67\n"
    "v_and_b32 v210, 3, v208\n"
    "v_lshlrev_b32 v210, 7, v210\n"
    "v_add_u32 v209, v209, v210\n"
    "v_add_u32 v209, 640, v209\n"
    "v_cmp_gt_u32 vcc, 256, v208\n"
    "s_nop 1\n"
    "v_cndmask_b32 v133, v212, v209, vcc\n"
    "s_waitcnt vmcnt(9)\n"
    "s_barrier\n"
    "v_add_u32 v254, 0, v246\n"
    "v_add_u32 v255, 0, v247\n"
    "v_add_u32 v248, 16384, v240\n"
    "v_add_u32 v249, 16384, v241\n"
    "ds_read_b128 v[0:3], v254 offset:0\n"
    "ds_read_b128 v[4:7], v254 offset:2048\n"
    "ds_read_b128 v[8:11], v255 offset:0\n"
    "ds_read_b128 v[12:15], v255 offset:2048\n"
    "ds_read_b64_tr_b16 v[208:209], v248 offset:0\n"
    "ds_read_b64_tr_b16 v[210:211], v248 offset:512\n"
    "ds_read_b64_tr_b16 v[212:213], v249 offset:0\n"
    "ds_read_b64_tr_b16 v[214:215], v249 offset:512\n"
    "ds_read_b64_tr_b16 v[216:217], v248 offset:8192\n"
    "ds_read_b64_tr_b16 v[218:219], v248 offset:8704\n"
    "ds_read_b64_tr_b16 v[220:221], v249 offset:8192\n"
    "ds_read_b64_tr_b16 v[222:223], v249 offset:8704\n"
    "s_waitcnt lgkmcnt(6)\n"
    "v_mfma_f32_16x16x32_bf16 v[144:147], v[208:211], v[0:3], 0\n"
    "ds_read_b64_tr_b16 v[224:225], v248 offset:4096\n"
    "v_mfma_f32_16x16x32_bf16 v[148:151], v[208:211], v[4:7], 0\n"
    "ds_read_b64_tr_b16 v[226:227], v248 offset:4608\n"
    "s_add_u32 m0, s74, 98304\n"
    "s_nop 0\n"
    "buffer_load_dwordx4 v244, s[36:39], s72 offen lds\n"
    "s_waitcnt lgkmcnt(6)\n"
    "v_mfma_f32_16x16x32_bf16 v[152:155], v[212:215], v[0:3], 0\n"
    "ds_read_b64_tr_b16 v[228:229], v249 offset:4096\n"
    "v_mfma_f32_16x16x32_bf16 v[156:159], v[212:215], v[4:7], 0\n"
    "ds_read_b64_tr_b16 v[230:231], v249 offset:4608\n"
    "s_waitcnt lgkmcnt(6)\n"
    "v_mfma_f32_16x16x32_bf16 v[160:163], v[216:219], v[0:3], 0\n"
    "ds_read_b64_tr_b16 v[232:233], v248 offset:12288\n"
    "s_add_u32 m0, m0, 1024\n"
    "s_nop 0\n"
    "buffer_load_dwordx4 v245, s[36:39], s72 offen lds\n"
    "s_add_u32 s72, s72, 128\n"
    "v_mfma_f32_16x16x32_bf16 v[164:167], v[216:219], v[4:7], 0\n"
    "ds_read_b64_tr_b16 v[234:235], v248 offset:12800\n"
    "s_waitcnt lgkmcnt(6)\n"
    "v_mfma_f32_16x16x32_bf16 v[168:171], v[220:223], v[0:3], 0\n"
    "ds_read_b64_tr_b16 v[236:237], v249 offset:12288\n"
    "v_mfma_f32_16x16x32_bf16 v[172:175], v[220:223], v[4:7], 0\n"
    "ds_read_b64_tr_b16 v[238:239], v249 offset:12800\n"
    "s_add_u32 m0, s73, 114688\n"
    "s_nop 0\n"
    "buffer_load_dwordx4 v242, s[40:43], s69 offen lds\n"
    "s_waitcnt vmcnt(10)\n"
    "s_barrier\n"
    "v_add_u32 v250, 32768, v240\n"
    "v_add_u32 v251, 32768, v241\n"
    "s_waitcnt lgkmcnt(6)\n"
    "v_mfma_f32_16x16x32_bf16 v[144:147], v[224:227], v[8:11], v[144:147]\n"
    "ds_read_b64_tr_b16 v[208:209], v250 offset:0\n"
    "v_mfma_f32_16x16x32_bf16 v[148:151], v[224:227], v[12:15], v[148:151]\n"
    "ds_read_b64_tr_b16 v[210:211], v250 offset:512\n"
    "s_add_u32 m0, m0, 8192\n"
    "s_nop 0\n"
    "buffer_load_dwordx4 v242, s[44:47], s69 offen lds\n"
    "s_waitcnt lgkmcnt(6)\n"
    "v_mfma_f32_16x16x32_bf16 v[152:155], v[228:231], v[8:11], v[152:155]\n"
    "ds_read_b64_tr_b16 v[212:213], v251 offset:0\n"
    "v_mfma_f32_16x16x32_bf16 v[156:159], v[228:231], v[12:15], v[156:159]\n"
    "ds_read_b64_tr_b16 v[214:215], v251 offset:512\n"
    "s_waitcnt lgkmcnt(6)\n"
    "v_mfma_f32_16x16x32_bf16 v[160:163], v[232:235], v[8:11], v[160:163]\n"
    "ds_read_b64_tr_b16 v[216:217], v250 offset:8192\n"
    "s_add_u32 s70, s69, 128\n"
    "s_add_u32 m0, s73, 131072\n"
    "s_nop 0\n"
    "buffer_load_dwordx4 v242, s[40:43], s70 offen lds\n"
    "v_mfma_f32_16x16x32_bf16 v[164:167], v[232:235], v[12:15], v[164:167]\n"
    "ds_read_b64_tr_b16 v[218:219], v250 offset:8704\n"
    "s_waitcnt lgkmcnt(6)\n"
    "v_mfma_f32_16x16x32_bf16 v[168:171], v[236:239], v[8:11], v[168:171]\n"
    "ds_read_b64_tr_b16 v[220:221], v251 offset:8192\n"
    "v_mfma_f32_16x16x32_bf16 v[172:175], v[236:239], v[12:15], v[172:175]\n"
    "ds_read_b64_tr_b16 v[222:223], v251 offset:8704\n"
    "s_add_u32 m0, m0, 8192\n"
    "s_nop 0\n"
    "buffer_load_dwordx4 v242, s[44:47], s70 offen lds\n"
    "s_add_u32 s69, s69, 589824\n"
    "s_waitcnt lgkmcnt(6)\n"
    "v_mfma_f32_16x16x32_bf16 v[176:179], v[208:211], v[0:3], 0\n"
    "ds_read_b64_tr_b16 v[224:225], v250 offset:4096\n"
    "v_mfma_f32_16x16x32_bf16 v[180:183], v[208:211], v[4:7], 0\n"
    "ds_read_b64_tr_b16 v[226:227], v250 offset:4608\n"
    "s_add_u32 m0, s74, 147456\n"
    "s_nop 0\n"
    "buffer_load_dwordx4 v244, s[36:39], s72 offen lds\n"
    "s_waitcnt lgkmcnt(6)\n"
    "v_mfma_f32_16x16x32_bf16 v[184:187], v[212:215], v[0:3], 0\n"
    "ds_read_b64_tr_b16 v[228:229], v251 offset:4096\n"
    "v_mfma_f32_16x16x32_bf16 v[188:191], v[212:215], v[4:7], 0\n"
    "ds_read_b64_tr_b16 v[230:231], v251 offset:4608\n"
    "s_waitcnt lgkmcnt(6)\n"
    "v_mfma_f32_16x16x32_bf16 v[192:195], v[216:219], v[0:3], 0\n"
    "ds_read_b64_tr_b16 v[232:233], v250 offset:12288\n"
    "s_add_u32 m0, m0, 1024\n"
    "s_nop 0\n"
    "buffer_load_dwordx4 v245, s[36:39], s72 offen lds\n"
    "s_add_u32 s72, s72, 128\n"
    "v_mfma_f32_16x16x32_bf16 v[196:199], v[216:219], v[4:7], 0\n"
    "ds_read_b64_tr_b16 v[234:235], v250 offset:12800\n"
    "s_waitcnt lgkmcnt(6)\n"
    "v_mfma_f32_16x16x32_bf16 v[200:203], v[220:223], v[0:3], 0\n"
    "ds_read_b64_tr_b16 v[236:237], v251 offset:12288\n"
    "v_mfma_f32_16x16x32_bf16 v[204:207], v[220:223], v[4:7], 0\n"
    "ds_read_b64_tr_b16 v[238:239], v251 offset:12800\n"
    "s_waitcnt vmcnt(9)\n"
    "s_barrier\n"
    "v_add_u32 v254, 49152, v246\n"
    "v_add_u32 v255, 49152, v247\n"
    "v_add_u32 v248, 65536, v240\n"
    "v_add_u32 v249, 65536, v241\n"
    "s_waitcnt lgkmcnt(6)\n"
    "v_mfma_f32_16x16x32_bf16 v[176:179], v[224:227], v[8:11], v[176:179]\n"
    "ds_read_b128 v[16:19], v254 offset:0\n"
    "ds_read_b64_tr_b16 v[208:209], v248 offset:0\n"
    "v_mfma_f32_16x16x32_bf16 v[180:183], v[224:227], v[12:15], v[180:183]\n"
    "ds_read_b128 v[20:23], v254 offset:2048\n"
    "ds_read_b64_tr_b16 v[210:211], v248 offset:512\n"
    "s_add_u32 m0, s73, 0\n"
    "s_nop 0\n"
    "buffer_load_dwordx4 v242, s[40:43], s69 offen lds\n"
    "s_waitcnt lgkmcnt(8)\n"
    "v_mfma_f32_16x16x32_bf16 v[184:187], v[228:231], v[8:11], v[184:187]\n"
    "ds_read_b128 v[24:27], v255 offset:0\n"
    "ds_read_b64_tr_b16 v[212:213], v249 offset:0\n"
    "v_mfma_f32_16x16x32_bf16 v[188:191], v[228:231], v[12:15], v[188:191]\n"
    "ds_read_b128 v[28:31], v255 offset:2048\n"
    "ds_read_b64_tr_b16 v[214:215], v249 offset:512\n"
    "s_waitcnt lgkmcnt(10)\n"
    "v_mfma_f32_16x16x32_bf16 v[192:195], v[232:235], v[8:11], v[192:195]\n"
    "ds_read_b64_tr_b16 v[216:217], v248 offset:8192\n"
    "s_add_u32 m0, m0, 8192\n"
    "s_nop 0\n"
    "buffer_load_dwordx4 v242, s[44:47], s69 offen lds\n"
    "v_mfma_f32_16x16x32_bf16 v[196:199], v[232:235], v[12:15], v[196:199]\n"
    "ds_read_b64_tr_b16 v[218:219], v248 offset:8704\n"
    "s_waitcnt lgkmcnt(10)\n"
    "v_mfma_f32_16x16x32_bf16 v[200:203], v[236:239], v[8:11], v[200:203]\n"
    "ds_read_b64_tr_b16 v[220:221], v249 offset:8192\n"
    "v_mfma_f32_16x16x32_bf16 v[204:207], v[236:239], v[12:15], v[204:207]\n"
    "ds_read_b64_tr_b16 v[222:223], v249 offset:8704\n"
    "s_add_u32 s70, s69, 128\n"
    "s_add_u32 m0, s73, 16384\n"
    "s_nop 0\n"
    "buffer_load_dwordx4 v242, s[40:43], s70 offen lds\n"
    "s_waitcnt lgkmcnt(8)\n"
    "v_mfma_f32_16x16x32_bf16 v[144:147], v[208:211], v[16:19], v[144:147]\n"
    "ds_read_b64_tr_b16 v[224:225], v248 offset:4096\n"
    "v_mfma_f32_16x16x32_bf16 v[148:151], v[208:211], v[20:23], v[148:151]\n"
    "ds_read_b64_tr_b16 v[226:227], v248 offset:4608\n"
    "s_add_u32 m0, m0, 8192\n"
    "s_nop 0\n"
    "buffer_load_dwordx4 v242, s[44:47], s70 offen lds\n"
    "s_add_u32 s69, s69, 589824\n"
    "s_waitcnt lgkmcnt(6)\n"
    "v_mfma_f32_16x16x32_bf16 v[152:155], v[212:215], v[16:19], v[152:155]\n"
    "ds_read_b64_tr_b16 v[228:229], v249 offset:4096\n"
    "v_mfma_f32_16x16x32_bf16 v[156:159], v[212:215], v[20:23], v[156:159]\n"
    "ds_read_b64_tr_b16 v[230:231], v249 offset:4608\n"
    "s_waitcnt lgkmcnt(6)\n"
    "v_mfma_f32_16x16x32_bf16 v[160:163], v[216:219], v[16:19], v[160:163]\n"
    "ds_read_b64_tr_b16 v[232:233], v248 offset:12288\n"
    "v_mfma_f32_16x16x32_bf16 v[164:167], v[216:219], v[20:23], v[164:167]\n"
    "ds_read_b64_tr_b16 v[234:235], v248 offset:12800\n"
    "s_waitcnt lgkmcnt(6)\n"
    "v_mfma_f32_16x16x32_bf16 v[168:171], v[220:223], v[16:19], v[168:171]\n"
    "ds_read_b64_tr_b16 v[236:237], v249 offset:12288\n"
    "v_mfma_f32_16x16x32_bf16 v[172:175], v[220:223], v[20:23], v[172:175]\n"
    "ds_read_b64_tr_b16 v[238:239], v249 offset:12800\n"
    "v_add_u32 v250, 81920, v240\n"
    "v_add_u32 v251, 81920, v241\n"
    "s_waitcnt lgkmcnt(6)\n"
    "v_mfma_f32_16x16x32_bf16 v[144:147], v[224:227], v[24:27], v[144:147]\n"
    "ds_read_b64_tr_b16 v[208:209], v250 offset:0\n"
    "v_mfma_f32_16x16x32_bf16 v[148:151], v[224:227], v[28:31], v[148:151]\n"
    "ds_read_b64_tr_b16 v[210:211], v250 offset:512\n"
    "s_waitcnt lgkmcnt(6)\n"
    "v_mfma_f32_16x16x32_bf16 v[152:155], v[228:231], v[24:27], v[152:155]\n"
    "ds_read_b64_tr_b16 v[212:213], v251 offset:0\n"
    "v_mfma_f32_16x16x32_bf16 v[156:159], v[228:231], v[28:31], v[156:159]\n"
    "ds_read_b64_tr_b16 v[214:215], v251 offset:512\n"
    "s_waitcnt lgkmcnt(6)\n"
    "v_mfma_f32_16x16x32_bf16 v[160:163], v[232:235], v[24:27], v[160:163]\n"
    "ds_read_b64_tr_b16 v[216:217], v250 offset:8192\n"
    "v_mfma_f32_16x16x32_bf16 v[164:167], v[232:235], v[28:31], v[164:167]\n"
    "ds_read_b64_tr_b16 v[218:219], v250 offset:8704\n"
    "s_waitcnt lgkmcnt(6)\n"
    "v_mfma_f32_16x16x32_bf16 v[168:171], v[236:239], v[24:27], v[168:171]\n"
    "ds_read_b64_tr_b16 v[220:221], v251 offset:8192\n"
    "v_mfma_f32_16x16x32_bf16 v[172:175], v[236:239], v[28:31], v[172:175]\n"
    "ds_read_b64_tr_b16 v[222:223], v251 offset:8704\n"
    "s_waitcnt lgkmcnt(6)\n"
    "v_mfma_f32_16x16x32_bf16 v[176:179], v[208:211], v[16:19], v[176:179]\n"
    "ds_read_b64_tr_b16 v[224:225], v250 offset:4096\n"
    "v_mfma_f32_16x16x32_bf16 v[180:183], v[208:211], v[20:23], v[180:183]\n"
    "ds_read_b64_tr_b16 v[226:227], v250 offset:4608\n"
    "s_waitcnt lgkmcnt(6)\n"
    "v_mfma_f32_16x16x32_bf16 v[184:187], v[212:215], v[16:19], v[184:187]\n"
    "ds_read_b64_tr_b16 v[228:229], v251 offset:4096\n"
    "v_mfma_f32_16x16x32_bf16 v[188:191], v[212:215], v[20:23], v[188:191]\n"
    "ds_read_b64_tr_b16 v[230:231], v251 offset:4608\n"
    "s_waitcnt lgkmcnt(6)\n"
    "v_mfma_f32_16x16x32_bf16 v[192:195], v[216:219], v[16:19], v[192:195]\n"
    "ds_read_b64_tr_b16 v[232:233], v250 offset:12288\n"
    "v_mfma_f32_16x16x32_bf16 v[196:199], v[216:219], v[20:23], v[196:199]\n"
    "ds_read_b64_tr_b16 v[234:235], v250 offset:12800\n"
    "s_waitcnt lgkmcnt(6)\n"
    "v_mfma_f32_16x16x32_bf16 v[200:203], v[220:223], v[16:19], v[200:203]\n"
    "ds_read_b64_tr_b16 v[236:237], v251 offset:12288\n"
    "v_mfma_f32_16x16x32_bf16 v[204:207], v[220:223], v[20:23], v[204:207]\n"
    "ds_read_b64_tr_b16 v[238:239], v251 offset:12800\n"
    "s_waitcnt vmcnt(6)\n"
    "s_barrier\n"
    "v_add_u32 v254, 98304, v246\n"
    "v_add_u32 v255, 98304, v247\n"
    "v_add_u32 v248, 114688, v240\n"
    "v_add_u32 v249, 114688, v241\n"
    "s_waitcnt lgkmcnt(6)\n"
    "v_mfma_f32_16x16x32_bf16 v[176:179], v[224:227], v[24:27], v[176:179]\n"
    "ds_read_b128 v[32:35], v254 offset:0\n"
    "ds_read_b64_tr_b16 v[208:209], v248 offset:0\n"
    "v_mfma_f32_16x16x32_bf16 v[180:183], v[224:227], v[28:31], v[180:183]\n"
    "ds_read_b128 v[36:39], v254 offset:2048\n"
    "ds_read_b64_tr_b16 v[210:211], v248 offset:512\n"
    "s_add_u32 m0, s74, 32768\n"
    "s_nop 0\n"
    "buffer_load_dwordx4 v244, s[36:39], s72 offen lds\n"
    "s_waitcnt lgkmcnt(8)\n"
    "v_mfma_f32_16x16x32_bf16 v[184:187], v[228:231], v[24:27], v[184:187]\n"
    "ds_read_b128 v[40:43], v255 offset:0\n"
    "ds_read_b64_tr_b16 v[212:213], v249 offset:0\n"
    "v_mfma_f32_16x16x32_bf16 v[188:191], v[228:231], v[28:31], v[188:191]\n"
    "ds_read_b128 v[44:47], v255 offset:2048\n"
    "ds_read_b64_tr_b16 v[214:215], v249 offset:512\n"
    "s_waitcnt lgkmcnt(10)\n"
    "v_mfma_f32_16x16x32_bf16 v[192:195], v[232:235], v[24:27], v[192:195]\n"
    "ds_read_b64_tr_b16 v[216:217], v248 offset:8192\n"
    "s_add_u32 m0, m0, 1024\n"
    "s_nop 0\n"
    "buffer_load_dwordx4 v245, s[36:39], s72 offen lds\n"
    "s_add_u32 s72, s72, 128\n"
    "v_mfma_f32_16x16x32_bf16 v[196:199], v[232:235], v[28:31], v[196:199]\n"
    "ds_read_b64_tr_b16 v[218:219], v248 offset:8704\n"
    "s_waitcnt lgkmcnt(10)\n"
    "v_mfma_f32_16x16x32_bf16 v[200:203], v[236:239], v[24:27], v[200:203]\n"
    "ds_read_b64_tr_b16 v[220:221], v249 offset:8192\n"
    "v_mfma_f32_16x16x32_bf16 v[204:207], v[236:239], v[28:31], v[204:207]\n"
    "ds_read_b64_tr_b16 v[222:223], v249 offset:8704\n"
    "s_add_u32 m0, s73, 49152\n"
    "s_nop 0\n"
    "buffer_load_dwordx4 v242, s[40:43], s69 offen lds\n"
    "s_waitcnt lgkmcnt(8)\n"
    "v_mfma_f32_16x16x32_bf16 v[144:147], v[208:211], v[32:35], v[144:147]\n"
    "ds_read_b64_tr_b16 v[224:225], v248 offset:4096\n"
    "v_mfma_f32_16x16x32_bf16 v[148:151], v[208:211], v[36:39], v[148:151]\n"
    "ds_read_b64_tr_b16 v[226:227], v248 offset:4608\n"
    "s_add_u32 m0, m0, 8192\n"
    "s_nop 0\n"
    "buffer_load_dwordx4 v242, s[44:47], s69 offen lds\n"
    "s_waitcnt lgkmcnt(6)\n"
    "v_mfma_f32_16x16x32_bf16 v[152:155], v[212:215], v[32:35], v[152:155]\n"
    "ds_read_b64_tr_b16 v[228:229], v249 offset:4096\n"
    "v_mfma_f32_16x16x32_bf16 v[156:159], v[212:215], v[36:39], v[156:159]\n"
    "ds_read_b64_tr_b16 v[230:231], v249 offset:4608\n"
    "s_waitcnt lgkmcnt(6)\n"
    "v_mfma_f32_16x16x32_bf16 v[160:163], v[216:219], v[32:35], v[160:163]\n"
    "ds_read_b64_tr_b16 v[232:233], v248 offset:12288\n"
    "s_add_u32 s70, s69, 128\n"
    "s_add_u32 m0, s73, 65536\n"
    "s_nop 0\n"
    "buffer_load_dwordx4 v242, s[40:43], s70 offen lds\n"
    "v_mfma_f32_16x16x32_bf16 v[164:167], v[216:219], v[36:39], v[164:167]\n"
    "ds_read_b64_tr_b16 v[234:235], v248 offset:12800\n"
    "s_waitcnt lgkmcnt(6)\n"
    "v_mfma_f32_16x16x32_bf16 v[168:171], v[220:223], v[32:35], v[168:171]\n"
    "ds_read_b64_tr_b16 v[236:237], v249 offset:12288\n"
    "v_mfma_f32_16x16x32_bf16 v[172:175], v[220:223], v[36:39], v[172:175]\n"
    "ds_read_b64_tr_b16 v[238:239], v249 offset:12800\n"
    "s_add_u32 m0, m0, 8192\n"
    "s_nop 0\n"
    "buffer_load_dwordx4 v242, s[44:47], s70 offen lds\n"
    "s_add_u32 s69, s69, 589824\n"
    "v_add_u32 v250, 131072, v240\n"
    "v_add_u32 v251, 131072, v241\n"
    "s_waitcnt lgkmcnt(6)\n"
    "v_mfma_f32_16x16x32_bf16 v[144:147], v[224:227], v[40:43], v[144:147]\n"
    "ds_read_b64_tr_b16 v[208:209], v250 offset:0\n"
    "v_mfma_f32_16x16x32_bf16 v[148:151], v[224:227], v[44:47], v[148:151]\n"
    "ds_read_b64_tr_b16 v[210:211], v250 offset:512\n"
    "s_waitcnt lgkmcnt(6)\n"
    "v_mfma_f32_16x16x32_bf16 v[152:155], v[228:231], v[40:43], v[152:155]\n"
    "ds_read_b64_tr_b16 v[212:213], v251 offset:0\n"
    "v_mfma_f32_16x16x32_bf16 v[156:159], v[228:231], v[44:47], v[156:159]\n"
    "ds_read_b64_tr_b16 v[214:215], v251 offset:512\n"
    "s_waitcnt lgkmcnt(6)\n"
    "v_mfma_f32_16x16x32_bf16 v[160:163], v[232:235], v[40:43], v[160:163]\n"
    "ds_read_b64_tr_b16 v[216:217], v250 offset:8192\n"
    "v_mfma_f32_16x16x32_bf16 v[164:167], v[232:235], v[44:47], v[164:167]\n"
    "ds_read_b64_tr_b16 v[218:219], v250 offset:8704\n"
    "s_waitcnt lgkmcnt(6)\n"
    "v_mfma_f32_16x16x32_bf16 v[168:171], v[236:239], v[40:43], v[168:171]\n"
    "ds_read_b64_tr_b16 v[220:221], v251 offset:8192\n"
    "v_mfma_f32_16x16x32_bf16 v[172:175], v[236:239], v[44:47], v[172:175]\n"
    "ds_read_b64_tr_b16 v[222:223], v251 offset:8704\n"
    "s_waitcnt lgkmcnt(6)\n"
    "v_mfma_f32_16x16x32_bf16 v[176:179], v[208:211], v[32:35], v[176:179]\n"
    "ds_read_b64_tr_b16 v[224:225], v250 offset:4096\n"
    "v_mfma_f32_16x16x32_bf16 v[180:183], v[208:211], v[36:39], v[180:183]\n"
    "ds_read_b64_tr_b16 v[226:227], v250 offset:4608\n"
    "s_waitcnt lgkmcnt(6)\n"
    "v_mfma_f32_16x16x32_bf16 v[184:187], v[212:215], v[32:35], v[184:187]\n"
    "ds_read_b64_tr_b16 v[228:229], v251 offset:4096\n"
    "v_mfma_f32_16x16x32_bf16 v[188:191], v[212:215], v[36:39], v[188:191]\n"
    "ds_read_b64_tr_b16 v[230:231], v251 offset:4608\n"
    "s_waitcnt lgkmcnt(6)\n"
    "v_mfma_f32_16x16x32_bf16 v[192:195], v[216:219], v[32:35], v[192:195]\n"
    "ds_read_b64_tr_b16 v[232:233], v250 offset:12288\n"
    "v_mfma_f32_16x16x32_bf16 v[196:199], v[216:219], v[36:39], v[196:199]\n"
    "ds_read_b64_tr_b16 v[234:235], v250 offset:12800\n"
    "s_waitcnt lgkmcnt(6)\n"
    "v_mfma_f32_16x16x32_bf16 v[200:203], v[220:223], v[32:35], v[200:203]\n"
    "ds_read_b64_tr_b16 v[236:237], v251 offset:12288\n"
    "v_mfma_f32_16x16x32_bf16 v[204:207], v[220:223], v[36:39], v[204:207]\n"
    "ds_read_b64_tr_b16 v[238:239], v251 offset:12800\n"
    "s_waitcnt vmcnt(6)\n"
    "s_barrier\n"
    "v_add_u32 v254, 147456, v246\n"
    "v_add_u32 v255, 147456, v247\n"
    "v_add_u32 v248, 0, v240\n"
    "v_add_u32 v249, 0, v241\n"
    "s_waitcnt lgkmcnt(6)\n"
    "v_mfma_f32_16x16x32_bf16 v[176:179], v[224:227], v[40:43], v[176:179]\n"
    "ds_read_b128 v[48:51], v254 offset:0\n"
    "ds_read_b64_tr_b16 v[208:209], v248 offset:0\n"
    "v_mfma_f32_16x16x32_bf16 v[180:183], v[224:227], v[44:47], v[180:183]\n"
    "ds_read_b128 v[52:55], v254 offset:2048\n"
    "ds_read_b64_tr_b16 v[210:211], v248 offset:512\n"
    "s_add_u32 m0, s74, 81920\n"
    "s_nop 0\n"
    "buffer_load_dwordx4 v244, s[36:39], s72 offen lds\n"
    "s_waitcnt lgkmcnt(8)\n"
    "v_mfma_f32_16x16x32_bf16 v[184:187], v[228:231], v[40:43], v[184:187]\n"
    "ds_read_b128 v[56:59], v255 offset:0\n"
    "ds_read_b64_tr_b16 v[212:213], v249 offset:0\n"
    "v_mfma_f32_16x16x32_bf16 v[188:191], v[228:231], v[44:47], v[188:191]\n"
    "ds_read_b128 v[60:63], v255 offset:2048\n"
    "ds_read_b64_tr_b16 v[214:215], v249 offset:512\n"
    "s_waitcnt lgkmcnt(10)\n"
    "v_mfma_f32_16x16x32_bf16 v[192:195], v[232:235], v[40:43], v[192:195]\n"
    "ds_read_b64_tr_b16 v[216:217], v248 offset:8192\n"
    "s_add_u32 m0, m0, 1024\n"
    "s_nop 0\n"
    "buffer_load_dwordx4 v245, s[36:39], s72 offen lds\n"
    "s_add_u32 s72, s72, 128\n"
    "v_mfma_f32_16x16x32_bf16 v[196:199], v[232:235], v[44:47], v[196:199]\n"
    "ds_read_b64_tr_b16 v[218:219], v248 offset:8704\n"
    "s_waitcnt lgkmcnt(10)\n"
    "v_mfma_f32_16x16x32_bf16 v[200:203], v[236:239], v[40:43], v[200:203]\n"
    "ds_read_b64_tr_b16 v[220:221], v249 offset:8192\n"
    "v_mfma_f32_16x16x32_bf16 v[204:207], v[236:239], v[44:47], v[204:207]\n"
    "ds_read_b64_tr_b16 v[222:223], v249 offset:8704\n"
    "s_add_u32 m0, s73, 98304\n"
    "s_nop 0\n"
    "buffer_load_dwordx4 v242, s[40:43], s69 offen lds\n"
    "s_waitcnt lgkmcnt(8)\n"
    "v_mfma_f32_16x16x32_bf16 v[144:147], v[208:211], v[48:51], v[144:147]\n"
    "ds_read_b64_tr_b16 v[224:225], v248 offset:4096\n"
    "v_mfma_f32_16x16x32_bf16 v[148:151], v[208:211], v[52:55], v[148:151]\n"
    "ds_read_b64_tr_b16 v[226:227], v248 offset:4608\n"
    "s_add_u32 m0, m0, 8192\n"
    "s_nop 0\n"
    "buffer_load_dwordx4 v242, s[44:47], s69 offen lds\n"
    "s_waitcnt lgkmcnt(6)\n"
    "v_mfma_f32_16x16x32_bf16 v[152:155], v[212:215], v[48:51], v[152:155]\n"
    "ds_read_b64_tr_b16 v[228:229], v249 offset:4096\n"
    "v_mfma_f32_16x16x32_bf16 v[156:159], v[212:215], v[52:55], v[156:159]\n"
    "ds_read_b64_tr_b16 v[230:231], v249 offset:4608\n"
    "s_waitcnt lgkmcnt(6)\n"
    "v_mfma_f32_16x16x32_bf16 v[160:163], v[216:219], v[48:51], v[160:163]\n"
    "ds_read_b64_tr_b16 v[232:233], v248 offset:12288\n"
    "s_add_u32 s70, s69, 128\n"
    "s_add_u32 m0, s73, 114688\n"
    "s_nop 0\n"
    "buffer_load_dwordx4 v242, s[40:43], s70 offen lds\n"
    "v_mfma_f32_16x16x32_bf16 v[164:167], v[216:219], v[52:55], v[164:167]\n"
    "ds_read_b64_tr_b16 v[234:235], v248 offset:12800\n"
    "s_waitcnt lgkmcnt(6)\n"
    "v_mfma_f32_16x16x32_bf16 v[168:171], v[220:223], v[48:51], v[168:171]\n"
    "ds_read_b64_tr_b16 v[236:237], v249 offset:12288\n"
    "v_mfma_f32_16x16x32_bf16 v[172:175], v[220:223], v[52:55], v[172:175]\n"
    "ds_read_b64_tr_b16 v[238:239], v249 offset:12800\n"
    "s_add_u32 m0, m0, 8192\n"
    "s_nop 0\n"
    "buffer_load_dwordx4 v242, s[44:47], s70 offen lds\n"
    "s_add_u32 s69, s69, 589824\n"
    "v_add_u32 v250, 16384, v240\n"
    "v_add_u32 v251, 16384, v241\n"
    "s_waitcnt lgkmcnt(6)\n"
    "v_mfma_f32_16x16x32_bf16 v[144:147], v[224:227], v[56:59], v[144:147]\n"
    "ds_read_b64_tr_b16 v[208:209], v250 offset:0\n"
    "v_mfma_f32_16x16x32_bf16 v[148:151], v[224:227], v[60:63], v[148:151]\n"
    "ds_read_b64_tr_b16 v[210:211], v250 offset:512\n"
    "s_waitcnt lgkmcnt(6)\n"
    "v_mfma_f32_16x16x32_bf16 v[152:155], v[228:231], v[56:59], v[152:155]\n"
    "ds_read_b64_tr_b16 v[212:213], v251 offset:0\n"
    "v_mfma_f32_16x16x32_bf16 v[156:159], v[228:231], v[60:63], v[156:159]\n"
    "ds_read_b64_tr_b16 v[214:215], v251 offset:512\n"
    "s_waitcnt lgkmcnt(6)\n"
    "v_mfma_f32_16x16x32_bf16 v[160:163], v[232:235], v[56:59], v[160:163]\n"
    "ds_read_b64_tr_b16 v[216:217], v250 offset:8192\n"
    "v_mfma_f32_16x16x32_bf16 v[164:167], v[232:235], v[60:63], v[164:167]\n"
    "ds_read_b64_tr_b16 v[218:219], v250 offset:8704\n"
    "s_waitcnt lgkmcnt(6)\n"
    "v_mfma_f32_16x16x32_bf16 v[168:171], v[236:239], v[56:59], v[168:171]\n"
    "ds_read_b64_tr_b16 v[220:221], v251 offset:8192\n"
    "v_mfma_f32_16x16x32_bf16 v[172:175], v[236:239], v[60:63], v[172:175]\n"
    "ds_read_b64_tr_b16 v[222:223], v251 offset:8704\n"
    "s_waitcnt lgkmcnt(6)\n"
    "v_mfma_f32_16x16x32_bf16 v[176:179], v[208:211], v[48:51], v[176:179]\n"
    "ds_read_b64_tr_b16 v[224:225], v250 offset:4096\n"
    "v_mfma_f32_16x16x32_bf16 v[180:183], v[208:211], v[52:55], v[180:183]\n"
    "ds_read_b64_tr_b16 v[226:227], v250 offset:4608\n"
    "s_waitcnt lgkmcnt(6)\n"
    "v_mfma_f32_16x16x32_bf16 v[184:187], v[212:215], v[48:51], v[184:187]\n"
    "ds_read_b64_tr_b16 v[228:229], v251 offset:4096\n"
    "v_mfma_f32_16x16x32_bf16 v[188:191], v[212:215], v[52:55], v[188:191]\n"
    "ds_read_b64_tr_b16 v[230:231], v251 offset:4608\n"
    "s_waitcnt lgkmcnt(6)\n"
    "v_mfma_f32_16x16x32_bf16 v[192:195], v[216:219], v[48:51], v[192:195]\n"
    "ds_read_b64_tr_b16 v[232:233], v250 offset:12288\n"
    "v_mfma_f32_16x16x32_bf16 v[196:199], v[216:219], v[52:55], v[196:199]\n"
    "ds_read_b64_tr_b16 v[234:235], v250 offset:12800\n"
    "s_waitcnt lgkmcnt(6)\n"
    "v_mfma_f32_16x16x32_bf16 v[200:203], v[220:223], v[48:51], v[200:203]\n"
    "ds_read_b64_tr_b16 v[236:237], v251 offset:12288\n"
    "v_mfma_f32_16x16x32_bf16 v[204:207], v[220:223], v[52:55], v[204:207]\n"
    "ds_read_b64_tr_b16 v[238:239], v251 offset:12800\n"
    "buffer_load_dword v128, v133, s[36:39], 0 offen\n"
    "s_waitcnt vmcnt(7)\n"
    "s_barrier\n"
    "v_add_u32 v254, 32768, v246\n"
    "v_add_u32 v255, 32768, v247\n"
    "v_add_u32 v248, 49152, v240\n"
    "v_add_u32 v249, 49152, v241\n"
    "s_waitcnt lgkmcnt(6)\n"
    "v_mfma_f32_16x16x32_bf16 v[176:179], v[224:227], v[56:59], v[176:179]\n"
    "ds_read_b128 v[64:67], v254 offset:0\n"
    "ds_read_b64_tr_b16 v[208:209], v248 offset:0\n"
    "v_mfma_f32_16x16x32_bf16 v[180:183], v[224:227], v[60:63], v[180:183]\n"
    "ds_read_b128 v[68:71], v254 offset:2048\n"
    "ds_read_b64_tr_b16 v[210:211], v248 offset:512\n"
    "s_add_u32 m0, s74, 131072\n"
    "s_nop 0\n"
    "buffer_load_dwordx4 v244, s[36:39], s72 offen lds\n"
    "s_waitcnt lgkmcnt(8)\n"
    "v_mfma_f32_16x16x32_bf16 v[184:187], v[228:231], v[56:59], v[184:187]\n"
    "ds_read_b128 v[72:75], v255 offset:0\n"
    "ds_read_b64_tr_b16 v[212:213], v249 offset:0\n"
    "v_mfma_f32_16x16x32_bf16 v[188:191], v[228:231], v[60:63], v[188:191]\n"
    "ds_read_b128 v[76:79], v255 offset:2048\n"
    "ds_read_b64_tr_b16 v[214:215], v249 offset:512\n"
    "s_waitcnt lgkmcnt(10)\n"
    "v_mfma_f32_16x16x32_bf16 v[192:195], v[232:235], v[56:59], v[192:195]\n"
    "ds_read_b64_tr_b16 v[216:217], v248 offset:8192\n"
    "s_add_u32 m0, m0, 1024\n"
    "s_nop 0\n"
    "buffer_load_dwordx4 v245, s[36:39], s72 offen lds\n"
    "s_add_u32 s72, s72, 128\n"
    "v_mfma_f32_16x16x32_bf16 v[196:199], v[232:235], v[60:63], v[196:199]\n"
    "ds_read_b64_tr_b16 v[218:219], v248 offset:8704\n"
    "s_waitcnt lgkmcnt(10)\n"
    "v_mfma_f32_16x16x32_bf16 v[200:203], v[236:239], v[56:59], v[200:203]\n"
    "ds_read_b64_tr_b16 v[220:221], v249 offset:8192\n"
    "v_mfma_f32_16x16x32_bf16 v[204:207], v[236:239], v[60:63], v[204:207]\n"
    "ds_read_b64_tr_b16 v[222:223], v249 offset:8704\n"
    "s_add_u32 m0, s73, 147456\n"
    "s_nop 0\n"
    "buffer_load_dwordx4 v242, s[40:43], s69 offen lds\n"
    "s_waitcnt lgkmcnt(8)\n"
    "v_mfma_f32_16x16x32_bf16 v[144:147], v[208:211], v[64:67], v[144:147]\n"
    "ds_read_b64_tr_b16 v[224:225], v248 offset:4096\n"
    "v_mfma_f32_16x16x32_bf16 v[148:151], v[208:211], v[68:71], v[148:151]\n"
    "ds_read_b64_tr_b16 v[226:227], v248 offset:4608\n"
    "s_add_u32 m0, m0, 8192\n"
    "s_nop 0\n"
    "buffer_load_dwordx4 v242, s[44:47], s69 offen lds\n"
    "s_waitcnt lgkmcnt(6)\n"
    "v_mfma_f32_16x16x32_bf16 v[152:155], v[212:215], v[64:67], v[152:155]\n"
    "ds_read_b64_tr_b16 v[228:229], v249 offset:4096\n"
    "v_mfma_f32_16x16x32_bf16 v[156:159], v[212:215], v[68:71], v[156:159]\n"
    "ds_read_b64_tr_b16 v[230:231], v249 offset:4608\n"
    "s_waitcnt lgkmcnt(6)\n"
    "v_mfma_f32_16x16x32_bf16 v[160:163], v[216:219], v[64:67], v[160:163]\n"
    "ds_read_b64_tr_b16 v[232:233], v248 offset:12288\n"
    "s_add_u32 s70, s69, 128\n"
    "s_add_u32 m0, s73, 0\n"
    "s_nop 0\n"
    "buffer_load_dwordx4 v242, s[40:43], s70 offen lds\n"
    "v_mfma_f32_16x16x32_bf16 v[164:167], v[216:219], v[68:71], v[164:167]\n"
    "ds_read_b64_tr_b16 v[234:235], v248 offset:12800\n"
    "s_waitcnt lgkmcnt(6)\n"
    "v_mfma_f32_16x16x32_bf16 v[168:171], v[220:223], v[64:67], v[168:171]\n"
    "ds_read_b64_tr_b16 v[236:237], v249 offset:12288\n"
    "v_mfma_f32_16x16x32_bf16 v[172:175], v[220:223], v[68:71], v[172:175]\n"
    "ds_read_b64_tr_b16 v[238:239], v249 offset:12800\n"
    "s_add_u32 m0, m0, 8192\n"
    "s_nop 0\n"
    "buffer_load_dwordx4 v242, s[44:47], s70 offen lds\n"
    "s_add_u32 s69, s69, 589824\n"
    "v_add_u32 v250, 65536, v240\n"
    "v_add_u32 v251, 65536, v241\n"
    "s_waitcnt lgkmcnt(6)\n"
    "v_mfma_f32_16x16x32_bf16 v[144:147], v[224:227], v[72:75], v[144:147]\n"
    "ds_read_b64_tr_b16 v[208:209], v250 offset:0\n"
    "v_mfma_f32_16x16x32_bf16 v[148:151], v[224:227], v[76:79], v[148:151]\n"
    "ds_read_b64_tr_b16 v[210:211], v250 offset:512\n"
    "s_waitcnt lgkmcnt(6)\n"
    "v_mfma_f32_16x16x32_bf16 v[152:155], v[228:231], v[72:75], v[152:155]\n"
    "ds_read_b64_tr_b16 v[212:213], v251 offset:0\n"
    "v_mfma_f32_16x16x32_bf16 v[156:159], v[228:231], v[76:79], v[156:159]\n"
    "ds_read_b64_tr_b16 v[214:215], v251 offset:512\n"
    "s_waitcnt lgkmcnt(6)\n"
    "v_mfma_f32_16x16x32_bf16 v[160:163], v[232:235], v[72:75], v[160:163]\n"
    "ds_read_b64_tr_b16 v[216:217], v250 offset:8192\n"
    "v_mfma_f32_16x16x32_bf16 v[164:167], v[232:235], v[76:79], v[164:167]\n"
    "ds_read_b64_tr_b16 v[218:219], v250 offset:8704\n"
    "s_waitcnt lgkmcnt(6)\n"
    "v_mfma_f32_16x16x32_bf16 v[168:171], v[236:239], v[72:75], v[168:171]\n"
    "ds_read_b64_tr_b16 v[220:221], v251 offset:8192\n"
    "v_mfma_f32_16x16x32_bf16 v[172:175], v[236:239], v[76:79], v[172:175]\n"
    "ds_read_b64_tr_b16 v[222:223], v251 offset:8704\n"
    "s_waitcnt lgkmcnt(6)\n"
    "v_mfma_f32_16x16x32_bf16 v[176:179], v[208:211], v[64:67], v[176:179]\n"
    "ds_read_b64_tr_b16 v[224:225], v250 offset:4096\n"
    "v_mfma_f32_16x16x32_bf16 v[180:183], v[208:211], v[68:71], v[180:183]\n"
    "ds_read_b64_tr_b16 v[226:227], v250 offset:4608\n"
    "s_waitcnt lgkmcnt(6)\n"
    "v_mfma_f32_16x16x32_bf16 v[184:187], v[212:215], v[64:67], v[184:187]\n"
    "ds_read_b64_tr_b16 v[228:229], v251 offset:4096\n"
    "v_mfma_f32_16x16x32_bf16 v[188:191], v[212:215], v[68:71], v[188:191]\n"
    "ds_read_b64_tr_b16 v[230:231], v251 offset:4608\n"
    "s_waitcnt lgkmcnt(6)\n"
    "v_mfma_f32_16x16x32_bf16 v[192:195], v[216:219], v[64:67], v[192:195]\n"
    "ds_read_b64_tr_b16 v[232:233], v250 offset:12288\n"
    "v_mfma_f32_16x16x32_bf16 v[196:199], v[216:219], v[68:71], v[196:199]\n"
    "ds_read_b64_tr_b16 v[234:235], v250 offset:12800\n"
    "s_waitcnt lgkmcnt(6)\n"
    "v_mfma_f32_16x16x32_bf16 v[200:203], v[220:223], v[64:67], v[200:203]\n"
    "ds_read_b64_tr_b16 v[236:237], v251 offset:12288\n"
    "v_mfma_f32_16x16x32_bf16 v[204:207], v[220:223], v[68:71], v[204:207]\n"
    "ds_read_b64_tr_b16 v[238:239], v251 offset:12800\n"
    "buffer_load_dword v129, v132, s[40:43], 0 offen\n"
    "s_waitcnt vmcnt(8)\n"
    "s_barrier\n"
    "v_add_u32 v254, 81920, v246\n"
    "v_add_u32 v255, 81920, v247\n"
    "v_add_u32 v248, 98304, v240\n"
    "v_add_u32 v249, 98304, v241\n"
    "s_waitcnt lgkmcnt(6)\n"
    "v_mfma_f32_16x16x32_bf16 v[176:179], v[224:227], v[72:75], v[176:179]\n"
    "ds_read_b128 v[80:83], v254 offset:0\n"
    "ds_read_b64_tr_b16 v[208:209], v248 offset:0\n"
    "v_mfma_f32_16x16x32_bf16 v[180:183], v[224:227], v[76:79], v[180:183]\n"
    "ds_read_b128 v[84:87], v254 offset:2048\n"
    "ds_read_b64_tr_b16 v[210:211], v248 offset:512\n"
    "s_add_u32 m0, s74, 16384\n"
    "s_nop 0\n"
    "buffer_load_dwordx4 v244, s[36:39], s72 offen lds\n"
    "s_waitcnt lgkmcnt(8)\n"
    "v_mfma_f32_16x16x32_bf16 v[184:187], v[228:231], v[72:75], v[184:187]\n"
    "ds_read_b128 v[88:91], v255 offset:0\n"
    "ds_read_b64_tr_b16 v[212:213], v249 offset:0\n"
    "v_mfma_f32_16x16x32_bf16 v[188:191], v[228:231], v[76:79], v[188:191]\n"
    "ds_read_b128 v[92:95], v255 offset:2048\n"
    "ds_read_b64_tr_b16 v[214:215], v249 offset:512\n"
    "s_waitcnt lgkmcnt(10)\n"
    "v_mfma_f32_16x16x32_bf16 v[192:195], v[232:235], v[72:75], v[192:195]\n"
    "ds_read_b64_tr_b16 v[216:217], v248 offset:8192\n"
    "s_add_u32 m0, m0, 1024\n"
    "s_nop 0\n"
    "buffer_load_dwordx4 v245, s[36:39], s72 offen lds\n"
    "s_add_u32 s72, s72, 128\n"
    "v_mfma_f32_16x16x32_bf16 v[196:199], v[232:235], v[76:79], v[196:199]\n"
    "ds_read_b64_tr_b16 v[218:219], v248 offset:8704\n"
    "s_waitcnt lgkmcnt(10)\n"
    "v_mfma_f32_16x16x32_bf16 v[200:203], v[236:239], v[72:75], v[200:203]\n"
    "ds_read_b64_tr_b16 v[220:221], v249 offset:8192\n"
    "v_mfma_f32_16x16x32_bf16 v[204:207], v[236:239], v[76:79], v[204:207]\n"
    "ds_read_b64_tr_b16 v[222:223], v249 offset:8704\n"
    "s_add_u32 m0, s73, 32768\n"
    "s_nop 0\n"
    "buffer_load_dwordx4 v242, s[40:43], s69 offen lds\n"
    "s_waitcnt lgkmcnt(8)\n"
    "v_mfma_f32_16x16x32_bf16 v[144:147], v[208:211], v[80:83], v[144:147]\n"
    "ds_read_b64_tr_b16 v[224:225], v248 offset:4096\n"
    "v_mfma_f32_16x16x32_bf16 v[148:151], v[208:211], v[84:87], v[148:151]\n"
    "ds_read_b64_tr_b16 v[226:227], v248 offset:4608\n"
    "s_add_u32 m0, m0, 8192\n"
    "s_nop 0\n"
    "buffer_load_dwordx4 v242, s[44:47], s69 offen lds\n"
    "s_waitcnt lgkmcnt(6)\n"
    "v_mfma_f32_16x16x32_bf16 v[152:155], v[212:215], v[80:83], v[152:155]\n"
    "ds_read_b64_tr_b16 v[228:229], v249 offset:4096\n"
    "v_mfma_f32_16x16x32_bf16 v[156:159], v[212:215], v[84:87], v[156:159]\n"
    "ds_read_b64_tr_b16 v[230:231], v249 offset:4608\n"
    "s_waitcnt lgkmcnt(6)\n"
    "v_mfma_f32_16x16x32_bf16 v[160:163], v[216:219], v[80:83], v[160:163]\n"
    "ds_read_b64_tr_b16 v[232:233], v248 offset:12288\n"
    "s_add_u32 s70, s69, 128\n"
    "s_add_u32 m0, s73, 49152\n"
    "s_nop 0\n"
    "buffer_load_dwordx4 v242, s[40:43], s70 offen lds\n"
    "v_mfma_f32_16x16x32_bf16 v[164:167], v[216:219], v[84:87], v[164:167]\n"
    "ds_read_b64_tr_b16 v[234:235], v248 offset:12800\n"
    "s_waitcnt lgkmcnt(6)\n"
    "v_mfma_f32_16x16x32_bf16 v[168:171], v[220:223], v[80:83], v[168:171]\n"
    "ds_read_b64_tr_b16 v[236:237], v249 offset:12288\n"
    "v_mfma_f32_16x16x32_bf16 v[172:175], v[220:223], v[84:87], v[172:175]\n"
    "ds_read_b64_tr_b16 v[238:239], v249 offset:12800\n"
    "s_add_u32 m0, m0, 8192\n"
    "s_nop 0\n"
    "buffer_load_dwordx4 v242, s[44:47], s70 offen lds\n"
    "s_add_u32 s69, s69, 589824\n"
    "v_add_u32 v250, 114688, v240\n"
    "v_add_u32 v251, 114688, v241\n"
    "s_waitcnt lgkmcnt(6)\n"
    "v_mfma_f32_16x16x32_bf16 v[144:147], v[224:227], v[88:91], v[144:147]\n"
    "ds_read_b64_tr_b16 v[208:209], v250 offset:0\n"
    "v_mfma_f32_16x16x32_bf16 v[148:151], v[224:227], v[92:95], v[148:151]\n"
    "ds_read_b64_tr_b16 v[210:211], v250 offset:512\n"
    "s_waitcnt lgkmcnt(6)\n"
    "v_mfma_f32_16x16x32_bf16 v[152:155], v[228:231], v[88:91], v[152:155]\n"
    "ds_read_b64_tr_b16 v[212:213], v251 offset:0\n"
    "v_mfma_f32_16x16x32_bf16 v[156:159], v[228:231], v[92:95], v[156:159]\n"
    "ds_read_b64_tr_b16 v[214:215], v251 offset:512\n"
    "s_waitcnt lgkmcnt(6)\n"
    "v_mfma_f32_16x16x32_bf16 v[160:163], v[232:235], v[88:91], v[160:163]\n"
    "ds_read_b64_tr_b16 v[216:217], v250 offset:8192\n"
    "v_mfma_f32_16x16x32_bf16 v[164:167], v[232:235], v[92:95], v[164:167]\n"
    "ds_read_b64_tr_b16 v[218:219], v250 offset:8704\n"
    "s_waitcnt lgkmcnt(6)\n"
    "v_mfma_f32_16x16x32_bf16 v[168:171], v[236:239], v[88:91], v[168:171]\n"
    "ds_read_b64_tr_b16 v[220:221], v251 offset:8192\n"
    "v_mfma_f32_16x16x32_bf16 v[172:175], v[236:239], v[92:95], v[172:175]\n"
    "ds_read_b64_tr_b16 v[222:223], v251 offset:8704\n"
    "s_waitcnt lgkmcnt(6)\n"
    "v_mfma_f32_16x16x32_bf16 v[176:179], v[208:211], v[80:83], v[176:179]\n"
    "ds_read_b64_tr_b16 v[224:225], v250 offset:4096\n"
    "v_mfma_f32_16x16x32_bf16 v[180:183], v[208:211], v[84:87], v[180:183]\n"
    "ds_read_b64_tr_b16 v[226:227], v250 offset:4608\n"
    "s_waitcnt lgkmcnt(6)\n"
    "v_mfma_f32_16x16x32_bf16 v[184:187], v[212:215], v[80:83], v[184:187]\n"
    "ds_read_b64_tr_b16 v[228:229], v251 offset:4096\n"
    "v_mfma_f32_16x16x32_bf16 v[188:191], v[212:215], v[84:87], v[188:191]\n"
    "ds_read_b64_tr_b16 v[230:231], v251 offset:4608\n"
    "s_waitcnt lgkmcnt(6)\n"
    "v_mfma_f32_16x16x32_bf16 v[192:195], v[216:219], v[80:83], v[192:195]\n"
    "ds_read_b64_tr_b16 v[232:233], v250 offset:12288\n"
    "v_mfma_f32_16x16x32_bf16 v[196:199], v[216:219], v[84:87], v[196:199]\n"
    "ds_read_b64_tr_b16 v[234:235], v250 offset:12800\n"
    "s_waitcnt lgkmcnt(6)\n"
    "v_mfma_f32_16x16x32_bf16 v[200:203], v[220:223], v[80:83], v[200:203]\n"
    "ds_read_b64_tr_b16 v[236:237], v251 offset:12288\n"
    "v_mfma_f32_16x16x32_bf16 v[204:207], v[220:223], v[84:87], v[204:207]\n"
    "ds_read_b64_tr_b16 v[238:239], v251 offset:12800\n"
    "buffer_load_dword v130, v132, s[44:47], 0 offen\n"
    "s_waitcnt vmcnt(8)\n"
    "s_barrier\n"
    "v_add_u32 v254, 131072, v246\n"
    "v_add_u32 v255, 131072, v247\n"
    "v_add_u32 v248, 147456, v240\n"
    "v_add_u32 v249, 147456, v241\n"
    "s_waitcnt lgkmcnt(6)\n"
    "v_mfma_f32_16x16x32_bf16 v[176:179], v[224:227], v[88:91], v[176:179]\n"
    "ds_read_b128 v[96:99], v254 offset:0\n"
    "ds_read_b64_tr_b16 v[208:209], v248 offset:0\n"
    "v_mfma_f32_16x16x32_bf16 v[180:183], v[224:227], v[92:95], v[180:183]\n"
    "ds_read_b128 v[100:103], v254 offset:2048\n"
    "ds_read_b64_tr_b16 v[210:211], v248 offset:512\n"
    "s_add_u32 m0, s74, 65536\n"
    "s_nop 0\n"
    "buffer_load_dwordx4 v244, s[36:39], s72 offen lds\n"
    "s_waitcnt lgkmcnt(8)\n"
    "v_mfma_f32_16x16x32_bf16 v[184:187], v[228:231], v[88:91], v[184:187]\n"
    "ds_read_b128 v[104:107], v255 offset:0\n"
    "ds_read_b64_tr_b16 v[212:213], v249 offset:0\n"
    "v_mfma_f32_16x16x32_bf16 v[188:191], v[228:231], v[92:95], v[188:191]\n"
    "ds_read_b128 v[108:111], v255 offset:2048\n"
    "ds_read_b64_tr_b16 v[214:215], v249 offset:512\n"
    "s_waitcnt lgkmcnt(10)\n"
    "v_mfma_f32_16x16x32_bf16 v[192:195], v[232:235], v[88:91], v[192:195]\n"
    "ds_read_b64_tr_b16 v[216:217], v248 offset:8192\n"
    "s_add_u32 m0, m0, 1024\n"
    "s_nop 0\n"
    "buffer_load_dwordx4 v245, s[36:39], s72 offen lds\n"
    "s_add_u32 s72, s72, 128\n"
    "v_mfma_f32_16x16x32_bf16 v[196:199], v[232:235], v[92:95], v[196:199]\n"
    "ds_read_b64_tr_b16 v[218:219], v248 offset:8704\n"
    "s_waitcnt lgkmcnt(10)\n"
    "v_mfma_f32_16x16x32_bf16 v[200:203], v[236:239], v[88:91], v[200:203]\n"
    "ds_read_b64_tr_b16 v[220:221], v249 offset:8192\n"
    "v_mfma_f32_16x16x32_bf16 v[204:207], v[236:239], v[92:95], v[204:207]\n"
    "ds_read_b64_tr_b16 v[222:223], v249 offset:8704\n"
    "s_add_u32 m0, s73, 81920\n"
    "s_nop 0\n"
    "buffer_load_dwordx4 v242, s[40:43], s69 offen lds\n"
    "s_waitcnt lgkmcnt(8)\n"
    "v_mfma_f32_16x16x32_bf16 v[144:147], v[208:211], v[96:99], v[144:147]\n"
    "ds_read_b64_tr_b16 v[224:225], v248 offset:4096\n"
    "v_mfma_f32_16x16x32_bf16 v[148:151], v[208:211], v[100:103], v[148:151]\n"
    "ds_read_b64_tr_b16 v[226:227], v248 offset:4608\n"
    "s_add_u32 m0, m0, 8192\n"
    "s_nop 0\n"
    "buffer_load_dwordx4 v242, s[44:47], s69 offen lds\n"
    "s_waitcnt lgkmcnt(6)\n"
    "v_mfma_f32_16x16x32_bf16 v[152:155], v[212:215], v[96:99], v[152:155]\n"
    "ds_read_b64_tr_b16 v[228:229], v249 offset:4096\n"
    "v_mfma_f32_16x16x32_bf16 v[156:159], v[212:215], v[100:103], v[156:159]\n"
    "ds_read_b64_tr_b16 v[230:231], v249 offset:4608\n"
    "s_waitcnt lgkmcnt(6)\n"
    "v_mfma_f32_16x16x32_bf16 v[160:163], v[216:219], v[96:99], v[160:163]\n"
    "ds_read_b64_tr_b16 v[232:233], v248 offset:12288\n"
    "s_add_u32 s70, s69, 128\n"
    "s_add_u32 m0, s73, 98304\n"
    "s_nop 0\n"
    "buffer_load_dwordx4 v242, s[40:43], s70 offen lds\n"
    "v_mfma_f32_16x16x32_bf16 v[164:167], v[216:219], v[100:103], v[164:167]\n"
    "ds_read_b64_tr_b16 v[234:235], v248 offset:12800\n"
    "s_waitcnt lgkmcnt(6)\n"
    "v_mfma_f32_16x16x32_bf16 v[168:171], v[220:223], v[96:99], v[168:171]\n"
    "ds_read_b64_tr_b16 v[236:237], v249 offset:12288\n"
    "v_mfma_f32_16x16x32_bf16 v[172:175], v[220:223], v[100:103], v[172:175]\n"
    "ds_read_b64_tr_b16 v[238:239], v249 offset:12800\n"
    "s_add_u32 m0, m0, 8192\n"
    "s_nop 0\n"
    "buffer_load_dwordx4 v242, s[44:47], s70 offen lds\n"
    "s_add_u32 s69, s69, 589824\n"
    "v_add_u32 v250, 0, v240\n"
    "v_add_u32 v251, 0, v241\n"
    "s_waitcnt lgkmcnt(6)\n"
    "v_mfma_f32_16x16x32_bf16 v[144:147], v[224:227], v[104:107], v[144:147]\n"
    "ds_read_b64_tr_b16 v[208:209], v250 offset:0\n"
    "v_mfma_f32_16x16x32_bf16 v[148:151], v[224:227], v[108:111], v[148:151]\n"
    "ds_read_b64_tr_b16 v[210:211], v250 offset:512\n"
    "s_waitcnt lgkmcnt(6)\n"
    "v_mfma_f32_16x16x32_bf16 v[152:155], v[228:231], v[104:107], v[152:155]\n"
    "ds_read_b64_tr_b16 v[212:213], v251 offset:0\n"
    "v_mfma_f32_16x16x32_bf16 v[156:159], v[228:231], v[108:111], v[156:159]\n"
    "ds_read_b64_tr_b16 v[214:215], v251 offset:512\n"
    "s_waitcnt lgkmcnt(6)\n"
    "v_mfma_f32_16x16x32_bf16 v[160:163], v[232:235], v[104:107], v[160:163]\n"
    "ds_read_b64_tr_b16 v[216:217], v250 offset:8192\n"
    "v_mfma_f32_16x16x32_bf16 v[164:167], v[232:235], v[108:111], v[164:167]\n"
    "ds_read_b64_tr_b16 v[218:219], v250 offset:8704\n"
    "s_waitcnt lgkmcnt(6)\n"
    "v_mfma_f32_16x16x32_bf16 v[168:171], v[236:239], v[104:107], v[168:171]\n"
    "ds_read_b64_tr_b16 v[220:221], v251 offset:8192\n"
    "v_mfma_f32_16x16x32_bf16 v[172:175], v[236:239], v[108:111], v[172:175]\n"
    "ds_read_b64_tr_b16 v[222:223], v251 offset:8704\n"
    "s_waitcnt lgkmcnt(6)\n"
    "v_mfma_f32_16x16x32_bf16 v[176:179], v[208:211], v[96:99], v[176:179]\n"
    "ds_read_b64_tr_b16 v[224:225], v250 offset:4096\n"
    "v_mfma_f32_16x16x32_bf16 v[180:183], v[208:211], v[100:103], v[180:183]\n"
    "ds_read_b64_tr_b16 v[226:227], v250 offset:4608\n"
    "s_waitcnt lgkmcnt(6)\n"
    "v_mfma_f32_16x16x32_bf16 v[184:187], v[212:215], v[96:99], v[184:187]\n"
    "ds_read_b64_tr_b16 v[228:229], v251 offset:4096\n"
    "v_mfma_f32_16x16x32_bf16 v[188:191], v[212:215], v[100:103], v[188:191]\n"
    "ds_read_b64_tr_b16 v[230:231], v251 offset:4608\n"
    "s_waitcnt lgkmcnt(6)\n"
    "v_mfma_f32_16x16x32_bf16 v[192:195], v[216:219], v[96:99], v[192:195]\n"
    "ds_read_b64_tr_b16 v[232:233], v250 offset:12288\n"
    "v_mfma_f32_16x16x32_bf16 v[196:199], v[216:219], v[100:103], v[196:199]\n"
    "ds_read_b64_tr_b16 v[234:235], v250 offset:12800\n"
    "s_waitcnt lgkmcnt(6)\n"
    "v_mfma_f32_16x16x32_bf16 v[200:203], v[220:223], v[96:99], v[200:203]\n"
    "ds_read_b64_tr_b16 v[236:237], v251 offset:12288\n"
    "v_mfma_f32_16x16x32_bf16 v[204:207], v[220:223], v[100:103], v[204:207]\n"
    "ds_read_b64_tr_b16 v[238:239], v251 offset:12800\n"
    "s_waitcnt vmcnt(7)\n"
    "s_barrier\n"
    "v_add_u32 v254, 16384, v246\n"
    "v_add_u32 v255, 16384, v247\n"
    "v_add_u32 v248, 32768, v240\n"
    "v_add_u32 v249, 32768, v241\n"
    "s_waitcnt lgkmcnt(6)\n"
    "v_mfma_f32_16x16x32_bf16 v[176:179], v[224:227], v[104:107], v[176:179]\n"
    "ds_read_b128 v[112:115], v254 offset:0\n"
    "ds_read_b64_tr_b16 v[208:209], v248 offset:0\n"
    "v_mfma_f32_16x16x32_bf16 v[180:183], v[224:227], v[108:111], v[180:183]\n"
    "ds_read_b128 v[116:119], v254 offset:2048\n"
    "ds_read_b64_tr_b16 v[210:211], v248 offset:512\n"
    "s_mov_b32 s71, s78\n"
    "s_add_u32 m0, s73, 114688\n"
    "s_nop 0\n"
    "buffer_load_dwordx4 v242, s[40:43], s71 offen lds\n"
    "s_waitcnt lgkmcnt(8)\n"
    "v_mfma_f32_16x16x32_bf16 v[184:187], v[228:231], v[104:107], v[184:187]\n"
    "ds_read_b128 v[120:123], v255 offset:0\n"
    "ds_read_b64_tr_b16 v[212:213], v249 offset:0\n"
    "v_mfma_f32_16x16x32_bf16 v[188:191], v[228:231], v[108:111], v[188:191]\n"
    "ds_read_b128 v[124:127], v255 offset:2048\n"
    "ds_read_b64_tr_b16 v[214:215], v249 offset:512\n"
    "s_waitcnt lgkmcnt(10)\n"
    "v_mfma_f32_16x16x32_bf16 v[192:195], v[232:235], v[104:107], v[192:195]\n"
    "ds_read_b64_tr_b16 v[216:217], v248 offset:8192\n"
    "s_add_u32 m0, m0, 8192\n"
    "s_nop 0\n"
    "buffer_load_dwordx4 v242, s[44:47], s71 offen lds\n"
    "s_add_u32 s71, s71, 589824\n"
    "v_mfma_f32_16x16x32_bf16 v[196:199], v[232:235], v[108:111], v[196:199]\n"
    "ds_read_b64_tr_b16 v[218:219], v248 offset:8704\n"
    "s_waitcnt lgkmcnt(10)\n"
    "v_mfma_f32_16x16x32_bf16 v[200:203], v[236:239], v[104:107], v[200:203]\n"
    "ds_read_b64_tr_b16 v[220:221], v249 offset:8192\n"
    "v_mfma_f32_16x16x32_bf16 v[204:207], v[236:239], v[108:111], v[204:207]\n"
    "ds_read_b64_tr_b16 v[222:223], v249 offset:8704\n"
    "s_add_u32 m0, s73, 131072\n"
    "s_nop 0\n"
    "buffer_load_dwordx4 v242, s[40:43], s71 offen lds\n"
    "s_waitcnt lgkmcnt(8)\n"
    "v_mfma_f32_16x16x32_bf16 v[144:147], v[208:211], v[112:115], v[144:147]\n"
    "ds_read_b64_tr_b16 v[224:225], v248 offset:4096\n"
    "v_mfma_f32_16x16x32_bf16 v[148:151], v[208:211], v[116:119], v[148:151]\n"
    "ds_read_b64_tr_b16 v[226:227], v248 offset:4608\n"
    "s_add_u32 m0, m0, 8192\n"
    "s_nop 0\n"
    "buffer_load_dwordx4 v242, s[44:47], s71 offen lds\n"
    "s_add_u32 s71, s71, 589824\n"
    "s_waitcnt lgkmcnt(6)\n"
    "v_mfma_f32_16x16x32_bf16 v[152:155], v[212:215], v[112:115], v[152:155]\n"
    "ds_read_b64_tr_b16 v[228:229], v249 offset:4096\n"
    "v_mfma_f32_16x16x32_bf16 v[156:159], v[212:215], v[116:119], v[156:159]\n"
    "ds_read_b64_tr_b16 v[230:231], v249 offset:4608\n"
    "s_waitcnt lgkmcnt(6)\n"
    "v_mfma_f32_16x16x32_bf16 v[160:163], v[216:219], v[112:115], v[160:163]\n"
    "ds_read_b64_tr_b16 v[232:233], v248 offset:12288\n"
    "s_add_u32 m0, s73, 147456\n"
    "s_nop 0\n"
    "buffer_load_dwordx4 v242, s[40:43], s71 offen lds\n"
    "v_mfma_f32_16x16x32_bf16 v[164:167], v[216:219], v[116:119], v[164:167]\n"
    "ds_read_b64_tr_b16 v[234:235], v248 offset:12800\n"
    "s_waitcnt lgkmcnt(6)\n"
    "v_mfma_f32_16x16x32_bf16 v[168:171], v[220:223], v[112:115], v[168:171]\n"
    "ds_read_b64_tr_b16 v[236:237], v249 offset:12288\n"
    "v_mfma_f32_16x16x32_bf16 v[172:175], v[220:223], v[116:119], v[172:175]\n"
    "ds_read_b64_tr_b16 v[238:239], v249 offset:12800\n"
    "s_add_u32 m0, m0, 8192\n"
    "s_nop 0\n"
    "buffer_load_dwordx4 v242, s[44:47], s71 offen lds\n"
    "s_add_u32 s71, s71, 589824\n"
    "v_add_u32 v250, 49152, v240\n"
    "v_add_u32 v251, 49152, v241\n"
    "s_waitcnt lgkmcnt(6)\n"
    "v_mfma_f32_16x16x32_bf16 v[144:147], v[224:227], v[120:123], v[144:147]\n"
    "ds_read_b64_tr_b16 v[208:209], v250 offset:0\n"
    "v_mfma_f32_16x16x32_bf16 v[148:151], v[224:227], v[124:127], v[148:151]\n"
    "ds_read_b64_tr_b16 v[210:211], v250 offset:512\n"
    "s_waitcnt lgkmcnt(6)\n"
    "v_mfma_f32_16x16x32_bf16 v[152:155], v[228:231], v[120:123], v[152:155]\n"
    "ds_read_b64_tr_b16 v[212:213], v251 offset:0\n"
    "v_mfma_f32_16x16x32_bf16 v[156:159], v[228:231], v[124:127], v[156:159]\n"
    "ds_read_b64_tr_b16 v[214:215], v251 offset:512\n"
    "s_waitcnt lgkmcnt(6)\n"
    "v_mfma_f32_16x16x32_bf16 v[160:163], v[232:235], v[120:123], v[160:163]\n"
    "ds_read_b64_tr_b16 v[216:217], v250 offset:8192\n"
    "v_mfma_f32_16x16x32_bf16 v[164:167], v[232:235], v[124:127], v[164:167]\n"
    "ds_read_b64_tr_b16 v[218:219], v250 offset:8704\n"
    "s_waitcnt lgkmcnt(6)\n"
    "v_mfma_f32_16x16x32_bf16 v[168:171], v[236:239], v[120:123], v[168:171]\n"
    "ds_read_b64_tr_b16 v[220:221], v251 offset:8192\n"
    "v_mfma_f32_16x16x32_bf16 v[172:175], v[236:239], v[124:127], v[172:175]\n"
    "ds_read_b64_tr_b16 v[222:223], v251 offset:8704\n"
    "s_waitcnt lgkmcnt(6)\n"
    "v_mfma_f32_16x16x32_bf16 v[176:179], v[208:211], v[112:115], v[176:179]\n"
    "ds_read_b64_tr_b16 v[224:225], v250 offset:4096\n"
    "v_mfma_f32_16x16x32_bf16 v[180:183], v[208:211], v[116:119], v[180:183]\n"
    "ds_read_b64_tr_b16 v[226:227], v250 offset:4608\n"
    "s_waitcnt lgkmcnt(6)\n"
    "v_mfma_f32_16x16x32_bf16 v[184:187], v[212:215], v[112:115], v[184:187]\n"
    "ds_read_b64_tr_b16 v[228:229], v251 offset:4096\n"
    "v_mfma_f32_16x16x32_bf16 v[188:191], v[212:215], v[116:119], v[188:191]\n"
    "ds_read_b64_tr_b16 v[230:231], v251 offset:4608\n"
    "s_waitcnt lgkmcnt(6)\n"
    "v_mfma_f32_16x16x32_bf16 v[192:195], v[216:219], v[112:115], v[192:195]\n"
    "ds_read_b64_tr_b16 v[232:233], v250 offset:12288\n"
    "v_mfma_f32_16x16x32_bf16 v[196:199], v[216:219], v[116:119], v[196:199]\n"
    "ds_read_b64_tr_b16 v[234:235], v250 offset:12800\n"
    "s_waitcnt lgkmcnt(6)\n"
    "v_mfma_f32_16x16x32_bf16 v[200:203], v[220:223], v[112:115], v[200:203]\n"
    "ds_read_b64_tr_b16 v[236:237], v251 offset:12288\n"
    "v_mfma_f32_16x16x32_bf16 v[204:207], v[220:223], v[116:119], v[204:207]\n"
    "ds_read_b64_tr_b16 v[238:239], v251 offset:12800\n"
    "s_waitcnt vmcnt(6)\n"
    "s_barrier\n"
    "v_add_u32 v254, 65536, v246\n"
    "v_add_u32 v255, 65536, v247\n"
    "v_add_u32 v248, 81920, v240\n"
    "v_add_u32 v249, 81920, v241\n"
    "s_waitcnt lgkmcnt(6)\n"
    "v_mfma_f32_16x16x32_bf16 v[176:179], v[224:227], v[120:123], v[176:179]\n"
    "ds_read_b128 v[128:131], v254 offset:0\n"
    "ds_read_b64_tr_b16 v[208:209], v248 offset:0\n"
    "v_mfma_f32_16x16x32_bf16 v[180:183], v[224:227], v[124:127], v[180:183]\n"
    "ds_read_b128 v[132:135], v254 offset:2048\n"
    "ds_read_b64_tr_b16 v[210:211], v248 offset:512\n"
    "s_add_u32 m0, s73, 0\n"
    "s_nop 0\n"
    "buffer_load_dwordx4 v242, s[40:43], s71 offen lds\n"
    "s_waitcnt lgkmcnt(8)\n"
    "v_mfma_f32_16x16x32_bf16 v[184:187], v[228:231], v[120:123], v[184:187]\n"
    "ds_read_b128 v[136:139], v255 offset:0\n"
    "ds_read_b64_tr_b16 v[212:213], v249 offset:0\n"
    "v_mfma_f32_16x16x32_bf16 v[188:191], v[228:231], v[124:127], v[188:191]\n"
    "ds_read_b128 v[140:143], v255 offset:2048\n"
    "ds_read_b64_tr_b16 v[214:215], v249 offset:512\n"
    "s_waitcnt lgkmcnt(10)\n"
    "v_mfma_f32_16x16x32_bf16 v[192:195], v[232:235], v[120:123], v[192:195]\n"
    "ds_read_b64_tr_b16 v[216:217], v248 offset:8192\n"
    "s_add_u32 m0, m0, 8192\n"
    "s_nop 0\n"
    "buffer_load_dwordx4 v242, s[44:47], s71 offen lds\n"
    "s_add_u32 s71, s71, 589824\n"
    "v_mfma_f32_16x16x32_bf16 v[196:199], v[232:235], v[124:127], v[196:199]\n"
    "ds_read_b64_tr_b16 v[218:219], v248 offset:8704\n"
    "s_waitcnt lgkmcnt(10)\n"
    "v_mfma_f32_16x16x32_bf16 v[200:203], v[236:239], v[120:123], v[200:203]\n"
    "ds_read_b64_tr_b16 v[220:221], v249 offset:8192\n"
    "v_mfma_f32_16x16x32_bf16 v[204:207], v[236:239], v[124:127], v[204:207]\n"
    "ds_read_b64_tr_b16 v[222:223], v249 offset:8704\n"
    "s_add_u32 m0, s73, 16384\n"
    "s_nop 0\n"
    "buffer_load_dwordx4 v242, s[40:43], s71 offen lds\n"
    "s_waitcnt lgkmcnt(8)\n"
    "v_mfma_f32_16x16x32_bf16 v[144:147], v[208:211], v[128:131], v[144:147]\n"
    "ds_read_b64_tr_b16 v[224:225], v248 offset:4096\n"
    "v_mfma_f32_16x16x32_bf16 v[148:151], v[208:211], v[132:135], v[148:151]\n"
    "ds_read_b64_tr_b16 v[226:227], v248 offset:4608\n"
    "s_add_u32 m0, m0, 8192\n"
    "s_nop 0\n"
    "buffer_load_dwordx4 v242, s[44:47], s71 offen lds\n"
    "s_add_u32 s71, s71, 589824\n"
    "s_waitcnt lgkmcnt(6)\n"
    "v_mfma_f32_16x16x32_bf16 v[152:155], v[212:215], v[128:131], v[152:155]\n"
    "ds_read_b64_tr_b16 v[228:229], v249 offset:4096\n"
    "v_mfma_f32_16x16x32_bf16 v[156:159], v[212:215], v[132:135], v[156:159]\n"
    "ds_read_b64_tr_b16 v[230:231], v249 offset:4608\n"
    "s_waitcnt lgkmcnt(6)\n"
    "v_mfma_f32_16x16x32_bf16 v[160:163], v[216:219], v[128:131], v[160:163]\n"
    "ds_read_b64_tr_b16 v[232:233], v248 offset:12288\n"
    "s_add_u32 m0, s73, 32768\n"
    "s_nop 0\n"
    "buffer_load_dwordx4 v242, s[40:43], s71 offen lds\n"
    "v_mfma_f32_16x16x32_bf16 v[164:167], v[216:219], v[132:135], v[164:167]\n"
    "ds_read_b64_tr_b16 v[234:235], v248 offset:12800\n"
    "s_waitcnt lgkmcnt(6)\n"
    "v_mfma_f32_16x16x32_bf16 v[168:171], v[220:223], v[128:131], v[168:171]\n"
    "ds_read_b64_tr_b16 v[236:237], v249 offset:12288\n"
    "v_mfma_f32_16x16x32_bf16 v[172:175], v[220:223], v[132:135], v[172:175]\n"
    "ds_read_b64_tr_b16 v[238:239], v249 offset:12800\n"
    "s_add_u32 m0, m0, 8192\n"
    "s_nop 0\n"
    "buffer_load_dwordx4 v242, s[44:47], s71 offen lds\n"
    "s_add_u32 s71, s71, 589824\n"
    "v_add_u32 v250, 98304, v240\n"
    "v_add_u32 v251, 98304, v241\n"
    "s_waitcnt lgkmcnt(6)\n"
    "v_mfma_f32_16x16x32_bf16 v[144:147], v[224:227], v[136:139], v[144:147]\n"
    "ds_read_b64_tr_b16 v[208:209], v250 offset:0\n"
    "v_mfma_f32_16x16x32_bf16 v[148:151], v[224:227], v[140:143], v[148:151]\n"
    "ds_read_b64_tr_b16 v[210:211], v250 offset:512\n"
    "s_waitcnt lgkmcnt(6)\n"
    "v_mfma_f32_16x16x32_bf16 v[152:155], v[228:231], v[136:139], v[152:155]\n"
    "ds_read_b64_tr_b16 v[212:213], v251 offset:0\n"
    "v_mfma_f32_16x16x32_bf16 v[156:159], v[228:231], v[140:143], v[156:159]\n"
    "ds_read_b64_tr_b16 v[214:215], v251 offset:512\n"
    "s_waitcnt lgkmcnt(6)\n"
    "v_mfma_f32_16x16x32_bf16 v[160:163], v[232:235], v[136:139], v[160:163]\n"
    "ds_read_b64_tr_b16 v[216:217], v250 offset:8192\n"
    "v_mfma_f32_16x16x32_bf16 v[164:167], v[232:235], v[140:143], v[164:167]\n"
    "ds_read_b64_tr_b16 v[218:219], v250 offset:8704\n"
    "s_waitcnt lgkmcnt(6)\n"
    "v_mfma_f32_16x16x32_bf16 v[168:171], v[236:239], v[136:139], v[168:171]\n"
    "ds_read_b64_tr_b16 v[220:221], v251 offset:8192\n"
    "v_mfma_f32_16x16x32_bf16 v[172:175], v[236:239], v[140:143], v[172:175]\n"
    "ds_read_b64_tr_b16 v[222:223], v251 offset:8704\n"
    "s_waitcnt lgkmcnt(6)\n"
    "v_mfma_f32_16x16x32_bf16 v[176:179], v[208:211], v[128:131], v[176:179]\n"
    "ds_read_b64_tr_b16 v[224:225], v250 offset:4096\n"
    "v_mfma_f32_16x16x32_bf16 v[180:183], v[208:211], v[132:135], v[180:183]\n"
    "ds_read_b64_tr_b16 v[226:227], v250 offset:4608\n"
    "v_mul_f32 v160, v144, v160\n"
    "v_mul_f32 v161, v145, v161\n"
    "v_mul_f32 v162, v146, v162\n"
    "v_mul_f32 v163, v147, v163\n"
    "v_mul_f32 v144, 0xbfb8aa3b, v144\n"
    "v_mul_f32 v145, 0xbfb8aa3b, v145\n"
    "v_mul_f32 v146, 0xbfb8aa3b, v146\n"
    "s_waitcnt lgkmcnt(6)\n"
    "v_mfma_f32_16x16x32_bf16 v[184:187], v[212:215], v[128:131], v[184:187]\n"
    "ds_read_b64_tr_b16 v[228:229], v251 offset:4096\n"
    "v_mul_f32 v147, 0xbfb8aa3b, v147\n"
    "v_exp_f32 v144, v144\n"
    "v_exp_f32 v145, v145\n"
    "v_exp_f32 v146, v146\n"
    "v_exp_f32 v147, v147\n"
    "v_add_f32 v144, 1.0, v144\n"
    "v_add_f32 v145, 1.0, v145\n"
    "v_mfma_f32_16x16x32_bf16 v[188:191], v[212:215], v[132:135], v[188:191]\n"
    "ds_read_b64_tr_b16 v[230:231], v251 offset:4608\n"
    "v_add_f32 v146, 1.0, v146\n"
    "v_add_f32 v147, 1.0, v147\n"
    "v_rcp_f32 v144, v144\n"
    "v_rcp_f32 v145, v145\n"
    "v_rcp_f32 v146, v146\n"
    "v_rcp_f32 v147, v147\n"
    "v_mul_f32 v160, v160, v144\n"
    "s_waitcnt lgkmcnt(6)\n"
    "v_mfma_f32_16x16x32_bf16 v[192:195], v[216:219], v[128:131], v[192:195]\n"
    "ds_read_b64_tr_b16 v[232:233], v250 offset:12288\n"
    "v_mul_f32 v161, v161, v145\n"
    "v_mul_f32 v162, v162, v146\n"
    "v_mul_f32 v163, v163, v147\n"
    "v_mul_f32 v168, v152, v168\n"
    "v_mul_f32 v169, v153, v169\n"
    "v_mul_f32 v170, v154, v170\n"
    "v_mul_f32 v171, v155, v171\n"
    "v_mfma_f32_16x16x32_bf16 v[196:199], v[216:219], v[132:135], v[196:199]\n"
    "ds_read_b64_tr_b16 v[234:235], v250 offset:12800\n"
    "v_mul_f32 v152, 0xbfb8aa3b, v152\n"
    "v_mul_f32 v153, 0xbfb8aa3b, v153\n"
    "v_mul_f32 v154, 0xbfb8aa3b, v154\n"
    "v_mul_f32 v155, 0xbfb8aa3b, v155\n"
    "v_exp_f32 v152, v152\n"
    "v_exp_f32 v153, v153\n"
    "v_exp_f32 v154, v154\n"
    "s_waitcnt lgkmcnt(6)\n"
    "v_mfma_f32_16x16x32_bf16 v[200:203], v[220:223], v[128:131], v[200:203]\n"
    "ds_read_b64_tr_b16 v[236:237], v251 offset:12288\n"
    "v_exp_f32 v155, v155\n"
    "v_add_f32 v152, 1.0, v152\n"
    "v_add_f32 v153, 1.0, v153\n"
    "v_add_f32 v154, 1.0, v154\n"
    "v_add_f32 v155, 1.0, v155\n"
    "v_rcp_f32 v152, v152\n"
    "v_rcp_f32 v153, v153\n"
    "v_mfma_f32_16x16x32_bf16 v[204:207], v[220:223], v[132:135], v[204:207]\n"
    "ds_read_b64_tr_b16 v[238:239], v251 offset:12800\n"
    "v_rcp_f32 v154, v154\n"
    "v_rcp_f32 v155, v155\n"
    "v_mul_f32 v168, v168, v152\n"
    "v_mul_f32 v169, v169, v153\n"
    "v_mul_f32 v170, v170, v154\n"
    "v_mul_f32 v171, v171, v155\n"
    "v_cvt_pk_bf16_f32 v144, v160, v161\n"
    "s_waitcnt vmcnt(8)\n"
    "s_barrier\n"
    "v_add_u32 v248, 114688, v240\n"
    "v_add_u32 v249, 114688, v241\n"
    "s_waitcnt lgkmcnt(6)\n"
    "v_mfma_f32_16x16x32_bf16 v[176:179], v[224:227], v[136:139], v[176:179]\n"
    "ds_read_b64_tr_b16 v[208:209], v248 offset:0\n"
    "v_cvt_pk_bf16_f32 v145, v162, v163\n"
    "v_cvt_pk_bf16_f32 v146, v168, v169\n"
    "v_cvt_pk_bf16_f32 v147, v170, v171\n"
    "buffer_store_dwordx4 v[144:147], v252, s[56:59], s75 offen offset:0 sc0 sc1\n"
    "v_mul_f32 v164, v148, v164\n"
    "v_mul_f32 v165, v149, v165\n"
    "v_mul_f32 v166, v150, v166\n"
    "v_mfma_f32_16x16x32_bf16 v[180:183], v[224:227], v[140:143], v[180:183]\n"
    "ds_read_b64_tr_b16 v[210:211], v248 offset:512\n"
    "s_add_u32 m0, s73, 49152\n"
    "s_nop 0\n"
    "buffer_load_dwordx4 v242, s[40:43], s71 offen lds\n"
    "v_mul_f32 v167, v151, v167\n"
    "v_mul_f32 v148, 0xbfb8aa3b, v148\n"
    "v_mul_f32 v149, 0xbfb8aa3b, v149\n"
    "v_mul_f32 v150, 0xbfb8aa3b, v150\n"
    "v_mul_f32 v151, 0xbfb8aa3b, v151\n"
    "v_exp_f32 v148, v148\n"
    "v_exp_f32 v149, v149\n"
    "s_waitcnt lgkmcnt(6)\n"
    "v_mfma_f32_16x16x32_bf16 v[184:187], v[228:231], v[136:139], v[184:187]\n"
    "ds_read_b64_tr_b16 v[212:213], v249 offset:0\n"
    "v_exp_f32 v150, v150\n"
    "v_exp_f32 v151, v151\n"
    "v_add_f32 v148, 1.0, v148\n"
    "v_add_f32 v149, 1.0, v149\n"
    "v_add_f32 v150, 1.0, v150\n"
    "v_add_f32 v151, 1.0, v151\n"
    "v_rcp_f32 v148, v148\n"
    "v_mfma_f32_16x16x32_bf16 v[188:191], v[228:231], v[140:143], v[188:191]\n"
    "ds_read_b64_tr_b16 v[214:215], v249 offset:512\n"
    "v_rcp_f32 v149, v149\n"
    "v_rcp_f32 v150, v150\n"
    "v_rcp_f32 v151, v151\n"
    "v_mul_f32 v164, v164, v148\n"
    "v_mul_f32 v165, v165, v149\n"
    "v_mul_f32 v166, v166, v150\n"
    "v_mul_f32 v167, v167, v151\n"
    "s_waitcnt lgkmcnt(6)\n"
    "v_mfma_f32_16x16x32_bf16 v[192:195], v[232:235], v[136:139], v[192:195]\n"
    "ds_read_b64_tr_b16 v[216:217], v248 offset:8192\n"
    "s_add_u32 m0, m0, 8192\n"
    "s_nop 0\n"
    "buffer_load_dwordx4 v242, s[44:47], s71 offen lds\n"
    "s_add_u32 s71, s71, 589824\n"
    "v_mul_f32 v172, v156, v172\n"
    "v_mul_f32 v173, v157, v173\n"
    "v_mul_f32 v174, v158, v174\n"
    "v_mul_f32 v175, v159, v175\n"
    "v_mul_f32 v156, 0xbfb8aa3b, v156\n"
    "v_mul_f32 v157, 0xbfb8aa3b, v157\n"
    "v_mul_f32 v158, 0xbfb8aa3b, v158\n"
    "v_mfma_f32_16x16x32_bf16 v[196:199], v[232:235], v[140:143], v[196:199]\n"
    "ds_read_b64_tr_b16 v[218:219], v248 offset:8704\n"
    "v_mul_f32 v159, 0xbfb8aa3b, v159\n"
    "v_exp_f32 v156, v156\n"
    "v_exp_f32 v157, v157\n"
    "v_exp_f32 v158, v158\n"
    "v_exp_f32 v159, v159\n"
    "v_add_f32 v156, 1.0, v156\n"
    "v_add_f32 v157, 1.0, v157\n"
    "s_waitcnt lgkmcnt(6)\n"
    "v_mfma_f32_16x16x32_bf16 v[200:203], v[236:239], v[136:139], v[200:203]\n"
    "ds_read_b64_tr_b16 v[220:221], v249 offset:8192\n"
    "v_add_f32 v158, 1.0, v158\n"
    "v_add_f32 v159, 1.0, v159\n"
    "v_rcp_f32 v156, v156\n"
    "v_rcp_f32 v157, v157\n"
    "v_rcp_f32 v158, v158\n"
    "v_rcp_f32 v159, v159\n"
    "v_mul_f32 v172, v172, v156\n"
    "v_mfma_f32_16x16x32_bf16 v[204:207], v[236:239], v[140:143], v[204:207]\n"
    "ds_read_b64_tr_b16 v[222:223], v249 offset:8704\n"
    "s_add_u32 m0, s73, 65536\n"
    "s_nop 0\n"
    "buffer_load_dwordx4 v242, s[40:43], s71 offen lds\n"
    "v_mul_f32 v173, v173, v157\n"
    "v_mul_f32 v174, v174, v158\n"
    "v_mul_f32 v175, v175, v159\n"
    "v_cvt_pk_bf16_f32 v148, v164, v165\n"
    "v_cvt_pk_bf16_f32 v149, v166, v167\n"
    "v_cvt_pk_bf16_f32 v150, v172, v173\n"
    "v_cvt_pk_bf16_f32 v151, v174, v175\n"
    "s_add_u32 m0, m0, 8192\n"
    "s_nop 0\n"
    "buffer_load_dwordx4 v242, s[44:47], s71 offen lds\n"
    "s_add_u32 s71, s71, 589824\n"
    "buffer_store_dwordx4 v[148:151], v252, s[56:59], s75 offen offset:1024 sc0 sc1\n"
    "s_add_u32 s75, s75, 16384\n"
    "s_mov_b32 s97, 81920\n"
    "s_mov_b32 s96, 114688\n"
    "s_mov_b64 s[36:37], %5\n"
    "s_and_b32 s37, s37, 0xffff\n"
    "s_mov_b32 s38, 50331648\n"
    "s_mov_b32 s39, 0x20000\n"
    "s_mul_i32 s69, %13, 27648\n"
    "s_add_u32 s69, s69, 29360128\n"
    "s_lshr_b32 s67, %13, 4\n"
    "s_mul_i32 s67, s67, 1335296\n"
    "s_and_b32 s68, %13, 15\n"
    "s_mul_i32 s68, s68, 9728\n"
    "s_add_u32 s67, s67, s68\n"
    "s_add_u32 s70, s67, 1179648\n"
    "v_mov_b32 v244, -1\n"
    "v_mov_b32 v245, -1\n"
    "v_mov_b32 v246, -1\n"
    "v_mov_b32 v247, -1\n"
    "v_mbcnt_lo_u32_b32 v243, -1, 0\n"
    "v_mbcnt_hi_u32_b32 v243, -1, v243\n"
    "v_lshlrev_b32 v243, 4, v243\n"
    "v_add_u32 v243, s73, v243\n"
    "v_mov_b32 v255, 0x40000000\n"
    "v_cmp_gt_u32 vcc, 3072, v243\n"
    "s_nop 1\n"
    "v_cndmask_b32 v254, v255, v243, vcc\n"
    "v_cmp_gt_u32 vcc, 1536, v243\n"
    "s_nop 1\n"
    "v_cndmask_b32 v255, v255, v243, vcc\n"
    "s_mov_b64 s[48:49], %14\n"
    "s_and_b32 s49, s49, 0xffff\n"
    "s_mov_b32 s50, %16\n"
    "s_mov_b32 s51, 0x20000\n"
    "s_mov_b64 s[52:53], %15\n"
    "s_and_b32 s53, s53, 0xffff\n"
    "s_mov_b32 s54, %18\n"
    "s_mov_b32 s55, 0x20000\n"
    "s_mov_b32 s95, 0\n"
    "s_mov_b32 s99, 589824\n"
    "L1JA%=:\n"
    "s_waitcnt lgkmcnt(6)\n"
    "v_mfma_f32_16x16x32_bf16 v[144:147], v[208:211], v[0:3], 0\n"
    "ds_read_b64_tr_b16 v[224:225], v248 offset:4096\n"
    "s_add_u32 s96, s96, 16384\n"
    "s_cmp_eq_u32 s96, 163840\n"
    "s_cselect_b32 s96, 0, s96\n"
    "v_mfma_f32_16x16x32_bf16 v[148:151], v[208:211], v[4:7], 0\n"
    "ds_read_b64_tr_b16 v[226:227], v248 offset:4608\n"
    "s_waitcnt lgkmcnt(6)\n"
    "v_mfma_f32_16x16x32_bf16 v[152:155], v[212:215], v[0:3], 0\n"
    "ds_read_b64_tr_b16 v[228:229], v249 offset:4096\n"
    "v_mfma_f32_16x16x32_bf16 v[156:159], v[212:215], v[4:7], 0\n"
    "ds_read_b64_tr_b16 v[230:231], v249 offset:4608\n"
    "s_waitcnt lgkmcnt(6)\n"
    "v_mfma_f32_16x16x32_bf16 v[160:163], v[216:219], v[0:3], 0\n"
    "ds_read_b64_tr_b16 v[232:233], v248 offset:12288\n"
    "v_add_u32 v250, s96, v240\n"
    "v_add_u32 v251, s96, v241\n"
    "v_mfma_f32_16x16x32_bf16 v[164:167], v[216:219], v[4:7], 0\n"
    "ds_read_b64_tr_b16 v[234:235], v248 offset:12800\n"
    "s_waitcnt lgkmcnt(6)\n"
    "v_mfma_f32_16x16x32_bf16 v[168:171], v[220:223], v[0:3], 0\n"
    "ds_read_b64_tr_b16 v[236:237], v249 offset:12288\n"
    "v_mfma_f32_16x16x32_bf16 v[172:175], v[220:223], v[4:7], 0\n"
    "ds_read_b64_tr_b16 v[238:239], v249 offset:12800\n"
    "s_waitcnt lgkmcnt(6)\n"
    "v_mfma_f32_16x16x32_bf16 v[144:147], v[224:227], v[8:11], v[144:147]\n"
    "ds_read_b64_tr_b16 v[208:209], v250 offset:0\n"
    "v_mul_f32 v192, v176, v192\n"
    "v_mfma_f32_16x16x32_bf16 v[148:151], v[224:227], v[12:15], v[148:151]\n"
    "ds_read_b64_tr_b16 v[210:211], v250 offset:512\n"
    "v_mul_f32 v193, v177, v193\n"
    "s_waitcnt lgkmcnt(6)\n"
    "v_mfma_f32_16x16x32_bf16 v[152:155], v[228:231], v[8:11], v[152:155]\n"
    "ds_read_b64_tr_b16 v[212:213], v251 offset:0\n"
    "v_mul_f32 v194, v178, v194\n"
    "v_mfma_f32_16x16x32_bf16 v[156:159], v[228:231], v[12:15], v[156:159]\n"
    "ds_read_b64_tr_b16 v[214:215], v251 offset:512\n"
    "v_mul_f32 v195, v179, v195\n"
    "s_waitcnt lgkmcnt(6)\n"
    "v_mfma_f32_16x16x32_bf16 v[160:163], v[232:235], v[8:11], v[160:163]\n"
    "ds_read_b64_tr_b16 v[216:217], v250 offset:8192\n"
    "v_mul_f32 v176, 0xbfb8aa3b, v176\n"
    "v_mfma_f32_16x16x32_bf16 v[164:167], v[232:235], v[12:15], v[164:167]\n"
    "ds_read_b64_tr_b16 v[218:219], v250 offset:8704\n"
    "v_mul_f32 v177, 0xbfb8aa3b, v177\n"
    "s_waitcnt lgkmcnt(6)\n"
    "v_mfma_f32_16x16x32_bf16 v[168:171], v[236:239], v[8:11], v[168:171]\n"
    "ds_read_b64_tr_b16 v[220:221], v251 offset:8192\n"
    "v_mul_f32 v178, 0xbfb8aa3b, v178\n"
    "v_mfma_f32_16x16x32_bf16 v[172:175], v[236:239], v[12:15], v[172:175]\n"
    "ds_read_b64_tr_b16 v[222:223], v251 offset:8704\n"
    "s_waitcnt lgkmcnt(6)\n"
    "v_mfma_f32_16x16x32_bf16 v[144:147], v[208:211], v[16:19], v[144:147]\n"
    "ds_read_b64_tr_b16 v[224:225], v250 offset:4096\n"
    "s_add_u32 s96, s96, 16384\n"
    "s_cmp_eq_u32 s96, 163840\n"
    "s_cselect_b32 s96, 0, s96\n"
    "v_mul_f32 v179, 0xbfb8aa3b, v179\n"
    "v_mfma_f32_16x16x32_bf16 v[148:151], v[208:211], v[20:23], v[148:151]\n"
    "ds_read_b64_tr_b16 v[226:227], v250 offset:4608\n"
    "v_exp_f32 v176, v176\n"
    "s_waitcnt lgkmcnt(6)\n"
    "v_mfma_f32_16x16x32_bf16 v[152:155], v[212:215], v[16:19], v[152:155]\n"
    "ds_read_b64_tr_b16 v[228:229], v251 offset:4096\n"
    "v_exp_f32 v177, v177\n"
    "v_mfma_f32_16x16x32_bf16 v[156:159], v[212:215], v[20:23], v[156:159]\n"
    "ds_read_b64_tr_b16 v[230:231], v251 offset:4608\n"
    "v_exp_f32 v178, v178\n"
    "s_waitcnt lgkmcnt(6)\n"
    "v_mfma_f32_16x16x32_bf16 v[160:163], v[216:219], v[16:19], v[160:163]\n"
    "ds_read_b64_tr_b16 v[232:233], v250 offset:12288\n"
    "v_add_u32 v248, s96, v240\n"
    "v_add_u32 v249, s96, v241\n"
    "v_exp_f32 v179, v179\n"
    "v_mfma_f32_16x16x32_bf16 v[164:167], v[216:219], v[20:23], v[164:167]\n"
    "ds_read_b64_tr_b16 v[234:235], v250 offset:12800\n"
    "v_add_f32 v176, 1.0, v176\n"
    "s_waitcnt lgkmcnt(6)\n"
    "v_mfma_f32_16x16x32_bf16 v[168:171], v[220:223], v[16:19], v[168:171]\n"
    "ds_read_b64_tr_b16 v[236:237], v251 offset:12288\n"
    "v_add_f32 v177, 1.0, v177\n"
    "v_mfma_f32_16x16x32_bf16 v[172:175], v[220:223], v[20:23], v[172:175]\n"
    "ds_read_b64_tr_b16 v[238:239], v251 offset:12800\n"
    "s_waitcnt vmcnt(6)\n"
    "s_barrier\n"
    "s_waitcnt lgkmcnt(6)\n"
    "v_mfma_f32_16x16x32_bf16 v[144:147], v[224:227], v[24:27], v[144:147]\n"
    "ds_read_b64_tr_b16 v[208:209], v248 offset:0\n"
    "v_add_f32 v178, 1.0, v178\n"
    "v_mfma_f32_16x16x32_bf16 v[148:151], v[224:227], v[28:31], v[148:151]\n"
    "ds_read_b64_tr_b16 v[210:211], v248 offset:512\n"
    "s_add_u32 m0, s97, s73\n"
    "s_nop 0\n"
    "buffer_load_dwordx4 v242, s[40:43], s71 offen lds\n"
    "s_add_u32 s68, s69, 0\n"
    "s_waitcnt lgkmcnt(6)\n"
    "v_mfma_f32_16x16x32_bf16 v[152:155], v[228:231], v[24:27], v[152:155]\n"
    "ds_read_b64_tr_b16 v[212:213], v249 offset:0\n"
    "buffer_store_dwordx4 v[244:247], v243, s[36:39], s68 offen sc0 sc1\n"
    "v_mfma_f32_16x16x32_bf16 v[156:159], v[228:231], v[28:31], v[156:159]\n"
    "ds_read_b64_tr_b16 v[214:215], v249 offset:512\n"
    "v_add_f32 v179, 1.0, v179\n"
    "s_waitcnt lgkmcnt(6)\n"
    "v_mfma_f32_16x16x32_bf16 v[160:163], v[232:235], v[24:27], v[160:163]\n"
    "ds_read_b64_tr_b16 v[216:217], v248 offset:8192\n"
    "v_rcp_f32 v176, v176\n"
    "v_mfma_f32_16x16x32_bf16 v[164:167], v[232:235], v[28:31], v[164:167]\n"
    "ds_read_b64_tr_b16 v[218:219], v248 offset:8704\n"
    "s_add_u32 m0, m0, 8192\n"
    "s_nop 0\n"
    "buffer_load_dwordx4 v242, s[44:47], s71 offen lds\n"
    "s_add_u32 s71, s71, s99\n"
    "s_add_u32 s97, s97, 16384\n"
    "s_cmp_eq_u32 s97, 163840\n"
    "s_cselect_b32 s97, 0, s97\n"
    "v_rcp_f32 v177, v177\n"
    "s_waitcnt lgkmcnt(6)\n"
    "v_mfma_f32_16x16x32_bf16 v[168:171], v[236:239], v[24:27], v[168:171]\n"
    "ds_read_b64_tr_b16 v[220:221], v249 offset:8192\n"
    "v_rcp_f32 v178, v178\n"
    "v_mfma_f32_16x16x32_bf16 v[172:175], v[236:239], v[28:31], v[172:175]\n"
    "ds_read_b64_tr_b16 v[222:223], v249 offset:8704\n"
    "s_waitcnt lgkmcnt(6)\n"
    "v_mfma_f32_16x16x32_bf16 v[144:147], v[208:211], v[32:35], v[144:147]\n"
    "ds_read_b64_tr_b16 v[224:225], v248 offset:4096\n"
    "s_add_u32 s96, s96, 16384\n"
    "s_cmp_eq_u32 s96, 163840\n"
    "s_cselect_b32 s96, 0, s96\n"
    "v_rcp_f32 v179, v179\n"
    "v_mfma_f32_16x16x32_bf16 v[148:151], v[208:211], v[36:39], v[148:151]\n"
    "ds_read_b64_tr_b16 v[226:227], v248 offset:4608\n"
    "s_add_u32 s78, s78, 128\n"
    "s_sub_u32 s76, s76, 1\n"
    "s_cmp_gt_i32 s76, s98\n"
    "s_cbranch_scc1 L1KEEP1%=\n"
    "s_cmp_eq_u32 s76, 2\n"
    "s_cbranch_scc0 L1SIDE1%=\n"
    "s_mov_b64 s[40:41], %4\n"
    "s_and_b32 s41, s41, 0xffff\n"
    "s_mov_b32 s42, 73728\n"
    "s_mov_b64 s[44:45], %6\n"
    "s_and_b32 s45, s45, 0xffff\n"
    "s_mov_b32 s46, 73728\n"
    "s_mov_b32 s99, 8192\n"
    "s_branch L1LIN1%=\n"
    "L1SIDE1%=:\n"
    "s_mov_b32 s40, s48\n"
    "s_mov_b32 s41, s49\n"
    "s_mov_b32 s42, s50\n"
    "s_mov_b32 s43, s51\n"
    "s_mov_b32 s44, s52\n"
    "s_mov_b32 s45, s53\n"
    "s_mov_b32 s46, s54\n"
    "s_mov_b32 s47, s55\n"
    "s_mov_b32 s99, %17\n"
    "s_mov_b32 s95, s97\n"
    "L1LIN1%=:\n"
    "v_mov_b32 v242, v243\n"
    "s_mov_b32 s78, 0\n"
    "L1KEEP1%=:\n"
    "s_mov_b32 s71, s78\n"
    "s_nop 3\n"
    "s_add_u32 m0, s97, s73\n"
    "s_nop 0\n"
    "buffer_load_dwordx4 v242, s[40:43], s71 offen lds\n"
    "v_mul_f32 v192, v192, v176\n"
    "s_waitcnt lgkmcnt(6)\n"
    "v_mfma_f32_16x16x32_bf16 v[152:155], v[212:215], v[32:35], v[152:155]\n"
    "ds_read_b64_tr_b16 v[228:229], v249 offset:4096\n"
    "v_mul_f32 v193, v193, v177\n"
    "v_mfma_f32_16x16x32_bf16 v[156:159], v[212:215], v[36:39], v[156:159]\n"
    "ds_read_b64_tr_b16 v[230:231], v249 offset:4608\n"
    "v_mul_f32 v194, v194, v178\n"
    "s_waitcnt lgkmcnt(6)\n"
    "v_mfma_f32_16x16x32_bf16 v[160:163], v[216:219], v[32:35], v[160:163]\n"
    "ds_read_b64_tr_b16 v[232:233], v248 offset:12288\n"
    "v_add_u32 v250, s96, v240\n"
    "v_add_u32 v251, s96, v241\n"
    "v_mul_f32 v195, v195, v179\n"
    "v_mfma_f32_16x16x32_bf16 v[164:167], v[216:219], v[36:39], v[164:167]\n"
    "ds_read_b64_tr_b16 v[234:235], v248 offset:12800\n"
    "s_add_u32 m0, m0, 8192\n"
    "s_nop 0\n"
    "buffer_load_dwordx4 v242, s[44:47], s71 offen lds\n"
    "s_add_u32 s71, s71, s99\n"
    "s_add_u32 s97, s97, 16384\n"
    "s_cmp_eq_u32 s97, 163840\n"
    "s_cselect_b32 s97, 0, s97\n"
    "v_mul_f32 v200, v184, v200\n"
    "s_waitcnt lgkmcnt(6)\n"
    "v_mfma_f32_16x16x32_bf16 v[168:171], v[220:223], v[32:35], v[168:171]\n"
    "ds_read_b64_tr_b16 v[236:237], v249 offset:12288\n"
    "v_mul_f32 v201, v185, v201\n"
    "v_mfma_f32_16x16x32_bf16 v[172:175], v[220:223], v[36:39], v[172:175]\n"
    "ds_read_b64_tr_b16 v[238:239], v249 offset:12800\n"
    "s_waitcnt lgkmcnt(6)\n"
    "v_mfma_f32_16x16x32_bf16 v[144:147], v[224:227], v[40:43], v[144:147]\n"
    "ds_read_b64_tr_b16 v[208:209], v250 offset:0\n"
    "v_mul_f32 v202, v186, v202\n"
    "v_mfma_f32_16x16x32_bf16 v[148:151], v[224:227], v[44:47], v[148:151]\n"
    "ds_read_b64_tr_b16 v[210:211], v250 offset:512\n"
    "s_add_u32 m0, s97, s73\n"
    "s_nop 0\n"
    "buffer_load_dwordx4 v242, s[40:43], s71 offen lds\n"
    "v_mul_f32 v203, v187, v203\n"
    "s_waitcnt lgkmcnt(6)\n"
    "v_mfma_f32_16x16x32_bf16 v[152:155], v[228:231], v[40:43], v[152:155]\n"
    "ds_read_b64_tr_b16 v[212:213], v251 offset:0\n"
    "v_mul_f32 v184, 0xbfb8aa3b, v184\n"
    "v_mfma_f32_16x16x32_bf16 v[156:159], v[228:231], v[44:47], v[156:159]\n"
    "ds_read_b64_tr_b16 v[214:215], v251 offset:512\n"
    "v_mul_f32 v185, 0xbfb8aa3b, v185\n"
    "s_waitcnt lgkmcnt(6)\n"
    "v_mfma_f32_16x16x32_bf16 v[160:163], v[232:235], v[40:43], v[160:163]\n"
    "ds_read_b64_tr_b16 v[216:217], v250 offset:8192\n"
    "s_add_u32 s68, s69, 8192\n"
    "v_mfma_f32_16x16x32_bf16 v[164:167], v[232:235], v[44:47], v[164:167]\n"
    "ds_read_b64_tr_b16 v[218:219], v250 offset:8704\n"
    "s_add_u32 m0, m0, 8192\n"
    "s_nop 0\n"
    "buffer_load_dwordx4 v242, s[44:47], s71 offen lds\n"
    "s_add_u32 s71, s71, s99\n"
    "s_add_u32 s97, s97, 16384\n"
    "s_cmp_eq_u32 s97, 163840\n"
    "s_cselect_b32 s97, 0, s97\n"
    "buffer_store_dwordx4 v[244:247], v243, s[36:39], s68 offen sc0 sc1\n"
    "s_waitcnt lgkmcnt(6)\n"
    "v_mfma_f32_16x16x32_bf16 v[168:171], v[236:239], v[40:43], v[168:171]\n"
    "ds_read_b64_tr_b16 v[220:221], v251 offset:8192\n"
    "v_mul_f32 v186, 0xbfb8aa3b, v186\n"
    "v_mfma_f32_16x16x32_bf16 v[172:175], v[236:239], v[44:47], v[172:175]\n"
    "ds_read_b64_tr_b16 v[222:223], v251 offset:8704\n"
    "s_waitcnt lgkmcnt(6)\n"
    "v_mfma_f32_16x16x32_bf16 v[144:147], v[208:211], v[48:51], v[144:147]\n"
    "ds_read_b64_tr_b16 v[224:225], v250 offset:4096\n"
    "s_add_u32 s96, s96, 16384\n"
    "s_cmp_eq_u32 s96, 163840\n"
    "s_cselect_b32 s96, 0, s96\n"
    "v_mul_f32 v187, 0xbfb8aa3b, v187\n"
    "v_mfma_f32_16x16x32_bf16 v[148:151], v[208:211], v[52:55], v[148:151]\n"
    "ds_read_b64_tr_b16 v[226:227], v250 offset:4608\n"
    "v_exp_f32 v184, v184\n"
    "s_waitcnt lgkmcnt(6)\n"
    "v_mfma_f32_16x16x32_bf16 v[152:155], v[212:215], v[48:51], v[152:155]\n"
    "ds_read_b64_tr_b16 v[228:229], v251 offset:4096\n"
    "v_exp_f32 v185, v185\n"
    "v_mfma_f32_16x16x32_bf16 v[156:159], v[212:215], v[52:55], v[156:159]\n"
    "ds_read_b64_tr_b16 v[230:231], v251 offset:4608\n"
    "v_exp_f32 v186, v186\n"
    "s_waitcnt lgkmcnt(6)\n"
    "v_mfma_f32_16x16x32_bf16 v[160:163], v[216:219], v[48:51], v[160:163]\n"
    "ds_read_b64_tr_b16 v[232:233], v250 offset:12288\n"
    "v_add_u32 v248, s96, v240\n"
    "v_add_u32 v249, s96, v241\n"
    "v_exp_f32 v187, v187\n"
    "v_mfma_f32_16x16x32_bf16 v[164:167], v[216:219], v[52:55], v[164:167]\n"
    "ds_read_b64_tr_b16 v[234:235], v250 offset:12800\n"
    "v_add_f32 v184, 1.0, v184\n"
    "s_waitcnt lgkmcnt(6)\n"
    "v_mfma_f32_16x16x32_bf16 v[168:171], v[220:223], v[48:51], v[168:171]\n"
    "ds_read_b64_tr_b16 v[236:237], v251 offset:12288\n"
    "v_add_f32 v185, 1.0, v185\n"
    "v_mfma_f32_16x16x32_bf16 v[172:175], v[220:223], v[52:55], v[172:175]\n"
    "ds_read_b64_tr_b16 v[238:239], v251 offset:12800\n"
    "s_waitcnt lgkmcnt(6)\n"
    "v_mfma_f32_16x16x32_bf16 v[144:147], v[224:227], v[56:59], v[144:147]\n"
    "ds_read_b64_tr_b16 v[208:209], v248 offset:0\n"
    "v_add_f32 v186, 1.0, v186\n"
    "v_mfma_f32_16x16x32_bf16 v[148:151], v[224:227], v[60:63], v[148:151]\n"
    "ds_read_b64_tr_b16 v[210:211], v248 offset:512\n"
    "v_add_f32 v187, 1.0, v187\n"
    "s_waitcnt lgkmcnt(6)\n"
    "v_mfma_f32_16x16x32_bf16 v[152:155], v[228:231], v[56:59], v[152:155]\n"
    "ds_read_b64_tr_b16 v[212:213], v249 offset:0\n"
    "v_rcp_f32 v184, v184\n"
    "v_mfma_f32_16x16x32_bf16 v[156:159], v[228:231], v[60:63], v[156:159]\n"
    "ds_read_b64_tr_b16 v[214:215], v249 offset:512\n"
    "v_rcp_f32 v185, v185\n"
    "s_waitcnt lgkmcnt(6)\n"
    "v_mfma_f32_16x16x32_bf16 v[160:163], v[232:235], v[56:59], v[160:163]\n"
    "ds_read_b64_tr_b16 v[216:217], v248 offset:8192\n"
    "v_rcp_f32 v186, v186\n"
    "v_mfma_f32_16x16x32_bf16 v[164:167], v[232:235], v[60:63], v[164:167]\n"
    "ds_read_b64_tr_b16 v[218:219], v248 offset:8704\n"
    "v_rcp_f32 v187, v187\n"
    "s_waitcnt lgkmcnt(6)\n"
    "v_mfma_f32_16x16x32_bf16 v[168:171], v[236:239], v[56:59], v[168:171]\n"
    "ds_read_b64_tr_b16 v[220:221], v249 offset:8192\n"
    "v_mul_f32 v200, v200, v184\n"
    "v_mfma_f32_16x16x32_bf16 v[172:175], v[236:239], v[60:63], v[172:175]\n"
    "ds_read_b64_tr_b16 v[222:223], v249 offset:8704\n"
    "s_waitcnt lgkmcnt(6)\n"
    "v_mfma_f32_16x16x32_bf16 v[144:147], v[208:211], v[64:67], v[144:147]\n"
    "ds_read_b64_tr_b16 v[224:225], v248 offset:4096\n"
    "s_add_u32 s96, s96, 16384\n"
    "s_cmp_eq_u32 s96, 163840\n"
    "s_cselect_b32 s96, 0, s96\n"
    "s_add_u32 s68, s69, 16384\n"
    "v_mfma_f32_16x16x32_bf16 v[148:151], v[208:211], v[68:71], v[148:151]\n"
    "ds_read_b64_tr_b16 v[226:227], v248 offset:4608\n"
    "buffer_store_dwordx4 v[244:247], v243, s[36:39], s68 offen sc0 sc1\n"
    "s_waitcnt lgkmcnt(6)\n"
    "v_mfma_f32_16x16x32_bf16 v[152:155], v[212:215], v[64:67], v[152:155]\n"
    "ds_read_b64_tr_b16 v[228:229], v249 offset:4096\n"
    "v_mul_f32 v201, v201, v185\n"
    "v_mfma_f32_16x16x32_bf16 v[156:159], v[212:215], v[68:71], v[156:159]\n"
    "ds_read_b64_tr_b16 v[230:231], v249 offset:4608\n"
    "v_mul_f32 v202, v202, v186\n"
    "s_waitcnt lgkmcnt(6)\n"
    "v_mfma_f32_16x16x32_bf16 v[160:163], v[216:219], v[64:67], v[160:163]\n"
    "ds_read_b64_tr_b16 v[232:233], v248 offset:12288\n"
    "v_add_u32 v250, s96, v240\n"
    "v_add_u32 v251, s96, v241\n"
    "v_mul_f32 v203, v203, v187\n"
    "v_mfma_f32_16x16x32_bf16 v[164:167], v[216:219], v[68:71], v[164:167]\n"
    "ds_read_b64_tr_b16 v[234:235], v248 offset:12800\n"
    "v_cvt_pk_bf16_f32 v176, v192, v193\n"
    "s_waitcnt lgkmcnt(6)\n"
    "v_mfma_f32_16x16x32_bf16 v[168:171], v[220:223], v[64:67], v[168:171]\n"
    "ds_read_b64_tr_b16 v[236:237], v249 offset:12288\n"
    "v_cvt_pk_bf16_f32 v177, v194, v195\n"
    "v_mfma_f32_16x16x32_bf16 v[172:175], v[220:223], v[68:71], v[172:175]\n"
    "ds_read_b64_tr_b16 v[238:239], v249 offset:12800\n"
    "s_waitcnt vmcnt(9)\n"
    "s_barrier\n"
    "s_waitcnt lgkmcnt(6)\n"
    "v_mfma_f32_16x16x32_bf16 v[144:147], v[224:227], v[72:75], v[144:147]\n"
    "ds_read_b64_tr_b16 v[208:209], v250 offset:0\n"
    "v_cvt_pk_bf16_f32 v178, v200, v201\n"
    "v_mfma_f32_16x16x32_bf16 v[148:151], v[224:227], v[76:79], v[148:151]\n"
    "ds_read_b64_tr_b16 v[210:211], v250 offset:512\n"
    "s_add_u32 m0, s97, s73\n"
    "s_nop 0\n"
    "buffer_load_dwordx4 v242, s[40:43], s71 offen lds\n"
    "v_cvt_pk_bf16_f32 v179, v202, v203\n"
    "s_waitcnt lgkmcnt(6)\n"
    "v_mfma_f32_16x16x32_bf16 v[152:155], v[228:231], v[72:75], v[152:155]\n"
    "ds_read_b64_tr_b16 v[212:213], v251 offset:0\n"
    "buffer_store_dwordx4 v[176:179], v252, s[56:59], s75 offen offset:0 sc0 sc1\n"
    "v_mfma_f32_16x16x32_bf16 v[156:159], v[228:231], v[76:79], v[156:159]\n"
    "ds_read_b64_tr_b16 v[214:215], v251 offset:512\n"
    "v_mul_f32 v196, v180, v196\n"
    "s_waitcnt lgkmcnt(6)\n"
    "v_mfma_f32_16x16x32_bf16 v[160:163], v[232:235], v[72:75], v[160:163]\n"
    "ds_read_b64_tr_b16 v[216:217], v250 offset:8192\n"
    "v_mul_f32 v197, v181, v197\n"
    "v_mfma_f32_16x16x32_bf16 v[164:167], v[232:235], v[76:79], v[164:167]\n"
    "ds_read_b64_tr_b16 v[218:219], v250 offset:8704\n"
    "s_add_u32 m0, m0, 8192\n"
    "s_nop 0\n"
    "buffer_load_dwordx4 v242, s[44:47], s71 offen lds\n"
    "s_add_u32 s71, s71, s99\n"
    "s_add_u32 s97, s97, 16384\n"
    "s_cmp_eq_u32 s97, 163840\n"
    "s_cselect_b32 s97, 0, s97\n"
    "v_mul_f32 v198, v182, v198\n"
    "s_waitcnt lgkmcnt(6)\n"
    "v_mfma_f32_16x16x32_bf16 v[168:171], v[236:239], v[72:75], v[168:171]\n"
    "ds_read_b64_tr_b16 v[220:221], v251 offset:8192\n"
    "v_mul_f32 v199, v183, v199\n"
    "v_mfma_f32_16x16x32_bf16 v[172:175], v[236:239], v[76:79], v[172:175]\n"
    "ds_read_b64_tr_b16 v[222:223], v251 offset:8704\n"
    "s_waitcnt lgkmcnt(6)\n"
    "v_mfma_f32_16x16x32_bf16 v[144:147], v[208:211], v[80:83], v[144:147]\n"
    "ds_read_b64_tr_b16 v[224:225], v250 offset:4096\n"
    "s_add_u32 s96, s96, 16384\n"
    "s_cmp_eq_u32 s96, 163840\n"
    "s_cselect_b32 s96, 0, s96\n"
    "v_mul_f32 v180, 0xbfb8aa3b, v180\n"
    "v_mfma_f32_16x16x32_bf16 v[148:151], v[208:211], v[84:87], v[148:151]\n"
    "ds_read_b64_tr_b16 v[226:227], v250 offset:4608\n"
    "s_add_u32 m0, s97, s73\n"
    "s_nop 0\n"
    "buffer_load_dwordx4 v242, s[40:43], s71 offen lds\n"
    "v_mul_f32 v181, 0xbfb8aa3b, v181\n"
    "s_waitcnt lgkmcnt(6)\n"
    "v_mfma_f32_16x16x32_bf16 v[152:155], v[212:215], v[80:83], v[152:155]\n"
    "ds_read_b64_tr_b16 v[228:229], v251 offset:4096\n"
    "v_mul_f32 v182, 0xbfb8aa3b, v182\n"
    "v_mfma_f32_16x16x32_bf16 v[156:159], v[212:215], v[84:87], v[156:159]\n"
    "ds_read_b64_tr_b16 v[230:231], v251 offset:4608\n"
    "s_add_u32 s68, s69, 24576\n"
    "s_waitcnt lgkmcnt(6)\n"
    "v_mfma_f32_16x16x32_bf16 v[160:163], v[216:219], v[80:83], v[160:163]\n"
    "ds_read_b64_tr_b16 v[232:233], v250 offset:12288\n"
    "v_add_u32 v248, s96, v240\n"
    "v_add_u32 v249, s96, v241\n"
    "buffer_store_dwordx4 v[244:247], v254, s[36:39], s68 offen sc0 sc1\n"
    "v_mfma_f32_16x16x32_bf16 v[164:167], v[216:219], v[84:87], v[164:167]\n"
    "ds_read_b64_tr_b16 v[234:235], v250 offset:12800\n"
    "s_add_u32 m0, m0, 8192\n"
    "s_nop 0\n"
    "buffer_load_dwordx4 v242, s[44:47], s71 offen lds\n"
    "s_add_u32 s71, s71, s99\n"
    "s_add_u32 s97, s97, 16384\n"
    "s_cmp_eq_u32 s97, 163840\n"
    "s_cselect_b32 s97, 0, s97\n"
    "v_mul_f32 v183, 0xbfb8aa3b, v183\n"
    "s_waitcnt lgkmcnt(6)\n"
    "v_mfma_f32_16x16x32_bf16 v[168:171], v[220:223], v[80:83], v[168:171]\n"
    "ds_read_b64_tr_b16 v[236:237], v251 offset:12288\n"
    "v_exp_f32 v180, v180\n"
    "v_mfma_f32_16x16x32_bf16 v[172:175], v[220:223], v[84:87], v[172:175]\n"
    "ds_read_b64_tr_b16 v[238:239], v251 offset:12800\n"
    "s_waitcnt lgkmcnt(6)\n"
    "v_mfma_f32_16x16x32_bf16 v[144:147], v[224:227], v[88:91], v[144:147]\n"
    "ds_read_b64_tr_b16 v[208:209], v248 offset:0\n"
    "v_exp_f32 v181, v181\n"
    "v_mfma_f32_16x16x32_bf16 v[148:151], v[224:227], v[92:95], v[148:151]\n"
    "ds_read_b64_tr_b16 v[210:211], v248 offset:512\n"
    "s_add_u32 m0, s97, s73\n"
    "s_nop 0\n"
    "buffer_load_dwordx4 v242, s[40:43], s71 offen lds\n"
    "v_exp_f32 v182, v182\n"
    "s_waitcnt lgkmcnt(6)\n"
    "v_mfma_f32_16x16x32_bf16 v[152:155], v[228:231], v[88:91], v[152:155]\n"
    "ds_read_b64_tr_b16 v[212:213], v249 offset:0\n"
    "v_exp_f32 v183, v183\n"
    "v_mfma_f32_16x16x32_bf16 v[156:159], v[228:231], v[92:95], v[156:159]\n"
    "ds_read_b64_tr_b16 v[214:215], v249 offset:512\n"
    "v_add_f32 v180, 1.0, v180\n"
    "s_waitcnt lgkmcnt(6)\n"
    "v_mfma_f32_16x16x32_bf16 v[160:163], v[232:235], v[88:91], v[160:163]\n"
    "ds_read_b64_tr_b16 v[216:217], v248 offset:8192\n"
    "v_add_f32 v181, 1.0, v181\n"
    "v_mfma_f32_16x16x32_bf16 v[164:167], v[232:235], v[92:95], v[164:167]\n"
    "ds_read_b64_tr_b16 v[218:219], v248 offset:8704\n"
    "s_add_u32 m0, m0, 8192\n"
    "s_nop 0\n"
    "buffer_load_dwordx4 v242, s[44:47], s71 offen lds\n"
    "s_add_u32 s71, s71, s99\n"
    "s_add_u32 s97, s97, 16384\n"
    "s_cmp_eq_u32 s97, 163840\n"
    "s_cselect_b32 s97, 0, s97\n"
    "v_add_f32 v182, 1.0, v182\n"
    "s_waitcnt lgkmcnt(6)\n"
    "v_mfma_f32_16x16x32_bf16 v[168:171], v[236:239], v[88:91], v[168:171]\n"
    "ds_read_b64_tr_b16 v[220:221], v249 offset:8192\n"
    "v_add_f32 v183, 1.0, v183\n"
    "v_mfma_f32_16x16x32_bf16 v[172:175], v[236:239], v[92:95], v[172:175]\n"
    "ds_read_b64_tr_b16 v[222:223], v249 offset:8704\n"
    "s_waitcnt lgkmcnt(6)\n"
    "v_mfma_f32_16x16x32_bf16 v[144:147], v[208:211], v[96:99], v[144:147]\n"
    "ds_read_b64_tr_b16 v[224:225], v248 offset:4096\n"
    "s_add_u32 s96, s96, 16384\n"
    "s_cmp_eq_u32 s96, 163840\n"
    "s_cselect_b32 s96, 0, s96\n"
    "v_rcp_f32 v180, v180\n"
    "v_mfma_f32_16x16x32_bf16 v[148:151], v[208:211], v[100:103], v[148:151]\n"
    "ds_read_b64_tr_b16 v[226:227], v248 offset:4608\n"
    "v_rcp_f32 v181, v181\n"
    "s_waitcnt lgkmcnt(6)\n"
    "v_mfma_f32_16x16x32_bf16 v[152:155], v[212:215], v[96:99], v[152:155]\n"
    "ds_read_b64_tr_b16 v[228:229], v249 offset:4096\n"
    "v_rcp_f32 v182, v182\n"
    "v_mfma_f32_16x16x32_bf16 v[156:159], v[212:215], v[100:103], v[156:159]\n"
    "ds_read_b64_tr_b16 v[230:231], v249 offset:4608\n"
    "v_rcp_f32 v183, v183\n"
    "s_waitcnt lgkmcnt(6)\n"
    "v_mfma_f32_16x16x32_bf16 v[160:163], v[216:219], v[96:99], v[160:163]\n"
    "ds_read_b64_tr_b16 v[232:233], v248 offset:12288\n"
    "v_add_u32 v250, s96, v240\n"
    "v_add_u32 v251, s96, v241\n"
    "v_mul_f32 v196, v196, v180\n"
    "v_mfma_f32_16x16x32_bf16 v[164:167], v[216:219], v[100:103], v[164:167]\n"
    "ds_read_b64_tr_b16 v[234:235], v248 offset:12800\n"
    "v_mul_f32 v197, v197, v181\n"
    "s_waitcnt lgkmcnt(6)\n"
    "v_mfma_f32_16x16x32_bf16 v[168:171], v[220:223], v[96:99], v[168:171]\n"
    "ds_read_b64_tr_b16 v[236:237], v249 offset:12288\n"
    "s_add_u32 s68, s70, 0\n"
    "v_mfma_f32_16x16x32_bf16 v[172:175], v[220:223], v[100:103], v[172:175]\n"
    "ds_read_b64_tr_b16 v[238:239], v249 offset:12800\n"
    "s_waitcnt lgkmcnt(6)\n"
    "v_mfma_f32_16x16x32_bf16 v[144:147], v[224:227], v[104:107], v[144:147]\n"
    "ds_read_b64_tr_b16 v[208:209], v250 offset:0\n"
    "buffer_store_dwordx4 v[244:247], v243, s[36:39], s68 offen sc0 sc1\n"
    "v_mfma_f32_16x16x32_bf16 v[148:151], v[224:227], v[108:111], v[148:151]\n"
    "ds_read_b64_tr_b16 v[210:211], v250 offset:512\n"
    "v_mul_f32 v198, v198, v182\n"
    "s_waitcnt lgkmcnt(6)\n"
    "v_mfma_f32_16x16x32_bf16 v[152:155], v[228:231], v[104:107], v[152:155]\n"
    "ds_read_b64_tr_b16 v[212:213], v251 offset:0\n"
    "v_mul_f32 v199, v199, v183\n"
    "v_mfma_f32_16x16x32_bf16 v[156:159], v[228:231], v[108:111], v[156:159]\n"
    "ds_read_b64_tr_b16 v[214:215], v251 offset:512\n"
    "v_mul_f32 v204, v188, v204\n"
    "s_waitcnt lgkmcnt(6)\n"
    "v_mfma_f32_16x16x32_bf16 v[160:163], v[232:235], v[104:107], v[160:163]\n"
    "ds_read_b64_tr_b16 v[216:217], v250 offset:8192\n"
    "v_mul_f32 v205, v189, v205\n"
    "v_mfma_f32_16x16x32_bf16 v[164:167], v[232:235], v[108:111], v[164:167]\n"
    "ds_read_b64_tr_b16 v[218:219], v250 offset:8704\n"
    "v_mul_f32 v206, v190, v206\n"
    "s_waitcnt lgkmcnt(6)\n"
    "v_mfma_f32_16x16x32_bf16 v[168:171], v[236:239], v[104:107], v[168:171]\n"
    "ds_read_b64_tr_b16 v[220:221], v251 offset:8192\n"
    "v_mul_f32 v207, v191, v207\n"
    "v_mfma_f32_16x16x32_bf16 v[172:175], v[236:239], v[108:111], v[172:175]\n"
    "ds_read_b64_tr_b16 v[222:223], v251 offset:8704\n"
    "s_waitcnt lgkmcnt(6)\n"
    "v_mfma_f32_16x16x32_bf16 v[144:147], v[208:211], v[112:115], v[144:147]\n"
    "ds_read_b64_tr_b16 v[224:225], v250 offset:4096\n"
    "s_add_u32 s96, s96, 16384\n"
    "s_cmp_eq_u32 s96, 163840\n"
    "s_cselect_b32 s96, 0, s96\n"
    "v_mul_f32 v188, 0xbfb8aa3b, v188\n"
    "v_mfma_f32_16x16x32_bf16 v[148:151], v[208:211], v[116:119], v[148:151]\n"
    "ds_read_b64_tr_b16 v[226:227], v250 offset:4608\n"
    "v_mul_f32 v189, 0xbfb8aa3b, v189\n"
    "s_waitcnt lgkmcnt(6)\n"
    "v_mfma_f32_16x16x32_bf16 v[152:155], v[212:215], v[112:115], v[152:155]\n"
    "ds_read_b64_tr_b16 v[228:229], v251 offset:4096\n"
    "v_mul_f32 v190, 0xbfb8aa3b, v190\n"
    "v_mfma_f32_16x16x32_bf16 v[156:159], v[212:215], v[116:119], v[156:159]\n"
    "ds_read_b64_tr_b16 v[230:231], v251 offset:4608\n"
    "v_mul_f32 v191, 0xbfb8aa3b, v191\n"
    "s_waitcnt lgkmcnt(6)\n"
    "v_mfma_f32_16x16x32_bf16 v[160:163], v[216:219], v[112:115], v[160:163]\n"
    "ds_read_b64_tr_b16 v[232:233], v250 offset:12288\n"
    "v_add_u32 v248, s96, v240\n"
    "v_add_u32 v249, s96, v241\n"
    "v_exp_f32 v188, v188\n"
    "v_mfma_f32_16x16x32_bf16 v[164:167], v[216:219], v[116:119], v[164:167]\n"
    "ds_read_b64_tr_b16 v[234:235], v250 offset:12800\n"
    "v_exp_f32 v189, v189\n"
    "s_waitcnt lgkmcnt(6)\n"
    "v_mfma_f32_16x16x32_bf16 v[168:171], v[220:223], v[112:115], v[168:171]\n"
    "ds_read_b64_tr_b16 v[236:237], v251 offset:12288\n"
    "v_exp_f32 v190, v190\n"
    "v_mfma_f32_16x16x32_bf16 v[172:175], v[220:223], v[116:119], v[172:175]\n"
    "ds_read_b64_tr_b16 v[238:239], v251 offset:12800\n"
    "s_waitcnt vmcnt(11)\n"
    "s_barrier\n"
    "s_waitcnt lgkmcnt(6)\n"
    "v_mfma_f32_16x16x32_bf16 v[144:147], v[224:227], v[120:123], v[144:147]\n"
    "ds_read_b64_tr_b16 v[208:209], v248 offset:0\n"
    "v_exp_f32 v191, v191\n"
    "v_mfma_f32_16x16x32_bf16 v[148:151], v[224:227], v[124:127], v[148:151]\n"
    "ds_read_b64_tr_b16 v[210:211], v248 offset:512\n"
    "s_add_u32 m0, s97, s73\n"
    "s_nop 0\n"
    "buffer_load_dwordx4 v242, s[40:43], s71 offen lds\n"
    "v_add_f32 v188, 1.0, v188\n"
    "s_waitcnt lgkmcnt(6)\n"
    "v_mfma_f32_16x16x32_bf16 v[152:155], v[228:231], v[120:123], v[152:155]\n"
    "ds_read_b64_tr_b16 v[212:213], v249 offset:0\n"
    "s_add_u32 s68, s70, 8192\n"
    "v_mfma_f32_16x16x32_bf16 v[156:159], v[228:231], v[124:127], v[156:159]\n"
    "ds_read_b64_tr_b16 v[214:215], v249 offset:512\n"
    "buffer_store_dwordx4 v[244:247], v255, s[36:39], s68 offen sc0 sc1\n"
    "s_waitcnt lgkmcnt(6)\n"
    "v_mfma_f32_16x16x32_bf16 v[160:163], v[232:235], v[120:123], v[160:163]\n"
    "ds_read_b64_tr_b16 v[216:217], v248 offset:8192\n"
    "v_add_f32 v189, 1.0, v189\n"
    "v_mfma_f32_16x16x32_bf16 v[164:167], v[232:235], v[124:127], v[164:167]\n"
    "ds_read_b64_tr_b16 v[218:219], v248 offset:8704\n"
    "s_add_u32 m0, m0, 8192\n"
    "s_nop 0\n"
    "buffer_load_dwordx4 v242, s[44:47], s71 offen lds\n"
    "s_add_u32 s71, s71, s99\n"
    "s_add_u32 s97, s97, 16384\n"
    "s_cmp_eq_u32 s97, 163840\n"
    "s_cselect_b32 s97, 0, s97\n"
    "v_add_f32 v190, 1.0, v190\n"
    "s_waitcnt lgkmcnt(6)\n"
    "v_mfma_f32_16x16x32_bf16 v[168:171], v[236:239], v[120:123], v[168:171]\n"
    "ds_read_b64_tr_b16 v[220:221], v249 offset:8192\n"
    "v_add_f32 v191, 1.0, v191\n"
    "v_mfma_f32_16x16x32_bf16 v[172:175], v[236:239], v[124:127], v[172:175]\n"
    "ds_read_b64_tr_b16 v[222:223], v249 offset:8704\n"
    "s_waitcnt lgkmcnt(6)\n"
    "v_mfma_f32_16x16x32_bf16 v[144:147], v[208:211], v[128:131], v[144:147]\n"
    "ds_read_b64_tr_b16 v[224:225], v248 offset:4096\n"
    "s_add_u32 s96, s96, 16384\n"
    "s_cmp_eq_u32 s96, 163840\n"
    "s_cselect_b32 s96, 0, s96\n"
    "v_rcp_f32 v188, v188\n"
    "v_mfma_f32_16x16x32_bf16 v[148:151], v[208:211], v[132:135], v[148:151]\n"
    "ds_read_b64_tr_b16 v[226:227], v248 offset:4608\n"
    "s_add_u32 m0, s97, s73\n"
    "s_nop 0\n"
    "buffer_load_dwordx4 v242, s[40:43], s71 offen lds\n"
    "v_rcp_f32 v189, v189\n"
    "s_waitcnt lgkmcnt(6)\n"
    "v_mfma_f32_16x16x32_bf16 v[152:155], v[212:215], v[128:131], v[152:155]\n"
    "ds_read_b64_tr_b16 v[228:229], v249 offset:4096\n"
    "v_rcp_f32 v190, v190\n"
    "v_mfma_f32_16x16x32_bf16 v[156:159], v[212:215], v[132:135], v[156:159]\n"
    "ds_read_b64_tr_b16 v[230:231], v249 offset:4608\n"
    "v_rcp_f32 v191, v191\n"
    "s_waitcnt lgkmcnt(6)\n"
    "v_mfma_f32_16x16x32_bf16 v[160:163], v[216:219], v[128:131], v[160:163]\n"
    "ds_read_b64_tr_b16 v[232:233], v248 offset:12288\n"
    "v_add_u32 v250, s96, v240\n"
    "v_add_u32 v251, s96, v241\n"
    "v_mul_f32 v204, v204, v188\n"
    "v_mfma_f32_16x16x32_bf16 v[164:167], v[216:219], v[132:135], v[164:167]\n"
    "ds_read_b64_tr_b16 v[234:235], v248 offset:12800\n"
    "s_add_u32 m0, m0, 8192\n"
    "s_nop 0\n"
    "buffer_load_dwordx4 v242, s[44:47], s71 offen lds\n"
    "s_add_u32 s71, s71, s99\n"
    "s_add_u32 s97, s97, 16384\n"
    "s_cmp_eq_u32 s97, 163840\n"
    "s_cselect_b32 s97, 0, s97\n"
    "v_mul_f32 v205, v205, v189\n"
    "s_waitcnt lgkmcnt(6)\n"
    "v_mfma_f32_16x16x32_bf16 v[168:171], v[220:223], v[128:131], v[168:171]\n"
    "ds_read_b64_tr_b16 v[236:237], v249 offset:12288\n"
    "v_mul_f32 v206, v206, v190\n"
    "v_mfma_f32_16x16x32_bf16 v[172:175], v[220:223], v[132:135], v[172:175]\n"
    "ds_read_b64_tr_b16 v[238:239], v249 offset:12800\n"
    "s_waitcnt lgkmcnt(6)\n"
    "v_mfma_f32_16x16x32_bf16 v[144:147], v[224:227], v[136:139], v[144:147]\n"
    "ds_read_b64_tr_b16 v[208:209], v250 offset:0\n"
    "v_mul_f32 v207, v207, v191\n"
    "v_mfma_f32_16x16x32_bf16 v[148:151], v[224:227], v[140:143], v[148:151]\n"
    "ds_read_b64_tr_b16 v[210:211], v250 offset:512\n"
    "s_add_u32 m0, s97, s73\n"
    "s_nop 0\n"
    "buffer_load_dwordx4 v242, s[40:43], s71 offen lds\n"
    "v_cvt_pk_bf16_f32 v180, v196, v197\n"
    "s_waitcnt lgkmcnt(6)\n"
    "v_mfma_f32_16x16x32_bf16 v[152:155], v[228:231], v[136:139], v[152:155]\n"
    "ds_read_b64_tr_b16 v[212:213], v251 offset:0\n"
    "v_cvt_pk_bf16_f32 v181, v198, v199\n"
    "v_mfma_f32_16x16x32_bf16 v[156:159], v[228:231], v[140:143], v[156:159]\n"
    "ds_read_b64_tr_b16 v[214:215], v251 offset:512\n"
    "v_cvt_pk_bf16_f32 v182, v204, v205\n"
    "s_waitcnt lgkmcnt(6)\n"
    "v_mfma_f32_16x16x32_bf16 v[160:163], v[232:235], v[136:139], v[160:163]\n"
    "ds_read_b64_tr_b16 v[216:217], v250 offset:8192\n"
    "v_cvt_pk_bf16_f32 v183, v206, v207\n"
    "v_mfma_f32_16x16x32_bf16 v[164:167], v[232:235], v[140:143], v[164:167]\n"
    "ds_read_b64_tr_b16 v[218:219], v250 offset:8704\n"
    "s_add_u32 m0, m0, 8192\n"
    "s_nop 0\n"
    "buffer_load_dwordx4 v242, s[44:47], s71 offen lds\n"
    "s_add_u32 s71, s71, s99\n"
    "s_add_u32 s97, s97, 16384\n"
    "s_cmp_eq_u32 s97, 163840\n"
    "s_cselect_b32 s97, 0, s97\n"
    "buffer_store_dwordx4 v[180:183], v252, s[56:59], s75 offen offset:1024 sc0 sc1\n"
    "s_waitcnt lgkmcnt(6)\n"
    "v_mfma_f32_16x16x32_bf16 v[168:171], v[236:239], v[136:139], v[168:171]\n"
    "ds_read_b64_tr_b16 v[220:221], v251 offset:8192\n"
    "s_add_u32 s75, s75, 16384\n"
    "v_mfma_f32_16x16x32_bf16 v[172:175], v[236:239], v[140:143], v[172:175]\n"
    "ds_read_b64_tr_b16 v[222:223], v251 offset:8704\n"
    "s_mov_b32 s38, 0\n"
    "s_cmp_lt_i32 s76, 2\n"
    "s_cbranch_scc1 L1DRA%=\n"
    "L1JB%=:\n"
    "s_waitcnt lgkmcnt(6)\n"
    "v_mfma_f32_16x16x32_bf16 v[176:179], v[208:211], v[0:3], 0\n"
    "ds_read_b64_tr_b16 v[224:225], v250 offset:4096\n"
    "s_add_u32 s96, s96, 16384\n"
    "s_cmp_eq_u32 s96, 163840\n"
    "s_cselect_b32 s96, 0, s96\n"
    "v_mfma_f32_16x16x32_bf16 v[180:183], v[208:211], v[4:7], 0\n"
    "ds_read_b64_tr_b16 v[226:227], v250 offset:4608\n"
    "s_waitcnt lgkmcnt(6)\n"
    "v_mfma_f32_16x16x32_bf16 v[184:187], v[212:215], v[0:3], 0\n"
    "ds_read_b64_tr_b16 v[228:229], v251 offset:4096\n"
    "v_mfma_f32_16x16x32_bf16 v[188:191], v[212:215], v[4:7], 0\n"
    "ds_read_b64_tr_b16 v[230:231], v251 offset:4608\n"
    "s_waitcnt lgkmcnt(6)\n"
    "v_mfma_f32_16x16x32_bf16 v[192:195], v[216:219], v[0:3], 0\n"
    "ds_read_b64_tr_b16 v[232:233], v250 offset:12288\n"
    "v_add_u32 v248, s96, v240\n"
    "v_add_u32 v249, s96, v241\n"
    "v_mfma_f32_16x16x32_bf16 v[196:199], v[216:219], v[4:7], 0\n"
    "ds_read_b64_tr_b16 v[234:235], v250 offset:12800\n"
    "s_waitcnt lgkmcnt(6)\n"
    "v_mfma_f32_16x16x32_bf16 v[200:203], v[220:223], v[0:3], 0\n"
    "ds_read_b64_tr_b16 v[236:237], v251 offset:12288\n"
    "v_mfma_f32_16x16x32_bf16 v[204:207], v[220:223], v[4:7], 0\n"
    "ds_read_b64_tr_b16 v[238:239], v251 offset:12800\n"
    "s_waitcnt lgkmcnt(6)\n"
    "v_mfma_f32_16x16x32_bf16 v[176:179], v[224:227], v[8:11], v[176:179]\n"
    "ds_read_b64_tr_b16 v[208:209], v248 offset:0\n"
    "v_mul_f32 v160, v144, v160\n"
    "v_mfma_f32_16x16x32_bf16 v[180:183], v[224:227], v[12:15], v[180:183]\n"
    "ds_read_b64_tr_b16 v[210:211], v248 offset:512\n"
    "v_mul_f32 v161, v145, v161\n"
    "s_waitcnt lgkmcnt(6)\n"
    "v_mfma_f32_16x16x32_bf16 v[184:187], v[228:231], v[8:11], v[184:187]\n"
    "ds_read_b64_tr_b16 v[212:213], v249 offset:0\n"
    "v_mul_f32 v162, v146, v162\n"
    "v_mfma_f32_16x16x32_bf16 v[188:191], v[228:231], v[12:15], v[188:191]\n"
    "ds_read_b64_tr_b16 v[214:215], v249 offset:512\n"
    "v_mul_f32 v163, v147, v163\n"
    "s_waitcnt lgkmcnt(6)\n"
    "v_mfma_f32_16x16x32_bf16 v[192:195], v[232:235], v[8:11], v[192:195]\n"
    "ds_read_b64_tr_b16 v[216:217], v248 offset:8192\n"
    "v_mfma_f32_16x16x32_bf16 v[196:199], v[232:235], v[12:15], v[196:199]\n"
    "ds_read_b64_tr_b16 v[218:219], v248 offset:8704\n"
    "v_mul_f32 v144, 0xbfb8aa3b, v144\n"
    "s_waitcnt lgkmcnt(6)\n"
    "v_mfma_f32_16x16x32_bf16 v[200:203], v[236:239], v[8:11], v[200:203]\n"
    "ds_read_b64_tr_b16 v[220:221], v249 offset:8192\n"
    "v_mul_f32 v145, 0xbfb8aa3b, v145\n"
    "v_mfma_f32_16x16x32_bf16 v[204:207], v[236:239], v[12:15], v[204:207]\n"
    "ds_read_b64_tr_b16 v[222:223], v249 offset:8704\n"
    "v_mul_f32 v146, 0xbfb8aa3b, v146\n"
    "s_waitcnt lgkmcnt(6)\n"
    "v_mfma_f32_16x16x32_bf16 v[176:179], v[208:211], v[16:19], v[176:179]\n"
    "ds_read_b64_tr_b16 v[224:225], v248 offset:4096\n"
    "s_add_u32 s96, s96, 16384\n"
    "s_cmp_eq_u32 s96, 163840\n"
    "s_cselect_b32 s96, 0, s96\n"
    "v_mul_f32 v147, 0xbfb8aa3b, v147\n"
    "v_mfma_f32_16x16x32_bf16 v[180:183], v[208:211], v[20:23], v[180:183]\n"
    "ds_read_b64_tr_b16 v[226:227], v248 offset:4608\n"
    "s_waitcnt lgkmcnt(6)\n"
    "v_mfma_f32_16x16x32_bf16 v[184:187], v[212:215], v[16:19], v[184:187]\n"
    "ds_read_b64_tr_b16 v[228:229], v249 offset:4096\n"
    "v_exp_f32 v144, v144\n"
    "v_mfma_f32_16x16x32_bf16 v[188:191], v[212:215], v[20:23], v[188:191]\n"
    "ds_read_b64_tr_b16 v[230:231], v249 offset:4608\n"
    "v_exp_f32 v145, v145\n"
    "s_waitcnt lgkmcnt(6)\n"
    "v_mfma_f32_16x16x32_bf16 v[192:195], v[216:219], v[16:19], v[192:195]\n"
    "ds_read_b64_tr_b16 v[232:233], v248 offset:12288\n"
    "v_add_u32 v250, s96, v240\n"
    "v_add_u32 v251, s96, v241\n"
    "v_exp_f32 v146, v146\n"
    "v_mfma_f32_16x16x32_bf16 v[196:199], v[216:219], v[20:23], v[196:199]\n"
    "ds_read_b64_tr_b16 v[234:235], v248 offset:12800\n"
    "v_exp_f32 v147, v147\n"
    "s_waitcnt lgkmcnt(6)\n"
    "v_mfma_f32_16x16x32_bf16 v[200:203], v[220:223], v[16:19], v[200:203]\n"
    "ds_read_b64_tr_b16 v[236:237], v249 offset:12288\n"
    "v_mfma_f32_16x16x32_bf16 v[204:207], v[220:223], v[20:23], v[204:207]\n"
    "ds_read_b64_tr_b16 v[238:239], v249 offset:12800\n"
    "v_add_f32 v144, 1.0, v144\n"
    "s_waitcnt vmcnt(6)\n"
    "s_barrier\n"
    "s_waitcnt lgkmcnt(6)\n"
    "v_mfma_f32_16x16x32_bf16 v[176:179], v[224:227], v[24:27], v[176:179]\n"
    "ds_read_b64_tr_b16 v[208:209], v250 offset:0\n"
    "v_add_f32 v145, 1.0, v145\n"
    "v_mfma_f32_16x16x32_bf16 v[180:183], v[224:227], v[28:31], v[180:183]\n"
    "ds_read_b64_tr_b16 v[210:211], v250 offset:512\n"
    "s_add_u32 m0, s97, s73\n"
    "s_nop 0\n"
    "buffer_load_dwordx4 v242, s[40:43], s71 offen lds\n"
    "v_add_f32 v146, 1.0, v146\n"
    "s_waitcnt lgkmcnt(6)\n"
    "v_mfma_f32_16x16x32_bf16 v[184:187], v[228:231], v[24:27], v[184:187]\n"
    "ds_read_b64_tr_b16 v[212:213], v251 offset:0\n"
    "v_mfma_f32_16x16x32_bf16 v[188:191], v[228:231], v[28:31], v[188:191]\n"
    "ds_read_b64_tr_b16 v[214:215], v251 offset:512\n"
    "v_add_f32 v147, 1.0, v147\n"
    "s_waitcnt lgkmcnt(6)\n"
    "v_mfma_f32_16x16x32_bf16 v[192:195], v[232:235], v[24:27], v[192:195]\n"
    "ds_read_b64_tr_b16 v[216:217], v250 offset:8192\n"
    "v_rcp_f32 v144, v144\n"
    "v_mfma_f32_16x16x32_bf16 v[196:199], v[232:235], v[28:31], v[196:199]\n"
    "ds_read_b64_tr_b16 v[218:219], v250 offset:8704\n"
    "s_add_u32 m0, m0, 8192\n"
    "s_nop 0\n"
    "buffer_load_dwordx4 v242, s[44:47], s71 offen lds\n"
    "s_add_u32 s71, s71, s99\n"
    "s_add_u32 s97, s97, 16384\n"
    "s_cmp_eq_u32 s97, 163840\n"
    "s_cselect_b32 s97, 0, s97\n"
    "v_rcp_f32 v145, v145\n"
    "s_waitcnt lgkmcnt(6)\n"
    "v_mfma_f32_16x16x32_bf16 v[200:203], v[236:239], v[24:27], v[200:203]\n"
    "ds_read_b64_tr_b16 v[220:221], v251 offset:8192\n"
    "v_rcp_f32 v146, v146\n"
    "v_mfma_f32_16x16x32_bf16 v[204:207], v[236:239], v[28:31], v[204:207]\n"
    "ds_read_b64_tr_b16 v[222:223], v251 offset:8704\n"
    "s_waitcnt lgkmcnt(6)\n"
    "v_mfma_f32_16x16x32_bf16 v[176:179], v[208:211], v[32:35], v[176:179]\n"
    "ds_read_b64_tr_b16 v[224:225], v250 offset:4096\n"
    "s_add_u32 s96, s96, 16384\n"
    "s_cmp_eq_u32 s96, 163840\n"
    "s_cselect_b32 s96, 0, s96\n"
    "v_rcp_f32 v147, v147\n"
    "v_mfma_f32_16x16x32_bf16 v[180:183], v[208:211], v[36:39], v[180:183]\n"
    "ds_read_b64_tr_b16 v[226:227], v250 offset:4608\n"
    "s_add_u32 s78, s78, 128\n"
    "s_sub_u32 s76, s76, 1\n"
    "s_cmp_gt_i32 s76, s98\n"
    "s_cbranch_scc1 L1KEEP2%=\n"
    "s_cmp_eq_u32 s76, 2\n"
    "s_cbranch_scc0 L1SIDE2%=\n"
    "s_mov_b64 s[40:41], %4\n"
    "s_and_b32 s41, s41, 0xffff\n"
    "s_mov_b32 s42, 73728\n"
    "s_mov_b64 s[44:45], %6\n"
    "s_and_b32 s45, s45, 0xffff\n"
    "s_mov_b32 s46, 73728\n"
    "s_mov_b32 s99, 8192\n"
    "s_branch L1LIN2%=\n"
    "L1SIDE2%=:\n"
    "s_mov_b32 s40, s48\n"
    "s_mov_b32 s41, s49\n"
    "s_mov_b32 s42, s50\n"
    "s_mov_b32 s43, s51\n"
    "s_mov_b32 s44, s52\n"
    "s_mov_b32 s45, s53\n"
    "s_mov_b32 s46, s54\n"
    "s_mov_b32 s47, s55\n"
    "s_mov_b32 s99, %17\n"
    "s_mov_b32 s95, s97\n"
    "L1LIN2%=:\n"
    "v_mov_b32 v242, v243\n"
    "s_mov_b32 s78, 0\n"
    "L1KEEP2%=:\n"
    "s_mov_b32 s71, s78\n"
    "s_nop 3\n"
    "s_add_u32 m0, s97, s73\n"
    "s_nop 0\n"
    "buffer_load_dwordx4 v242, s[40:43], s71 offen lds\n"
    "v_mul_f32 v160, v160, v144\n"
    "s_waitcnt lgkmcnt(6)\n"
    "v_mfma_f32_16x16x32_bf16 v[184:187], v[212:215], v[32:35], v[184:187]\n"
    "ds_read_b64_tr_b16 v[228:229], v251 offset:4096\n"
    "v_mul_f32 v161, v161, v145\n"
    "v_mfma_f32_16x16x32_bf16 v[188:191], v[212:215], v[36:39], v[188:191]\n"
    "ds_read_b64_tr_b16 v[230:231], v251 offset:4608\n"
    "v_mul_f32 v162, v162, v146\n"
    "s_waitcnt lgkmcnt(6)\n"
    "v_mfma_f32_16x16x32_bf16 v[192:195], v[216:219], v[32:35], v[192:195]\n"
    "ds_read_b64_tr_b16 v[232:233], v250 offset:12288\n"
    "v_add_u32 v248, s96, v240\n"
    "v_add_u32 v249, s96, v241\n"
    "v_mfma_f32_16x16x32_bf16 v[196:199], v[216:219], v[36:39], v[196:199]\n"
    "ds_read_b64_tr_b16 v[234:235], v250 offset:12800\n"
    "s_add_u32 m0, m0, 8192\n"
    "s_nop 0\n"
    "buffer_load_dwordx4 v242, s[44:47], s71 offen lds\n"
    "s_add_u32 s71, s71, s99\n"
    "s_add_u32 s97, s97, 16384\n"
    "s_cmp_eq_u32 s97, 163840\n"
    "s_cselect_b32 s97, 0, s97\n"
    "v_mul_f32 v163, v163, v147\n"
    "s_waitcnt lgkmcnt(6)\n"
    "v_mfma_f32_16x16x32_bf16 v[200:203], v[220:223], v[32:35], v[200:203]\n"
    "ds_read_b64_tr_b16 v[236:237], v251 offset:12288\n"
    "v_mul_f32 v168, v152, v168\n"
    "v_mfma_f32_16x16x32_bf16 v[204:207], v[220:223], v[36:39], v[204:207]\n"
    "ds_read_b64_tr_b16 v[238:239], v251 offset:12800\n"
    "v_mul_f32 v169, v153, v169\n"
    "s_waitcnt lgkmcnt(6)\n"
    "v_mfma_f32_16x16x32_bf16 v[176:179], v[224:227], v[40:43], v[176:179]\n"
    "ds_read_b64_tr_b16 v[208:209], v248 offset:0\n"
    "v_mfma_f32_16x16x32_bf16 v[180:183], v[224:227], v[44:47], v[180:183]\n"
    "ds_read_b64_tr_b16 v[210:211], v248 offset:512\n"
    "s_add_u32 m0, s97, s73\n"
    "s_nop 0\n"
    "buffer_load_dwordx4 v242, s[40:43], s71 offen lds\n"
    "v_mul_f32 v170, v154, v170\n"
    "s_waitcnt lgkmcnt(6)\n"
    "v_mfma_f32_16x16x32_bf16 v[184:187], v[228:231], v[40:43], v[184:187]\n"
    "ds_read_b64_tr_b16 v[212:213], v249 offset:0\n"
    "v_mul_f32 v171, v155, v171\n"
    "v_mfma_f32_16x16x32_bf16 v[188:191], v[228:231], v[44:47], v[188:191]\n"
    "ds_read_b64_tr_b16 v[214:215], v249 offset:512\n"
    "v_mul_f32 v152, 0xbfb8aa3b, v152\n"
    "s_waitcnt lgkmcnt(6)\n"
    "v_mfma_f32_16x16x32_bf16 v[192:195], v[232:235], v[40:43], v[192:195]\n"
    "ds_read_b64_tr_b16 v[216:217], v248 offset:8192\n"
    "v_mul_f32 v153, 0xbfb8aa3b, v153\n"
    "v_mfma_f32_16x16x32_bf16 v[196:199], v[232:235], v[44:47], v[196:199]\n"
    "ds_read_b64_tr_b16 v[218:219], v248 offset:8704\n"
    "s_add_u32 m0, m0, 8192\n"
    "s_nop 0\n"
    "buffer_load_dwordx4 v242, s[44:47], s71 offen lds\n"
    "s_add_u32 s71, s71, s99\n"
    "s_add_u32 s97, s97, 16384\n"
    "s_cmp_eq_u32 s97, 163840\n"
    "s_cselect_b32 s97, 0, s97\n"
    "s_waitcnt lgkmcnt(6)\n"
    "v_mfma_f32_16x16x32_bf16 v[200:203], v[236:239], v[40:43], v[200:203]\n"
    "ds_read_b64_tr_b16 v[220:221], v249 offset:8192\n"
    "v_mul_f32 v154, 0xbfb8aa3b, v154\n"
    "v_mfma_f32_16x16x32_bf16 v[204:207], v[236:239], v[44:47], v[204:207]\n"
    "ds_read_b64_tr_b16 v[222:223], v249 offset:8704\n"
    "v_mul_f32 v155, 0xbfb8aa3b, v155\n"
    "s_waitcnt lgkmcnt(6)\n"
    "v_mfma_f32_16x16x32_bf16 v[176:179], v[208:211], v[48:51], v[176:179]\n"
    "ds_read_b64_tr_b16 v[224:225], v248 offset:4096\n"
    "s_add_u32 s96, s96, 16384\n"
    "s_cmp_eq_u32 s96, 163840\n"
    "s_cselect_b32 s96, 0, s96\n"
    "v_exp_f32 v152, v152\n"
    "v_mfma_f32_16x16x32_bf16 v[180:183], v[208:211], v[52:55], v[180:183]\n"
    "ds_read_b64_tr_b16 v[226:227], v248 offset:4608\n"
    "v_exp_f32 v153, v153\n"
    "s_waitcnt lgkmcnt(6)\n"
    "v_mfma_f32_16x16x32_bf16 v[184:187], v[212:215], v[48:51], v[184:187]\n"
    "ds_read_b64_tr_b16 v[228:229], v249 offset:4096\n"
    "v_mfma_f32_16x16x32_bf16 v[188:191], v[212:215], v[52:55], v[188:191]\n"
    "ds_read_b64_tr_b16 v[230:231], v249 offset:4608\n"
    "v_exp_f32 v154, v154\n"
    "s_waitcnt lgkmcnt(6)\n"
    "v_mfma_f32_16x16x32_bf16 v[192:195], v[216:219], v[48:51], v[192:195]\n"
    "ds_read_b64_tr_b16 v[232:233], v248 offset:12288\n"
    "v_add_u32 v250, s96, v240\n"
    "v_add_u32 v251, s96, v241\n"
    "v_exp_f32 v155, v155\n"
    "v_mfma_f32_16x16x32_bf16 v[196:199], v[216:219], v[52:55], v[196:199]\n"
    "ds_read_b64_tr_b16 v[234:235], v248 offset:12800\n"
    "v_add_f32 v152, 1.0, v152\n"
    "s_waitcnt lgkmcnt(6)\n"
    "v_mfma_f32_16x16x32_bf16 v[200:203], v[220:223], v[48:51], v[200:203]\n"
    "ds_read_b64_tr_b16 v[236:237], v249 offset:12288\n"
    "v_mfma_f32_16x16x32_bf16 v[204:207], v[220:223], v[52:55], v[204:207]\n"
    "ds_read_b64_tr_b16 v[238:239], v249 offset:12800\n"
    "v_add_f32 v153, 1.0, v153\n"
    "s_waitcnt lgkmcnt(6)\n"
    "v_mfma_f32_16x16x32_bf16 v[176:179], v[224:227], v[56:59], v[176:179]\n"
    "ds_read_b64_tr_b16 v[208:209], v250 offset:0\n"
    "v_add_f32 v154, 1.0, v154\n"
    "v_mfma_f32_16x16x32_bf16 v[180:183], v[224:227], v[60:63], v[180:183]\n"
    "ds_read_b64_tr_b16 v[210:211], v250 offset:512\n"
    "v_add_f32 v155, 1.0, v155\n"
    "s_waitcnt lgkmcnt(6)\n"
    "v_mfma_f32_16x16x32_bf16 v[184:187], v[228:231], v[56:59], v[184:187]\n"
    "ds_read_b64_tr_b16 v[212:213], v251 offset:0\n"
    "v_rcp_f32 v152, v152\n"
    "v_mfma_f32_16x16x32_bf16 v[188:191], v[228:231], v[60:63], v[188:191]\n"
    "ds_read_b64_tr_b16 v[214:215], v251 offset:512\n"
    "s_waitcnt lgkmcnt(6)\n"
    "v_mfma_f32_16x16x32_bf16 v[192:195], v[232:235], v[56:59], v[192:195]\n"
    "ds_read_b64_tr_b16 v[216:217], v250 offset:8192\n"
    "v_rcp_f32 v153, v153\n"
    "v_mfma_f32_16x16x32_bf16 v[196:199], v[232:235], v[60:63], v[196:199]\n"
    "ds_read_b64_tr_b16 v[218:219], v250 offset:8704\n"
    "v_rcp_f32 v154, v154\n"
    "s_waitcnt lgkmcnt(6)\n"
    "v_mfma_f32_16x16x32_bf16 v[200:203], v[236:239], v[56:59], v[200:203]\n"
    "ds_read_b64_tr_b16 v[220:221], v251 offset:8192\n"
    "v_rcp_f32 v155, v155\n"
    "v_mfma_f32_16x16x32_bf16 v[204:207], v[236:239], v[60:63], v[204:207]\n"
    "ds_read_b64_tr_b16 v[222:223], v251 offset:8704\n"
    "v_mul_f32 v168, v168, v152\n"
    "s_waitcnt lgkmcnt(6)\n"
    "v_mfma_f32_16x16x32_bf16 v[176:179], v[208:211], v[64:67], v[176:179]\n"
    "ds_read_b64_tr_b16 v[224:225], v250 offset:4096\n"
    "s_add_u32 s96, s96, 16384\n"
    "s_cmp_eq_u32 s96, 163840\n"
    "s_cselect_b32 s96, 0, s96\n"
    "v_mfma_f32_16x16x32_bf16 v[180:183], v[208:211], v[68:71], v[180:183]\n"
    "ds_read_b64_tr_b16 v[226:227], v250 offset:4608\n"
    "v_mul_f32 v169, v169, v153\n"
    "s_waitcnt lgkmcnt(6)\n"
    "v_mfma_f32_16x16x32_bf16 v[184:187], v[212:215], v[64:67], v[184:187]\n"
    "ds_read_b64_tr_b16 v[228:229], v251 offset:4096\n"
    "v_mul_f32 v170, v170, v154\n"
    "v_mfma_f32_16x16x32_bf16 v[188:191], v[212:215], v[68:71], v[188:191]\n"
    "ds_read_b64_tr_b16 v[230:231], v251 offset:4608\n"
    "v_mul_f32 v171, v171, v155\n"
    "s_waitcnt lgkmcnt(6)\n"
    "v_mfma_f32_16x16x32_bf16 v[192:195], v[216:219], v[64:67], v[192:195]\n"
    "ds_read_b64_tr_b16 v[232:233], v250 offset:12288\n"
    "v_add_u32 v248, s96, v240\n"
    "v_add_u32 v249, s96, v241\n"
    "v_mfma_f32_16x16x32_bf16 v[196:199], v[216:219], v[68:71], v[196:199]\n"
    "ds_read_b64_tr_b16 v[234:235], v250 offset:12800\n"
    "v_cvt_pk_bf16_f32 v144, v160, v161\n"
    "s_waitcnt lgkmcnt(6)\n"
    "v_mfma_f32_16x16x32_bf16 v[200:203], v[220:223], v[64:67], v[200:203]\n"
    "ds_read_b64_tr_b16 v[236:237], v251 offset:12288\n"
    "v_cvt_pk_bf16_f32 v145, v162, v163\n"
    "v_mfma_f32_16x16x32_bf16 v[204:207], v[220:223], v[68:71], v[204:207]\n"
    "ds_read_b64_tr_b16 v[238:239], v251 offset:12800\n"
    "v_cvt_pk_bf16_f32 v146, v168, v169\n"
    "s_waitcnt vmcnt(6)\n"
    "s_barrier\n"
    "s_waitcnt lgkmcnt(6)\n"
    "v_mfma_f32_16x16x32_bf16 v[176:179], v[224:227], v[72:75], v[176:179]\n"
    "ds_read_b64_tr_b16 v[208:209], v248 offset:0\n"
    "v_cvt_pk_bf16_f32 v147, v170, v171\n"
    "v_mfma_f32_16x16x32_bf16 v[180:183], v[224:227], v[76:79], v[180:183]\n"
    "ds_read_b64_tr_b16 v[210:211], v248 offset:512\n"
    "s_add_u32 m0, s97, s73\n"
    "s_nop 0\n"
    "buffer_load_dwordx4 v242, s[40:43], s71 offen lds\n"
    "s_waitcnt lgkmcnt(6)\n"
    "v_mfma_f32_16x16x32_bf16 v[184:187], v[228:231], v[72:75], v[184:187]\n"
    "ds_read_b64_tr_b16 v[212:213], v249 offset:0\n"
    "buffer_store_dwordx4 v[144:147], v252, s[56:59], s75 offen offset:0 sc0 sc1\n"
    "v_mfma_f32_16x16x32_bf16 v[188:191], v[228:231], v[76:79], v[188:191]\n"
    "ds_read_b64_tr_b16 v[214:215], v249 offset:512\n"
    "v_mul_f32 v164, v148, v164\n"
    "s_waitcnt lgkmcnt(6)\n"
    "v_mfma_f32_16x16x32_bf16 v[192:195], v[232:235], v[72:75], v[192:195]\n"
    "ds_read_b64_tr_b16 v[216:217], v248 offset:8192\n"
    "v_mul_f32 v165, v149, v165\n"
    "v_mfma_f32_16x16x32_bf16 v[196:199], v[232:235], v[76:79], v[196:199]\n"
    "ds_read_b64_tr_b16 v[218:219], v248 offset:8704\n"
    "s_add_u32 m0, m0, 8192\n"
    "s_nop 0\n"
    "buffer_load_dwordx4 v242, s[44:47], s71 offen lds\n"
    "s_add_u32 s71, s71, s99\n"
    "s_add_u32 s97, s97, 16384\n"
    "s_cmp_eq_u32 s97, 163840\n"
    "s_cselect_b32 s97, 0, s97\n"
    "v_mul_f32 v166, v150, v166\n"
    "s_waitcnt lgkmcnt(6)\n"
    "v_mfma_f32_16x16x32_bf16 v[200:203], v[236:239], v[72:75], v[200:203]\n"
    "ds_read_b64_tr_b16 v[220:221], v249 offset:8192\n"
    "v_mfma_f32_16x16x32_bf16 v[204:207], v[236:239], v[76:79], v[204:207]\n"
    "ds_read_b64_tr_b16 v[222:223], v249 offset:8704\n"
    "v_mul_f32 v167, v151, v167\n"
    "s_waitcnt lgkmcnt(6)\n"
    "v_mfma_f32_16x16x32_bf16 v[176:179], v[208:211], v[80:83], v[176:179]\n"
    "ds_read_b64_tr_b16 v[224:225], v248 offset:4096\n"
    "s_add_u32 s96, s96, 16384\n"
    "s_cmp_eq_u32 s96, 163840\n"
    "s_cselect_b32 s96, 0, s96\n"
    "v_mul_f32 v148, 0xbfb8aa3b, v148\n"
    "v_mfma_f32_16x16x32_bf16 v[180:183], v[208:211], v[84:87], v[180:183]\n"
    "ds_read_b64_tr_b16 v[226:227], v248 offset:4608\n"
    "s_add_u32 m0, s97, s73\n"
    "s_nop 0\n"
    "buffer_load_dwordx4 v242, s[40:43], s71 offen lds\n"
    "v_mul_f32 v149, 0xbfb8aa3b, v149\n"
    "s_waitcnt lgkmcnt(6)\n"
    "v_mfma_f32_16x16x32_bf16 v[184:187], v[212:215], v[80:83], v[184:187]\n"
    "ds_read_b64_tr_b16 v[228:229], v249 offset:4096\n"
    "v_mul_f32 v150, 0xbfb8aa3b, v150\n"
    "v_mfma_f32_16x16x32_bf16 v[188:191], v[212:215], v[84:87], v[188:191]\n"
    "ds_read_b64_tr_b16 v[230:231], v249 offset:4608\n"
    "s_waitcnt lgkmcnt(6)\n"
    "v_mfma_f32_16x16x32_bf16 v[192:195], v[216:219], v[80:83], v[192:195]\n"
    "ds_read_b64_tr_b16 v[232:233], v248 offset:12288\n"
    "v_add_u32 v250, s96, v240\n"
    "v_add_u32 v251, s96, v241\n"
    "v_mul_f32 v151, 0xbfb8aa3b, v151\n"
    "v_mfma_f32_16x16x32_bf16 v[196:199], v[216:219], v[84:87], v[196:199]\n"
    "ds_read_b64_tr_b16 v[234:235], v248 offset:12800\n"
    "s_add_u32 m0, m0, 8192\n"
    "s_nop 0\n"
    "buffer_load_dwordx4 v242, s[44:47], s71 offen lds\n"
    "s_add_u32 s71, s71, s99\n"
    "s_add_u32 s97, s97, 16384\n"
    "s_cmp_eq_u32 s97, 163840\n"
    "s_cselect_b32 s97, 0, s97\n"
    "v_exp_f32 v148, v148\n"
    "s_waitcnt lgkmcnt(6)\n"
    "v_mfma_f32_16x16x32_bf16 v[200:203], v[220:223], v[80:83], v[200:203]\n"
    "ds_read_b64_tr_b16 v[236:237], v249 offset:12288\n"
    "v_exp_f32 v149, v149\n"
    "v_mfma_f32_16x16x32_bf16 v[204:207], v[220:223], v[84:87], v[204:207]\n"
    "ds_read_b64_tr_b16 v[238:239], v249 offset:12800\n"
    "s_waitcnt lgkmcnt(6)\n"
    "v_mfma_f32_16x16x32_bf16 v[176:179], v[224:227], v[88:91], v[176:179]\n"
    "ds_read_b64_tr_b16 v[208:209], v250 offset:0\n"
    "v_exp_f32 v150, v150\n"
    "v_mfma_f32_16x16x32_bf16 v[180:183], v[224:227], v[92:95], v[180:183]\n"
    "ds_read_b64_tr_b16 v[210:211], v250 offset:512\n"
    "s_add_u32 m0, s97, s73\n"
    "s_nop 0\n"
    "buffer_load_dwordx4 v242, s[40:43], s71 offen lds\n"
    "v_exp_f32 v151, v151\n"
    "s_waitcnt lgkmcnt(6)\n"
    "v_mfma_f32_16x16x32_bf16 v[184:187], v[228:231], v[88:91], v[184:187]\n"
    "ds_read_b64_tr_b16 v[212:213], v251 offset:0\n"
    "v_add_f32 v148, 1.0, v148\n"
    "v_mfma_f32_16x16x32_bf16 v[188:191], v[228:231], v[92:95], v[188:191]\n"
    "ds_read_b64_tr_b16 v[214:215], v251 offset:512\n"
    "v_add_f32 v149, 1.0, v149\n"
    "s_waitcnt lgkmcnt(6)\n"
    "v_mfma_f32_16x16x32_bf16 v[192:195], v[232:235], v[88:91], v[192:195]\n"
    "ds_read_b64_tr_b16 v[216:217], v250 offset:8192\n"
    "v_mfma_f32_16x16x32_bf16 v[196:199], v[232:235], v[92:95], v[196:199]\n"
    "ds_read_b64_tr_b16 v[218:219], v250 offset:8704\n"
    "s_add_u32 m0, m0, 8192\n"
    "s_nop 0\n"
    "buffer_load_dwordx4 v242, s[44:47], s71 offen lds\n"
    "s_add_u32 s71, s71, s99\n"
    "s_add_u32 s97, s97, 16384\n"
    "s_cmp_eq_u32 s97, 163840\n"
    "s_cselect_b32 s97, 0, s97\n"
    "v_add_f32 v150, 1.0, v150\n"
    "s_waitcnt lgkmcnt(6)\n"
    "v_mfma_f32_16x16x32_bf16 v[200:203], v[236:239], v[88:91], v[200:203]\n"
    "ds_read_b64_tr_b16 v[220:221], v251 offset:8192\n"
    "v_add_f32 v151, 1.0, v151\n"
    "v_mfma_f32_16x16x32_bf16 v[204:207], v[236:239], v[92:95], v[204:207]\n"
    "ds_read_b64_tr_b16 v[222:223], v251 offset:8704\n"
    "v_rcp_f32 v148, v148\n"
    "s_waitcnt lgkmcnt(6)\n"
    "v_mfma_f32_16x16x32_bf16 v[176:179], v[208:211], v[96:99], v[176:179]\n"
    "ds_read_b64_tr_b16 v[224:225], v250 offset:4096\n"
    "s_add_u32 s96, s96, 16384\n"
    "s_cmp_eq_u32 s96, 163840\n"
    "s_cselect_b32 s96, 0, s96\n"
    "v_rcp_f32 v149, v149\n"
    "v_mfma_f32_16x16x32_bf16 v[180:183], v[208:211], v[100:103], v[180:183]\n"
    "ds_read_b64_tr_b16 v[226:227], v250 offset:4608\n"
    "s_waitcnt lgkmcnt(6)\n"
    "v_mfma_f32_16x16x32_bf16 v[184:187], v[212:215], v[96:99], v[184:187]\n"
    "ds_read_b64_tr_b16 v[228:229], v251 offset:4096\n"
    "v_rcp_f32 v150, v150\n"
    "v_mfma_f32_16x16x32_bf16 v[188:191], v[212:215], v[100:103], v[188:191]\n"
    "ds_read_b64_tr_b16 v[230:231], v251 offset:4608\n"
    "v_rcp_f32 v151, v151\n"
    "s_waitcnt lgkmcnt(6)\n"
    "v_mfma_f32_16x16x32_bf16 v[192:195], v[216:219], v[96:99], v[192:195]\n"
    "ds_read_b64_tr_b16 v[232:233], v250 offset:12288\n"
    "v_add_u32 v248, s96, v240\n"
    "v_add_u32 v249, s96, v241\n"
    "v_mul_f32 v164, v164, v148\n"
    "v_mfma_f32_16x16x32_bf16 v[196:199], v[216:219], v[100:103], v[196:199]\n"
    "ds_read_b64_tr_b16 v[234:235], v250 offset:12800\n"
    "s_waitcnt lgkmcnt(6)\n"
    "v_mfma_f32_16x16x32_bf16 v[200:203], v[220:223], v[96:99], v[200:203]\n"
    "ds_read_b64_tr_b16 v[236:237], v251 offset:12288\n"
    "v_mul_f32 v165, v165, v149\n"
    "v_mfma_f32_16x16x32_bf16 v[204:207], v[220:223], v[100:103], v[204:207]\n"
    "ds_read_b64_tr_b16 v[238:239], v251 offset:12800\n"
    "v_mul_f32 v166, v166, v150\n"
    "s_waitcnt lgkmcnt(6)\n"
    "v_mfma_f32_16x16x32_bf16 v[176:179], v[224:227], v[104:107], v[176:179]\n"
    "ds_read_b64_tr_b16 v[208:209], v248 offset:0\n"
    "v_mul_f32 v167, v167, v151\n"
    "v_mfma_f32_16x16x32_bf16 v[180:183], v[224:227], v[108:111], v[180:183]\n"
    "ds_read_b64_tr_b16 v[210:211], v248 offset:512\n"
    "v_mul_f32 v172, v156, v172\n"
    "s_waitcnt lgkmcnt(6)\n"
    "v_mfma_f32_16x16x32_bf16 v[184:187], v[228:231], v[104:107], v[184:187]\n"
    "ds_read_b64_tr_b16 v[212:213], v249 offset:0\n"
    "v_mfma_f32_16x16x32_bf16 v[188:191], v[228:231], v[108:111], v[188:191]\n"
    "ds_read_b64_tr_b16 v[214:215], v249 offset:512\n"
    "v_mul_f32 v173, v157, v173\n"
    "s_waitcnt lgkmcnt(6)\n"
    "v_mfma_f32_16x16x32_bf16 v[192:195], v[232:235], v[104:107], v[192:195]\n"
    "ds_read_b64_tr_b16 v[216:217], v248 offset:8192\n"
    "v_mul_f32 v174, v158, v174\n"
    "v_mfma_f32_16x16x32_bf16 v[196:199], v[232:235], v[108:111], v[196:199]\n"
    "ds_read_b64_tr_b16 v[218:219], v248 offset:8704\n"
    "v_mul_f32 v175, v159, v175\n"
    "s_waitcnt lgkmcnt(6)\n"
    "v_mfma_f32_16x16x32_bf16 v[200:203], v[236:239], v[104:107], v[200:203]\n"
    "ds_read_b64_tr_b16 v[220:221], v249 offset:8192\n"
    "v_mul_f32 v156, 0xbfb8aa3b, v156\n"
    "v_mfma_f32_16x16x32_bf16 v[204:207], v[236:239], v[108:111], v[204:207]\n"
    "ds_read_b64_tr_b16 v[222:223], v249 offset:8704\n"
    "s_waitcnt lgkmcnt(6)\n"
    "v_mfma_f32_16x16x32_bf16 v[176:179], v[208:211], v[112:115], v[176:179]\n"
    "ds_read_b64_tr_b16 v[224:225], v248 offset:4096\n"
    "s_add_u32 s96, s96, 16384\n"
    "s_cmp_eq_u32 s96, 163840\n"
    "s_cselect_b32 s96, 0, s96\n"
    "v_mul_f32 v157, 0xbfb8aa3b, v157\n"
    "v_mfma_f32_16x16x32_bf16 v[180:183], v[208:211], v[116:119], v[180:183]\n"
    "ds_read_b64_tr_b16 v[226:227], v248 offset:4608\n"
    "v_mul_f32 v158, 0xbfb8aa3b, v158\n"
    "s_waitcnt lgkmcnt(6)\n"
    "v_mfma_f32_16x16x32_bf16 v[184:187], v[212:215], v[112:115], v[184:187]\n"
    "ds_read_b64_tr_b16 v[228:229], v249 offset:4096\n"
    "v_mul_f32 v159, 0xbfb8aa3b, v159\n"
    "v_mfma_f32_16x16x32_bf16 v[188:191], v[212:215], v[116:119], v[188:191]\n"
    "ds_read_b64_tr_b16 v[230:231], v249 offset:4608\n"
    "s_waitcnt lgkmcnt(6)\n"
    "v_mfma_f32_16x16x32_bf16 v[192:195], v[216:219], v[112:115], v[192:195]\n"
    "ds_read_b64_tr_b16 v[232:233], v248 offset:12288\n"
    "v_add_u32 v250, s96, v240\n"
    "v_add_u32 v251, s96, v241\n"
    "v_exp_f32 v156, v156\n"
    "v_mfma_f32_16x16x32_bf16 v[196:199], v[216:219], v[116:119], v[196:199]\n"
    "ds_read_b64_tr_b16 v[234:235], v248 offset:12800\n"
    "v_exp_f32 v157, v157\n"
    "s_waitcnt lgkmcnt(6)\n"
    "v_mfma_f32_16x16x32_bf16 v[200:203], v[220:223], v[112:115], v[200:203]\n"
    "ds_read_b64_tr_b16 v[236:237], v249 offset:12288\n"
    "v_exp_f32 v158, v158\n"
    "v_mfma_f32_16x16x32_bf16 v[204:207], v[220:223], v[116:119], v[204:207]\n"
    "ds_read_b64_tr_b16 v[238:239], v249 offset:12800\n"
    "v_exp_f32 v159, v159\n"
    "s_waitcnt vmcnt(7)\n"
    "s_barrier\n"
    "s_waitcnt lgkmcnt(6)\n"
    "v_mfma_f32_16x16x32_bf16 v[176:179], v[224:227], v[120:123], v[176:179]\n"
    "ds_read_b64_tr_b16 v[208:209], v250 offset:0\n"
    "v_mfma_f32_16x16x32_bf16 v[180:183], v[224:227], v[124:127], v[180:183]\n"
    "ds_read_b64_tr_b16 v[210:211], v250 offset:512\n"
    "s_add_u32 m0, s97, s73\n"
    "s_nop 0\n"
    "buffer_load_dwordx4 v242, s[40:43], s71 offen lds\n"
    "v_add_f32 v156, 1.0, v156\n"
    "s_waitcnt lgkmcnt(6)\n"
    "v_mfma_f32_16x16x32_bf16 v[184:187], v[228:231], v[120:123], v[184:187]\n"
    "ds_read_b64_tr_b16 v[212:213], v251 offset:0\n"
    "v_add_f32 v157, 1.0, v157\n"
    "v_mfma_f32_16x16x32_bf16 v[188:191], v[228:231], v[124:127], v[188:191]\n"
    "ds_read_b64_tr_b16 v[214:215], v251 offset:512\n"
    "v_add_f32 v158, 1.0, v158\n"
    "s_waitcnt lgkmcnt(6)\n"
    "v_mfma_f32_16x16x32_bf16 v[192:195], v[232:235], v[120:123], v[192:195]\n"
    "ds_read_b64_tr_b16 v[216:217], v250 offset:8192\n"
    "v_add_f32 v159, 1.0, v159\n"
    "v_mfma_f32_16x16x32_bf16 v[196:199], v[232:235], v[124:127], v[196:199]\n"
    "ds_read_b64_tr_b16 v[218:219], v250 offset:8704\n"
    "s_add_u32 m0, m0, 8192\n"
    "s_nop 0\n"
    "buffer_load_dwordx4 v242, s[44:47], s71 offen lds\n"
    "s_add_u32 s71, s71, s99\n"
    "s_add_u32 s97, s97, 16384\n"
    "s_cmp_eq_u32 s97, 163840\n"
    "s_cselect_b32 s97, 0, s97\n"
    "s_waitcnt lgkmcnt(6)\n"
    "v_mfma_f32_16x16x32_bf16 v[200:203], v[236:239], v[120:123], v[200:203]\n"
    "ds_read_b64_tr_b16 v[220:221], v251 offset:8192\n"
    "v_rcp_f32 v156, v156\n"
    "v_mfma_f32_16x16x32_bf16 v[204:207], v[236:239], v[124:127], v[204:207]\n"
    "ds_read_b64_tr_b16 v[222:223], v251 offset:8704\n"
    "v_rcp_f32 v157, v157\n"
    "s_waitcnt lgkmcnt(6)\n"
    "v_mfma_f32_16x16x32_bf16 v[176:179], v[208:211], v[128:131], v[176:179]\n"
    "ds_read_b64_tr_b16 v[224:225], v250 offset:4096\n"
    "s_add_u32 s96, s96, 16384\n"
    "s_cmp_eq_u32 s96, 163840\n"
    "s_cselect_b32 s96, 0, s96\n"
    "v_rcp_f32 v158, v158\n"
    "v_mfma_f32_16x16x32_bf16 v[180:183], v[208:211], v[132:135], v[180:183]\n"
    "ds_read_b64_tr_b16 v[226:227], v250 offset:4608\n"
    "s_add_u32 m0, s97, s73\n"
    "s_nop 0\n"
    "buffer_load_dwordx4 v242, s[40:43], s71 offen lds\n"
    "s_waitcnt lgkmcnt(6)\n"
    "v_mfma_f32_16x16x32_bf16 v[184:187], v[212:215], v[128:131], v[184:187]\n"
    "ds_read_b64_tr_b16 v[228:229], v251 offset:4096\n"
    "v_rcp_f32 v159, v159\n"
    "v_mfma_f32_16x16x32_bf16 v[188:191], v[212:215], v[132:135], v[188:191]\n"
    "ds_read_b64_tr_b16 v[230:231], v251 offset:4608\n"
    "v_mul_f32 v172, v172, v156\n"
    "s_waitcnt lgkmcnt(6)\n"
    "v_mfma_f32_16x16x32_bf16 v[192:195], v[216:219], v[128:131], v[192:195]\n"
    "ds_read_b64_tr_b16 v[232:233], v250 offset:12288\n"
    "v_add_u32 v248, s96, v240\n"
    "v_add_u32 v249, s96, v241\n"
    "v_mul_f32 v173, v173, v157\n"
    "v_mfma_f32_16x16x32_bf16 v[196:199], v[216:219], v[132:135], v[196:199]\n"
    "ds_read_b64_tr_b16 v[234:235], v250 offset:12800\n"
    "s_add_u32 m0, m0, 8192\n"
    "s_nop 0\n"
    "buffer_load_dwordx4 v242, s[44:47], s71 offen lds\n"
    "s_add_u32 s71, s71, s99\n"
    "s_add_u32 s97, s97, 16384\n"
    "s_cmp_eq_u32 s97, 163840\n"
    "s_cselect_b32 s97, 0, s97\n"
    "v_mul_f32 v174, v174, v158\n"
    "s_waitcnt lgkmcnt(6)\n"
    "v_mfma_f32_16x16x32_bf16 v[200:203], v[220:223], v[128:131], v[200:203]\n"
    "ds_read_b64_tr_b16 v[236:237], v251 offset:12288\n"
    "v_mfma_f32_16x16x32_bf16 v[204:207], v[220:223], v[132:135], v[204:207]\n"
    "ds_read_b64_tr_b16 v[238:239], v251 offset:12800\n"
    "v_mul_f32 v175, v175, v159\n"
    "s_waitcnt lgkmcnt(6)\n"
    "v_mfma_f32_16x16x32_bf16 v[176:179], v[224:227], v[136:139], v[176:179]\n"
    "ds_read_b64_tr_b16 v[208:209], v248 offset:0\n"
    "v_cvt_pk_bf16_f32 v148, v164, v165\n"
    "v_mfma_f32_16x16x32_bf16 v[180:183], v[224:227], v[140:143], v[180:183]\n"
    "ds_read_b64_tr_b16 v[210:211], v248 offset:512\n"
    "s_add_u32 m0, s97, s73\n"
    "s_nop 0\n"
    "buffer_load_dwordx4 v242, s[40:43], s71 offen lds\n"
    "v_cvt_pk_bf16_f32 v149, v166, v167\n"
    "s_waitcnt lgkmcnt(6)\n"
    "v_mfma_f32_16x16x32_bf16 v[184:187], v[228:231], v[136:139], v[184:187]\n"
    "ds_read_b64_tr_b16 v[212:213], v249 offset:0\n"
    "v_cvt_pk_bf16_f32 v150, v172, v173\n"
    "v_mfma_f32_16x16x32_bf16 v[188:191], v[228:231], v[140:143], v[188:191]\n"
    "ds_read_b64_tr_b16 v[214:215], v249 offset:512\n"
    "s_waitcnt lgkmcnt(6)\n"
    "v_mfma_f32_16x16x32_bf16 v[192:195], v[232:235], v[136:139], v[192:195]\n"
    "ds_read_b64_tr_b16 v[216:217], v248 offset:8192\n"
    "v_cvt_pk_bf16_f32 v151, v174, v175\n"
    "v_mfma_f32_16x16x32_bf16 v[196:199], v[232:235], v[140:143], v[196:199]\n"
    "ds_read_b64_tr_b16 v[218:219], v248 offset:8704\n"
    "s_add_u32 m0, m0, 8192\n"
    "s_nop 0\n"
    "buffer_load_dwordx4 v242, s[44:47], s71 offen lds\n"
    "s_add_u32 s71, s71, s99\n"
    "s_add_u32 s97, s97, 16384\n"
    "s_cmp_eq_u32 s97, 163840\n"
    "s_cselect_b32 s97, 0, s97\n"
    "buffer_store_dwordx4 v[148:151], v252, s[56:59], s75 offen offset:1024 sc0 sc1\n"
    "s_waitcnt lgkmcnt(6)\n"
    "v_mfma_f32_16x16x32_bf16 v[200:203], v[236:239], v[136:139], v[200:203]\n"
    "ds_read_b64_tr_b16 v[220:221], v249 offset:8192\n"
    "s_add_u32 s75, s75, 16384\n"
    "v_mfma_f32_16x16x32_bf16 v[204:207], v[236:239], v[140:143], v[204:207]\n"
    "ds_read_b64_tr_b16 v[222:223], v249 offset:8704\n"
    "s_cmp_lt_i32 s76, 2\n"
    "s_cbranch_scc1 L1DRB%=\n"
    "s_cmp_eq_u32 s76, 2\n"
    "s_cselect_b32 s67, s77, 0\n"
    "s_cmp_eq_u32 s67, 1\n"
    "s_cbranch_scc0 L1JA%=\n"
    "s_waitcnt lgkmcnt(6)\n"
    "v_mfma_f32_16x16x32_bf16 v[144:147], v[0:3], v[208:211], 0\n"
    "ds_read_b64_tr_b16 v[224:225], v248 offset:4096\n"
    "s_add_u32 s96, s96, 16384\n"
    "s_cmp_eq_u32 s96, 163840\n"
    "s_cselect_b32 s96, 0, s96\n"
    "v_mfma_f32_16x16x32_bf16 v[148:151], v[4:7], v[208:211], 0\n"
    "ds_read_b64_tr_b16 v[226:227], v248 offset:4608\n"
    "s_waitcnt lgkmcnt(6)\n"
    "v_mfma_f32_16x16x32_bf16 v[152:155], v[0:3], v[212:215], 0\n"
    "ds_read_b64_tr_b16 v[228:229], v249 offset:4096\n"
    "v_mfma_f32_16x16x32_bf16 v[156:159], v[4:7], v[212:215], 0\n"
    "ds_read_b64_tr_b16 v[230:231], v249 offset:4608\n"
    "s_waitcnt lgkmcnt(6)\n"
    "v_mfma_f32_16x16x32_bf16 v[160:163], v[0:3], v[216:219], 0\n"
    "ds_read_b64_tr_b16 v[232:233], v248 offset:12288\n"
    "v_add_u32 v250, s96, v240\n"
    "v_add_u32 v251, s96, v241\n"
    "v_mfma_f32_16x16x32_bf16 v[164:167], v[4:7], v[216:219], 0\n"
    "ds_read_b64_tr_b16 v[234:235], v248 offset:12800\n"
    "s_waitcnt lgkmcnt(6)\n"
    "v_mfma_f32_16x16x32_bf16 v[168:171], v[0:3], v[220:223], 0\n"
    "ds_read_b64_tr_b16 v[236:237], v249 offset:12288\n"
    "v_mfma_f32_16x16x32_bf16 v[172:175], v[4:7], v[220:223], 0\n"
    "ds_read_b64_tr_b16 v[238:239], v249 offset:12800\n"
    "s_waitcnt lgkmcnt(6)\n"
    "v_mfma_f32_16x16x32_bf16 v[144:147], v[8:11], v[224:227], v[144:147]\n"
    "ds_read_b64_tr_b16 v[208:209], v250 offset:0\n"
    "v_mul_f32 v192, v176, v192\n"
    "v_mfma_f32_16x16x32_bf16 v[148:151], v[12:15], v[224:227], v[148:151]\n"
    "ds_read_b64_tr_b16 v[210:211], v250 offset:512\n"
    "v_mul_f32 v193, v177, v193\n"
    "s_waitcnt lgkmcnt(6)\n"
    "v_mfma_f32_16x16x32_bf16 v[152:155], v[8:11], v[228:231], v[152:155]\n"
    "ds_read_b64_tr_b16 v[212:213], v251 offset:0\n"
    "v_mul_f32 v194, v178, v194\n"
    "v_mfma_f32_16x16x32_bf16 v[156:159], v[12:15], v[228:231], v[156:159]\n"
    "ds_read_b64_tr_b16 v[214:215], v251 offset:512\n"
    "v_mul_f32 v195, v179, v195\n"
    "s_waitcnt lgkmcnt(6)\n"
    "v_mfma_f32_16x16x32_bf16 v[160:163], v[8:11], v[232:235], v[160:163]\n"
    "ds_read_b64_tr_b16 v[216:217], v250 offset:8192\n"
    "v_mfma_f32_16x16x32_bf16 v[164:167], v[12:15], v[232:235], v[164:167]\n"
    "ds_read_b64_tr_b16 v[218:219], v250 offset:8704\n"
    "v_mul_f32 v176, 0xbfb8aa3b, v176\n"
    "s_waitcnt lgkmcnt(6)\n"
    "v_mfma_f32_16x16x32_bf16 v[168:171], v[8:11], v[236:239], v[168:171]\n"
    "ds_read_b64_tr_b16 v[220:221], v251 offset:8192\n"
    "v_mul_f32 v177, 0xbfb8aa3b, v177\n"
    "v_mfma_f32_16x16x32_bf16 v[172:175], v[12:15], v[236:239], v[172:175]\n"
    "ds_read_b64_tr_b16 v[222:223], v251 offset:8704\n"
    "v_mul_f32 v178, 0xbfb8aa3b, v178\n"
    "s_waitcnt lgkmcnt(6)\n"
    "v_mfma_f32_16x16x32_bf16 v[144:147], v[16:19], v[208:211], v[144:147]\n"
    "ds_read_b64_tr_b16 v[224:225], v250 offset:4096\n"
    "s_add_u32 s96, s96, 16384\n"
    "s_cmp_eq_u32 s96, 163840\n"
    "s_cselect_b32 s96, 0, s96\n"
    "v_mul_f32 v179, 0xbfb8aa3b, v179\n"
    "v_mfma_f32_16x16x32_bf16 v[148:151], v[20:23], v[208:211], v[148:151]\n"
    "ds_read_b64_tr_b16 v[226:227], v250 offset:4608\n"
    "s_waitcnt lgkmcnt(6)\n"
    "v_mfma_f32_16x16x32_bf16 v[152:155], v[16:19], v[212:215], v[152:155]\n"
    "ds_read_b64_tr_b16 v[228:229], v251 offset:4096\n"
    "v_exp_f32 v176, v176\n"
    "v_mfma_f32_16x16x32_bf16 v[156:159], v[20:23], v[212:215], v[156:159]\n"
    "ds_read_b64_tr_b16 v[230:231], v251 offset:4608\n"
    "v_exp_f32 v177, v177\n"
    "s_waitcnt lgkmcnt(6)\n"
    "v_mfma_f32_16x16x32_bf16 v[160:163], v[16:19], v[216:219], v[160:163]\n"
    "ds_read_b64_tr_b16 v[232:233], v250 offset:12288\n"
    "v_add_u32 v248, s96, v240\n"
    "v_add_u32 v249, s96, v241\n"
    "v_exp_f32 v178, v178\n"
    "v_mfma_f32_16x16x32_bf16 v[164:167], v[20:23], v[216:219], v[164:167]\n"
    "ds_read_b64_tr_b16 v[234:235], v250 offset:12800\n"
    "v_exp_f32 v179, v179\n"
    "s_waitcnt lgkmcnt(6)\n"
    "v_mfma_f32_16x16x32_bf16 v[168:171], v[16:19], v[220:223], v[168:171]\n"
    "ds_read_b64_tr_b16 v[236:237], v251 offset:12288\n"
    "v_mfma_f32_16x16x32_bf16 v[172:175], v[20:23], v[220:223], v[172:175]\n"
    "ds_read_b64_tr_b16 v[238:239], v251 offset:12800\n"
    "v_add_f32 v176, 1.0, v176\n"
    "s_waitcnt vmcnt(6)\n"
    "s_barrier\n"
    "s_waitcnt lgkmcnt(6)\n"
    "v_mfma_f32_16x16x32_bf16 v[144:147], v[24:27], v[224:227], v[144:147]\n"
    "ds_read_b64_tr_b16 v[208:209], v248 offset:0\n"
    "v_add_f32 v177, 1.0, v177\n"
    "v_mfma_f32_16x16x32_bf16 v[148:151], v[28:31], v[224:227], v[148:151]\n"
    "ds_read_b64_tr_b16 v[210:211], v248 offset:512\n"
    "s_add_u32 m0, s97, s73\n"
    "s_nop 0\n"
    "buffer_load_dwordx4 v242, s[40:43], s71 offen lds\n"
    "v_add_f32 v178, 1.0, v178\n"
    "s_waitcnt lgkmcnt(6)\n"
    "v_mfma_f32_16x16x32_bf16 v[152:155], v[24:27], v[228:231], v[152:155]\n"
    "ds_read_b64_tr_b16 v[212:213], v249 offset:0\n"
    "v_mfma_f32_16x16x32_bf16 v[156:159], v[28:31], v[228:231], v[156:159]\n"
    "ds_read_b64_tr_b16 v[214:215], v249 offset:512\n"
    "v_add_f32 v179, 1.0, v179\n"
    "s_waitcnt lgkmcnt(6)\n"
    "v_mfma_f32_16x16x32_bf16 v[160:163], v[24:27], v[232:235], v[160:163]\n"
    "ds_read_b64_tr_b16 v[216:217], v248 offset:8192\n"
    "v_rcp_f32 v176, v176\n"
    "v_mfma_f32_16x16x32_bf16 v[164:167], v[28:31], v[232:235], v[164:167]\n"
    "ds_read_b64_tr_b16 v[218:219], v248 offset:8704\n"
    "s_add_u32 m0, m0, 8192\n"
    "s_nop 0\n"
    "buffer_load_dwordx4 v242, s[44:47], s71 offen lds\n"
    "s_add_u32 s71, s71, s99\n"
    "s_add_u32 s97, s97, 16384\n"
    "s_cmp_eq_u32 s97, 163840\n"
    "s_cselect_b32 s97, 0, s97\n"
    "v_rcp_f32 v177, v177\n"
    "s_waitcnt lgkmcnt(6)\n"
    "v_mfma_f32_16x16x32_bf16 v[168:171], v[24:27], v[236:239], v[168:171]\n"
    "ds_read_b64_tr_b16 v[220:221], v249 offset:8192\n"
    "v_rcp_f32 v178, v178\n"
    "v_mfma_f32_16x16x32_bf16 v[172:175], v[28:31], v[236:239], v[172:175]\n"
    "ds_read_b64_tr_b16 v[222:223], v249 offset:8704\n"
    "s_waitcnt lgkmcnt(6)\n"
    "v_mfma_f32_16x16x32_bf16 v[144:147], v[32:35], v[208:211], v[144:147]\n"
    "ds_read_b64_tr_b16 v[224:225], v248 offset:4096\n"
    "s_add_u32 s96, s96, 16384\n"
    "s_cmp_eq_u32 s96, 163840\n"
    "s_cselect_b32 s96, 0, s96\n"
    "v_rcp_f32 v179, v179\n"
    "v_mfma_f32_16x16x32_bf16 v[148:151], v[36:39], v[208:211], v[148:151]\n"
    "ds_read_b64_tr_b16 v[226:227], v248 offset:4608\n"
    "s_add_u32 s78, s78, 128\n"
    "s_sub_u32 s76, s76, 1\n"
    "s_cmp_gt_i32 s76, s98\n"
    "s_cbranch_scc1 L1KEEP3%=\n"
    "s_cmp_eq_u32 s76, 2\n"
    "s_cbranch_scc0 L1SIDE3%=\n"
    "s_mov_b64 s[40:41], %4\n"
    "s_and_b32 s41, s41, 0xffff\n"
    "s_mov_b32 s42, 73728\n"
    "s_mov_b64 s[44:45], %6\n"
    "s_and_b32 s45, s45, 0xffff\n"
    "s_mov_b32 s46, 73728\n"
    "s_mov_b32 s99, 8192\n"
    "s_branch L1LIN3%=\n"
    "L1SIDE3%=:\n"
    "s_mov_b32 s40, s48\n"
    "s_mov_b32 s41, s49\n"
    "s_mov_b32 s42, s50\n"
    "s_mov_b32 s43, s51\n"
    "s_mov_b32 s44, s52\n"
    "s_mov_b32 s45, s53\n"
    "s_mov_b32 s46, s54\n"
    "s_mov_b32 s47, s55\n"
    "s_mov_b32 s99, %17\n"
    "s_mov_b32 s95, s97\n"
    "L1LIN3%=:\n"
    "v_mov_b32 v242, v243\n"
    "s_mov_b32 s78, 0\n"
    "L1KEEP3%=:\n"
    "s_mov_b32 s71, s78\n"
    "s_nop 3\n"
    "s_add_u32 m0, s97, s73\n"
    "s_nop 0\n"
    "buffer_load_dwordx4 v242, s[40:43], s71 offen lds\n"
    "v_mul_f32 v192, v192, v176\n"
    "s_waitcnt lgkmcnt(6)\n"
    "v_mfma_f32_16x16x32_bf16 v[152:155], v[32:35], v[212:215], v[152:155]\n"
    "ds_read_b64_tr_b16 v[228:229], v249 offset:4096\n"
    "v_mul_f32 v193, v193, v177\n"
    "v_mfma_f32_16x16x32_bf16 v[156:159], v[36:39], v[212:215], v[156:159]\n"
    "ds_read_b64_tr_b16 v[230:231], v249 offset:4608\n"
    "v_mul_f32 v194, v194, v178\n"
    "s_waitcnt lgkmcnt(6)\n"
    "v_mfma_f32_16x16x32_bf16 v[160:163], v[32:35], v[216:219], v[160:163]\n"
    "ds_read_b64_tr_b16 v[232:233], v248 offset:12288\n"
    "v_add_u32 v250, s96, v240\n"
    "v_add_u32 v251, s96, v241\n"
    "v_mfma_f32_16x16x32_bf16 v[164:167], v[36:39], v[216:219], v[164:167]\n"
    "ds_read_b64_tr_b16 v[234:235], v248 offset:12800\n"
    "s_add_u32 m0, m0, 8192\n"
    "s_nop 0\n"
    "buffer_load_dwordx4 v242, s[44:47], s71 offen lds\n"
    "s_add_u32 s71, s71, s99\n"
    "s_add_u32 s97, s97, 16384\n"
    "s_cmp_eq_u32 s97, 163840\n"
    "s_cselect_b32 s97, 0, s97\n"
    "v_mul_f32 v195, v195, v179\n"
    "s_waitcnt lgkmcnt(6)\n"
    "v_mfma_f32_16x16x32_bf16 v[168:171], v[32:35], v[220:223], v[168:171]\n"
    "ds_read_b64_tr_b16 v[236:237], v249 offset:12288\n"
    "v_mul_f32 v200, v184, v200\n"
    "v_mfma_f32_16x16x32_bf16 v[172:175], v[36:39], v[220:223], v[172:175]\n"
    "ds_read_b64_tr_b16 v[238:239], v249 offset:12800\n"
    "v_mul_f32 v201, v185, v201\n"
    "s_waitcnt lgkmcnt(6)\n"
    "v_mfma_f32_16x16x32_bf16 v[144:147], v[40:43], v[224:227], v[144:147]\n"
    "ds_read_b64_tr_b16 v[208:209], v250 offset:0\n"
    "v_mfma_f32_16x16x32_bf16 v[148:151], v[44:47], v[224:227], v[148:151]\n"
    "ds_read_b64_tr_b16 v[210:211], v250 offset:512\n"
    "s_add_u32 m0, s97, s73\n"
    "s_nop 0\n"
    "buffer_load_dwordx4 v242, s[40:43], s71 offen lds\n"
    "v_mul_f32 v202, v186, v202\n"
    "s_waitcnt lgkmcnt(6)\n"
    "v_mfma_f32_16x16x32_bf16 v[152:155], v[40:43], v[228:231], v[152:155]\n"
    "ds_read_b64_tr_b16 v[212:213], v251 offset:0\n"
    "v_mul_f32 v203, v187, v203\n"
    "v_mfma_f32_16x16x32_bf16 v[156:159], v[44:47], v[228:231], v[156:159]\n"
    "ds_read_b64_tr_b16 v[214:215], v251 offset:512\n"
    "v_mul_f32 v184, 0xbfb8aa3b, v184\n"
    "s_waitcnt lgkmcnt(6)\n"
    "v_mfma_f32_16x16x32_bf16 v[160:163], v[40:43], v[232:235], v[160:163]\n"
    "ds_read_b64_tr_b16 v[216:217], v250 offset:8192\n"
    "v_mul_f32 v185, 0xbfb8aa3b, v185\n"
    "v_mfma_f32_16x16x32_bf16 v[164:167], v[44:47], v[232:235], v[164:167]\n"
    "ds_read_b64_tr_b16 v[218:219], v250 offset:8704\n"
    "s_add_u32 m0, m0, 8192\n"
    "s_nop 0\n"
    "buffer_load_dwordx4 v242, s[44:47], s71 offen lds\n"
    "s_add_u32 s71, s71, s99\n"
    "s_add_u32 s97, s97, 16384\n"
    "s_cmp_eq_u32 s97, 163840\n"
    "s_cselect_b32 s97, 0, s97\n"
    "s_waitcnt lgkmcnt(6)\n"
    "v_mfma_f32_16x16x32_bf16 v[168:171], v[40:43], v[236:239], v[168:171]\n"
    "ds_read_b64_tr_b16 v[220:221], v251 offset:8192\n"
    "v_mul_f32 v186, 0xbfb8aa3b, v186\n"
    "v_mfma_f32_16x16x32_bf16 v[172:175], v[44:47], v[236:239], v[172:175]\n"
    "ds_read_b64_tr_b16 v[222:223], v251 offset:8704\n"
    "v_mul_f32 v187, 0xbfb8aa3b, v187\n"
    "s_waitcnt lgkmcnt(6)\n"
    "v_mfma_f32_16x16x32_bf16 v[144:147], v[48:51], v[208:211], v[144:147]\n"
    "ds_read_b64_tr_b16 v[224:225], v250 offset:4096\n"
    "s_add_u32 s96, s96, 16384\n"
    "s_cmp_eq_u32 s96, 163840\n"
    "s_cselect_b32 s96, 0, s96\n"
    "v_exp_f32 v184, v184\n"
    "v_mfma_f32_16x16x32_bf16 v[148:151], v[52:55], v[208:211], v[148:151]\n"
    "ds_read_b64_tr_b16 v[226:227], v250 offset:4608\n"
    "v_exp_f32 v185, v185\n"
    "s_waitcnt lgkmcnt(6)\n"
    "v_mfma_f32_16x16x32_bf16 v[152:155], v[48:51], v[212:215], v[152:155]\n"
    "ds_read_b64_tr_b16 v[228:229], v251 offset:4096\n"
    "v_mfma_f32_16x16x32_bf16 v[156:159], v[52:55], v[212:215], v[156:159]\n"
    "ds_read_b64_tr_b16 v[230:231], v251 offset:4608\n"
    "v_exp_f32 v186, v186\n"
    "s_waitcnt lgkmcnt(6)\n"
    "v_mfma_f32_16x16x32_bf16 v[160:163], v[48:51], v[216:219], v[160:163]\n"
    "ds_read_b64_tr_b16 v[232:233], v250 offset:12288\n"
    "v_add_u32 v248, s96, v240\n"
    "v_add_u32 v249, s96, v241\n"
    "v_exp_f32 v187, v187\n"
    "v_mfma_f32_16x16x32_bf16 v[164:167], v[52:55], v[216:219], v[164:167]\n"
    "ds_read_b64_tr_b16 v[234:235], v250 offset:12800\n"
    "v_add_f32 v184, 1.0, v184\n"
    "s_waitcnt lgkmcnt(6)\n"
    "v_mfma_f32_16x16x32_bf16 v[168:171], v[48:51], v[220:223], v[168:171]\n"
    "ds_read_b64_tr_b16 v[236:237], v251 offset:12288\n"
    "v_mfma_f32_16x16x32_bf16 v[172:175], v[52:55], v[220:223], v[172:175]\n"
    "ds_read_b64_tr_b16 v[238:239], v251 offset:12800\n"
    "v_add_f32 v185, 1.0, v185\n"
    "s_waitcnt lgkmcnt(6)\n"
    "v_mfma_f32_16x16x32_bf16 v[144:147], v[56:59], v[224:227], v[144:147]\n"
    "ds_read_b64_tr_b16 v[208:209], v248 offset:0\n"
    "v_add_f32 v186, 1.0, v186\n"
    "v_mfma_f32_16x16x32_bf16 v[148:151], v[60:63], v[224:227], v[148:151]\n"
    "ds_read_b64_tr_b16 v[210:211], v248 offset:512\n"
    "v_add_f32 v187, 1.0, v187\n"
    "s_waitcnt lgkmcnt(6)\n"
    "v_mfma_f32_16x16x32_bf16 v[152:155], v[56:59], v[228:231], v[152:155]\n"
    "ds_read_b64_tr_b16 v[212:213], v249 offset:0\n"
    "v_rcp_f32 v184, v184\n"
    "v_mfma_f32_16x16x32_bf16 v[156:159], v[60:63], v[228:231], v[156:159]\n"
    "ds_read_b64_tr_b16 v[214:215], v249 offset:512\n"
    "s_waitcnt lgkmcnt(6)\n"
    "v_mfma_f32_16x16x32_bf16 v[160:163], v[56:59], v[232:235], v[160:163]\n"
    "ds_read_b64_tr_b16 v[216:217], v248 offset:8192\n"
    "v_rcp_f32 v185, v185\n"
    "v_mfma_f32_16x16x32_bf16 v[164:167], v[60:63], v[232:235], v[164:167]\n"
    "ds_read_b64_tr_b16 v[218:219], v248 offset:8704\n"
    "v_rcp_f32 v186, v186\n"
    "s_waitcnt lgkmcnt(6)\n"
    "v_mfma_f32_16x16x32_bf16 v[168:171], v[56:59], v[236:239], v[168:171]\n"
    "ds_read_b64_tr_b16 v[220:221], v249 offset:8192\n"
    "v_rcp_f32 v187, v187\n"
    "v_mfma_f32_16x16x32_bf16 v[172:175], v[60:63], v[236:239], v[172:175]\n"
    "ds_read_b64_tr_b16 v[222:223], v249 offset:8704\n"
    "v_mul_f32 v200, v200, v184\n"
    "s_waitcnt lgkmcnt(6)\n"
    "v_mfma_f32_16x16x32_bf16 v[144:147], v[64:67], v[208:211], v[144:147]\n"
    "ds_read_b64_tr_b16 v[224:225], v248 offset:4096\n"
    "s_add_u32 s96, s96, 16384\n"
    "s_cmp_eq_u32 s96, 163840\n"
    "s_cselect_b32 s96, 0, s96\n"
    "v_mfma_f32_16x16x32_bf16 v[148:151], v[68:71], v[208:211], v[148:151]\n"
    "ds_read_b64_tr_b16 v[226:227], v248 offset:4608\n"
    "v_mul_f32 v201, v201, v185\n"
    "s_waitcnt lgkmcnt(6)\n"
    "v_mfma_f32_16x16x32_bf16 v[152:155], v[64:67], v[212:215], v[152:155]\n"
    "ds_read_b64_tr_b16 v[228:229], v249 offset:4096\n"
    "v_mul_f32 v202, v202, v186\n"
    "v_mfma_f32_16x16x32_bf16 v[156:159], v[68:71], v[212:215], v[156:159]\n"
    "ds_read_b64_tr_b16 v[230:231], v249 offset:4608\n"
    "v_mul_f32 v203, v203, v187\n"
    "s_waitcnt lgkmcnt(6)\n"
    "v_mfma_f32_16x16x32_bf16 v[160:163], v[64:67], v[216:219], v[160:163]\n"
    "ds_read_b64_tr_b16 v[232:233], v248 offset:12288\n"
    "v_add_u32 v250, s96, v240\n"
    "v_add_u32 v251, s96, v241\n"
    "v_mfma_f32_16x16x32_bf16 v[164:167], v[68:71], v[216:219], v[164:167]\n"
    "ds_read_b64_tr_b16 v[234:235], v248 offset:12800\n"
    "v_cvt_pk_bf16_f32 v176, v192, v193\n"
    "s_waitcnt lgkmcnt(6)\n"
    "v_mfma_f32_16x16x32_bf16 v[168:171], v[64:67], v[220:223], v[168:171]\n"
    "ds_read_b64_tr_b16 v[236:237], v249 offset:12288\n"
    "v_cvt_pk_bf16_f32 v177, v194, v195\n"
    "v_mfma_f32_16x16x32_bf16 v[172:175], v[68:71], v[220:223], v[172:175]\n"
    "ds_read_b64_tr_b16 v[238:239], v249 offset:12800\n"
    "v_cvt_pk_bf16_f32 v178, v200, v201\n"
    "s_waitcnt vmcnt(6)\n"
    "s_barrier\n"
    "s_waitcnt lgkmcnt(6)\n"
    "v_mfma_f32_16x16x32_bf16 v[144:147], v[72:75], v[224:227], v[144:147]\n"
    "ds_read_b64_tr_b16 v[208:209], v250 offset:0\n"
    "v_cvt_pk_bf16_f32 v179, v202, v203\n"
    "v_mfma_f32_16x16x32_bf16 v[148:151], v[76:79], v[224:227], v[148:151]\n"
    "ds_read_b64_tr_b16 v[210:211], v250 offset:512\n"
    "s_add_u32 m0, s97, s73\n"
    "s_nop 0\n"
    "buffer_load_dwordx4 v242, s[40:43], s71 offen lds\n"
    "s_waitcnt lgkmcnt(6)\n"
    "v_mfma_f32_16x16x32_bf16 v[152:155], v[72:75], v[228:231], v[152:155]\n"
    "ds_read_b64_tr_b16 v[212:213], v251 offset:0\n"
    "buffer_store_dwordx4 v[176:179], v252, s[56:59], s75 offen offset:0 sc0 sc1\n"
    "v_mfma_f32_16x16x32_bf16 v[156:159], v[76:79], v[228:231], v[156:159]\n"
    "ds_read_b64_tr_b16 v[214:215], v251 offset:512\n"
    "v_mul_f32 v196, v180, v196\n"
    "s_waitcnt lgkmcnt(6)\n"
    "v_mfma_f32_16x16x32_bf16 v[160:163], v[72:75], v[232:235], v[160:163]\n"
    "ds_read_b64_tr_b16 v[216:217], v250 offset:8192\n"
    "v_mul_f32 v197, v181, v197\n"
    "v_mfma_f32_16x16x32_bf16 v[164:167], v[76:79], v[232:235], v[164:167]\n"
    "ds_read_b64_tr_b16 v[218:219], v250 offset:8704\n"
    "s_add_u32 m0, m0, 8192\n"
    "s_nop 0\n"
    "buffer_load_dwordx4 v242, s[44:47], s71 offen lds\n"
    "s_add_u32 s71, s71, s99\n"
    "s_add_u32 s97, s97, 16384\n"
    "s_cmp_eq_u32 s97, 163840\n"
    "s_cselect_b32 s97, 0, s97\n"
    "v_mul_f32 v198, v182, v198\n"
    "s_waitcnt lgkmcnt(6)\n"
    "v_mfma_f32_16x16x32_bf16 v[168:171], v[72:75], v[236:239], v[168:171]\n"
    "ds_read_b64_tr_b16 v[220:221], v251 offset:8192\n"
    "v_mfma_f32_16x16x32_bf16 v[172:175], v[76:79], v[236:239], v[172:175]\n"
    "ds_read_b64_tr_b16 v[222:223], v251 offset:8704\n"
    "v_mul_f32 v199, v183, v199\n"
    "s_waitcnt lgkmcnt(6)\n"
    "v_mfma_f32_16x16x32_bf16 v[144:147], v[80:83], v[208:211], v[144:147]\n"
    "ds_read_b64_tr_b16 v[224:225], v250 offset:4096\n"
    "s_add_u32 s96, s96, 16384\n"
    "s_cmp_eq_u32 s96, 163840\n"
    "s_cselect_b32 s96, 0, s96\n"
    "v_mul_f32 v180, 0xbfb8aa3b, v180\n"
    "v_mfma_f32_16x16x32_bf16 v[148:151], v[84:87], v[208:211], v[148:151]\n"
    "ds_read_b64_tr_b16 v[226:227], v250 offset:4608\n"
    "s_add_u32 m0, s97, s73\n"
    "s_nop 0\n"
    "buffer_load_dwordx4 v242, s[40:43], s71 offen lds\n"
    "v_mul_f32 v181, 0xbfb8aa3b, v181\n"
    "s_waitcnt lgkmcnt(6)\n"
    "v_mfma_f32_16x16x32_bf16 v[152:155], v[80:83], v[212:215], v[152:155]\n"
    "ds_read_b64_tr_b16 v[228:229], v251 offset:4096\n"
    "v_mul_f32 v182, 0xbfb8aa3b, v182\n"
    "v_mfma_f32_16x16x32_bf16 v[156:159], v[84:87], v[212:215], v[156:159]\n"
    "ds_read_b64_tr_b16 v[230:231], v251 offset:4608\n"
    "s_waitcnt lgkmcnt(6)\n"
    "v_mfma_f32_16x16x32_bf16 v[160:163], v[80:83], v[216:219], v[160:163]\n"
    "ds_read_b64_tr_b16 v[232:233], v250 offset:12288\n"
    "v_add_u32 v248, s96, v240\n"
    "v_add_u32 v249, s96, v241\n"
    "v_mul_f32 v183, 0xbfb8aa3b, v183\n"
    "v_mfma_f32_16x16x32_bf16 v[164:167], v[84:87], v[216:219], v[164:167]\n"
    "ds_read_b64_tr_b16 v[234:235], v250 offset:12800\n"
    "s_add_u32 m0, m0, 8192\n"
    "s_nop 0\n"
    "buffer_load_dwordx4 v242, s[44:47], s71 offen lds\n"
    "s_add_u32 s71, s71, s99\n"
    "s_add_u32 s97, s97, 16384\n"
    "s_cmp_eq_u32 s97, 163840\n"
    "s_cselect_b32 s97, 0, s97\n"
    "v_exp_f32 v180, v180\n"
    "s_waitcnt lgkmcnt(6)\n"
    "v_mfma_f32_16x16x32_bf16 v[168:171], v[80:83], v[220:223], v[168:171]\n"
    "ds_read_b64_tr_b16 v[236:237], v251 offset:12288\n"
    "v_exp_f32 v181, v181\n"
    "v_mfma_f32_16x16x32_bf16 v[172:175], v[84:87], v[220:223], v[172:175]\n"
    "ds_read_b64_tr_b16 v[238:239], v251 offset:12800\n"
    "s_waitcnt lgkmcnt(6)\n"
    "v_mfma_f32_16x16x32_bf16 v[144:147], v[88:91], v[224:227], v[144:147]\n"
    "ds_read_b64_tr_b16 v[208:209], v248 offset:0\n"
    "v_exp_f32 v182, v182\n"
    "v_mfma_f32_16x16x32_bf16 v[148:151], v[92:95], v[224:227], v[148:151]\n"
    "ds_read_b64_tr_b16 v[210:211], v248 offset:512\n"
    "s_add_u32 m0, s97, s73\n"
    "s_nop 0\n"
    "buffer_load_dwordx4 v242, s[40:43], s71 offen lds\n"
    "v_exp_f32 v183, v183\n"
    "s_waitcnt lgkmcnt(6)\n"
    "v_mfma_f32_16x16x32_bf16 v[152:155], v[88:91], v[228:231], v[152:155]\n"
    "ds_read_b64_tr_b16 v[212:213], v249 offset:0\n"
    "v_add_f32 v180, 1.0, v180\n"
    "v_mfma_f32_16x16x32_bf16 v[156:159], v[92:95], v[228:231], v[156:159]\n"
    "ds_read_b64_tr_b16 v[214:215], v249 offset:512\n"
    "v_add_f32 v181, 1.0, v181\n"
    "s_waitcnt lgkmcnt(6)\n"
    "v_mfma_f32_16x16x32_bf16 v[160:163], v[88:91], v[232:235], v[160:163]\n"
    "ds_read_b64_tr_b16 v[216:217], v248 offset:8192\n"
    "v_mfma_f32_16x16x32_bf16 v[164:167], v[92:95], v[232:235], v[164:167]\n"
    "ds_read_b64_tr_b16 v[218:219], v248 offset:8704\n"
    "s_add_u32 m0, m0, 8192\n"
    "s_nop 0\n"
    "buffer_load_dwordx4 v242, s[44:47], s71 offen lds\n"
    "s_add_u32 s71, s71, s99\n"
    "s_add_u32 s97, s97, 16384\n"
    "s_cmp_eq_u32 s97, 163840\n"
    "s_cselect_b32 s97, 0, s97\n"
    "v_add_f32 v182, 1.0, v182\n"
    "s_waitcnt lgkmcnt(6)\n"
    "v_mfma_f32_16x16x32_bf16 v[168:171], v[88:91], v[236:239], v[168:171]\n"
    "ds_read_b64_tr_b16 v[220:221], v249 offset:8192\n"
    "v_add_f32 v183, 1.0, v183\n"
    "v_mfma_f32_16x16x32_bf16 v[172:175], v[92:95], v[236:239], v[172:175]\n"
    "ds_read_b64_tr_b16 v[222:223], v249 offset:8704\n"
    "v_rcp_f32 v180, v180\n"
    "s_waitcnt lgkmcnt(6)\n"
    "v_mfma_f32_16x16x32_bf16 v[144:147], v[96:99], v[208:211], v[144:147]\n"
    "ds_read_b64_tr_b16 v[224:225], v248 offset:4096\n"
    "s_add_u32 s96, s96, 16384\n"
    "s_cmp_eq_u32 s96, 163840\n"
    "s_cselect_b32 s96, 0, s96\n"
    "v_rcp_f32 v181, v181\n"
    "v_mfma_f32_16x16x32_bf16 v[148:151], v[100:103], v[208:211], v[148:151]\n"
    "ds_read_b64_tr_b16 v[226:227], v248 offset:4608\n"
    "s_waitcnt lgkmcnt(6)\n"
    "v_mfma_f32_16x16x32_bf16 v[152:155], v[96:99], v[212:215], v[152:155]\n"
    "ds_read_b64_tr_b16 v[228:229], v249 offset:4096\n"
    "v_rcp_f32 v182, v182\n"
    "v_mfma_f32_16x16x32_bf16 v[156:159], v[100:103], v[212:215], v[156:159]\n"
    "ds_read_b64_tr_b16 v[230:231], v249 offset:4608\n"
    "v_rcp_f32 v183, v183\n"
    "s_waitcnt lgkmcnt(6)\n"
    "v_mfma_f32_16x16x32_bf16 v[160:163], v[96:99], v[216:219], v[160:163]\n"
    "ds_read_b64_tr_b16 v[232:233], v248 offset:12288\n"
    "v_add_u32 v250, s96, v240\n"
    "v_add_u32 v251, s96, v241\n"
    "v_mul_f32 v196, v196, v180\n"
    "v_mfma_f32_16x16x32_bf16 v[164:167], v[100:103], v[216:219], v[164:167]\n"
    "ds_read_b64_tr_b16 v[234:235], v248 offset:12800\n"
    "s_waitcnt lgkmcnt(6)\n"
    "v_mfma_f32_16x16x32_bf16 v[168:171], v[96:99], v[220:223], v[168:171]\n"
    "ds_read_b64_tr_b16 v[236:237], v249 offset:12288\n"
    "v_mul_f32 v197, v197, v181\n"
    "v_mfma_f32_16x16x32_bf16 v[172:175], v[100:103], v[220:223], v[172:175]\n"
    "ds_read_b64_tr_b16 v[238:239], v249 offset:12800\n"
    "v_mul_f32 v198, v198, v182\n"
    "s_waitcnt lgkmcnt(6)\n"
    "v_mfma_f32_16x16x32_bf16 v[144:147], v[104:107], v[224:227], v[144:147]\n"
    "ds_read_b64_tr_b16 v[208:209], v250 offset:0\n"
    "v_mul_f32 v199, v199, v183\n"
    "v_mfma_f32_16x16x32_bf16 v[148:151], v[108:111], v[224:227], v[148:151]\n"
    "ds_read_b64_tr_b16 v[210:211], v250 offset:512\n"
    "v_mul_f32 v204, v188, v204\n"
    "s_waitcnt lgkmcnt(6)\n"
    "v_mfma_f32_16x16x32_bf16 v[152:155], v[104:107], v[228:231], v[152:155]\n"
    "ds_read_b64_tr_b16 v[212:213], v251 offset:0\n"
    "v_mfma_f32_16x16x32_bf16 v[156:159], v[108:111], v[228:231], v[156:159]\n"
    "ds_read_b64_tr_b16 v[214:215], v251 offset:512\n"
    "v_mul_f32 v205, v189, v205\n"
    "s_waitcnt lgkmcnt(6)\n"
    "v_mfma_f32_16x16x32_bf16 v[160:163], v[104:107], v[232:235], v[160:163]\n"
    "ds_read_b64_tr_b16 v[216:217], v250 offset:8192\n"
    "v_mul_f32 v206, v190, v206\n"
    "v_mfma_f32_16x16x32_bf16 v[164:167], v[108:111], v[232:235], v[164:167]\n"
    "ds_read_b64_tr_b16 v[218:219], v250 offset:8704\n"
    "v_mul_f32 v207, v191, v207\n"
    "s_waitcnt lgkmcnt(6)\n"
    "v_mfma_f32_16x16x32_bf16 v[168:171], v[104:107], v[236:239], v[168:171]\n"
    "ds_read_b64_tr_b16 v[220:221], v251 offset:8192\n"
    "v_mul_f32 v188, 0xbfb8aa3b, v188\n"
    "v_mfma_f32_16x16x32_bf16 v[172:175], v[108:111], v[236:239], v[172:175]\n"
    "ds_read_b64_tr_b16 v[222:223], v251 offset:8704\n"
    "s_waitcnt lgkmcnt(6)\n"
    "v_mfma_f32_16x16x32_bf16 v[144:147], v[112:115], v[208:211], v[144:147]\n"
    "ds_read_b64_tr_b16 v[224:225], v250 offset:4096\n"
    "s_add_u32 s96, s96, 16384\n"
    "s_cmp_eq_u32 s96, 163840\n"
    "s_cselect_b32 s96, 0, s96\n"
    "v_mul_f32 v189, 0xbfb8aa3b, v189\n"
    "v_mfma_f32_16x16x32_bf16 v[148:151], v[116:119], v[208:211], v[148:151]\n"
    "ds_read_b64_tr_b16 v[226:227], v250 offset:4608\n"
    "v_mul_f32 v190, 0xbfb8aa3b, v190\n"
    "s_waitcnt lgkmcnt(6)\n"
    "v_mfma_f32_16x16x32_bf16 v[152:155], v[112:115], v[212:215], v[152:155]\n"
    "ds_read_b64_tr_b16 v[228:229], v251 offset:4096\n"
    "v_mul_f32 v191, 0xbfb8aa3b, v191\n"
    "v_mfma_f32_16x16x32_bf16 v[156:159], v[116:119], v[212:215], v[156:159]\n"
    "ds_read_b64_tr_b16 v[230:231], v251 offset:4608\n"
    "s_waitcnt lgkmcnt(6)\n"
    "v_mfma_f32_16x16x32_bf16 v[160:163], v[112:115], v[216:219], v[160:163]\n"
    "ds_read_b64_tr_b16 v[232:233], v250 offset:12288\n"
    "v_add_u32 v248, s96, v240\n"
    "v_add_u32 v249, s96, v241\n"
    "v_exp_f32 v188, v188\n"
    "v_mfma_f32_16x16x32_bf16 v[164:167], v[116:119], v[216:219], v[164:167]\n"
    "ds_read_b64_tr_b16 v[234:235], v250 offset:12800\n"
    "v_exp_f32 v189, v189\n"
    "s_waitcnt lgkmcnt(6)\n"
    "v_mfma_f32_16x16x32_bf16 v[168:171], v[112:115], v[220:223], v[168:171]\n"
    "ds_read_b64_tr_b16 v[236:237], v251 offset:12288\n"
    "v_exp_f32 v190, v190\n"
    "v_mfma_f32_16x16x32_bf16 v[172:175], v[116:119], v[220:223], v[172:175]\n"
    "ds_read_b64_tr_b16 v[238:239], v251 offset:12800\n"
    "v_exp_f32 v191, v191\n"
    "s_waitcnt vmcnt(7)\n"
    "s_barrier\n"
    "s_waitcnt lgkmcnt(6)\n"
    "v_mfma_f32_16x16x32_bf16 v[144:147], v[120:123], v[224:227], v[144:147]\n"
    "ds_read_b64_tr_b16 v[208:209], v248 offset:0\n"
    "v_mfma_f32_16x16x32_bf16 v[148:151], v[124:127], v[224:227], v[148:151]\n"
    "ds_read_b64_tr_b16 v[210:211], v248 offset:512\n"
    "s_add_u32 m0, s97, s73\n"
    "s_nop 0\n"
    "buffer_load_dwordx4 v242, s[40:43], s71 offen lds\n"
    "v_add_f32 v188, 1.0, v188\n"
    "s_waitcnt lgkmcnt(6)\n"
    "v_mfma_f32_16x16x32_bf16 v[152:155], v[120:123], v[228:231], v[152:155]\n"
    "ds_read_b64_tr_b16 v[212:213], v249 offset:0\n"
    "v_add_f32 v189, 1.0, v189\n"
    "v_mfma_f32_16x16x32_bf16 v[156:159], v[124:127], v[228:231], v[156:159]\n"
    "ds_read_b64_tr_b16 v[214:215], v249 offset:512\n"
    "v_add_f32 v190, 1.0, v190\n"
    "s_waitcnt lgkmcnt(6)\n"
    "v_mfma_f32_16x16x32_bf16 v[160:163], v[120:123], v[232:235], v[160:163]\n"
    "ds_read_b64_tr_b16 v[216:217], v248 offset:8192\n"
    "v_add_f32 v191, 1.0, v191\n"
    "v_mfma_f32_16x16x32_bf16 v[164:167], v[124:127], v[232:235], v[164:167]\n"
    "ds_read_b64_tr_b16 v[218:219], v248 offset:8704\n"
    "s_add_u32 m0, m0, 8192\n"
    "s_nop 0\n"
    "buffer_load_dwordx4 v242, s[44:47], s71 offen lds\n"
    "s_add_u32 s71, s71, s99\n"
    "s_add_u32 s97, s97, 16384\n"
    "s_cmp_eq_u32 s97, 163840\n"
    "s_cselect_b32 s97, 0, s97\n"
    "s_waitcnt lgkmcnt(6)\n"
    "v_mfma_f32_16x16x32_bf16 v[168:171], v[120:123], v[236:239], v[168:171]\n"
    "ds_read_b64_tr_b16 v[220:221], v249 offset:8192\n"
    "v_rcp_f32 v188, v188\n"
    "v_mfma_f32_16x16x32_bf16 v[172:175], v[124:127], v[236:239], v[172:175]\n"
    "ds_read_b64_tr_b16 v[222:223], v249 offset:8704\n"
    "v_rcp_f32 v189, v189\n"
    "s_waitcnt lgkmcnt(6)\n"
    "v_mfma_f32_16x16x32_bf16 v[144:147], v[128:131], v[208:211], v[144:147]\n"
    "ds_read_b64_tr_b16 v[224:225], v248 offset:4096\n"
    "s_add_u32 s96, s96, 16384\n"
    "s_cmp_eq_u32 s96, 163840\n"
    "s_cselect_b32 s96, 0, s96\n"
    "v_rcp_f32 v190, v190\n"
    "v_mfma_f32_16x16x32_bf16 v[148:151], v[132:135], v[208:211], v[148:151]\n"
    "ds_read_b64_tr_b16 v[226:227], v248 offset:4608\n"
    "s_add_u32 m0, s97, s73\n"
    "s_nop 0\n"
    "buffer_load_dwordx4 v242, s[40:43], s71 offen lds\n"
    "s_waitcnt lgkmcnt(6)\n"
    "v_mfma_f32_16x16x32_bf16 v[152:155], v[128:131], v[212:215], v[152:155]\n"
    "ds_read_b64_tr_b16 v[228:229], v249 offset:4096\n"
    "v_rcp_f32 v191, v191\n"
    "v_mfma_f32_16x16x32_bf16 v[156:159], v[132:135], v[212:215], v[156:159]\n"
    "ds_read_b64_tr_b16 v[230:231], v249 offset:4608\n"
    "v_mul_f32 v204, v204, v188\n"
    "s_waitcnt lgkmcnt(6)\n"
    "v_mfma_f32_16x16x32_bf16 v[160:163], v[128:131], v[216:219], v[160:163]\n"
    "ds_read_b64_tr_b16 v[232:233], v248 offset:12288\n"
    "v_add_u32 v250, s96, v240\n"
    "v_add_u32 v251, s96, v241\n"
    "v_mul_f32 v205, v205, v189\n"
    "v_mfma_f32_16x16x32_bf16 v[164:167], v[132:135], v[216:219], v[164:167]\n"
    "ds_read_b64_tr_b16 v[234:235], v248 offset:12800\n"
    "s_add_u32 m0, m0, 8192\n"
    "s_nop 0\n"
    "buffer_load_dwordx4 v242, s[44:47], s71 offen lds\n"
    "s_add_u32 s71, s71, s99\n"
    "s_add_u32 s97, s97, 16384\n"
    "s_cmp_eq_u32 s97, 163840\n"
    "s_cselect_b32 s97, 0, s97\n"
    "v_mul_f32 v206, v206, v190\n"
    "s_waitcnt lgkmcnt(6)\n"
    "v_mfma_f32_16x16x32_bf16 v[168:171], v[128:131], v[220:223], v[168:171]\n"
    "ds_read_b64_tr_b16 v[236:237], v249 offset:12288\n"
    "v_mfma_f32_16x16x32_bf16 v[172:175], v[132:135], v[220:223], v[172:175]\n"
    "ds_read_b64_tr_b16 v[238:239], v249 offset:12800\n"
    "v_mul_f32 v207, v207, v191\n"
    "s_waitcnt lgkmcnt(6)\n"
    "v_mfma_f32_16x16x32_bf16 v[144:147], v[136:139], v[224:227], v[144:147]\n"
    "ds_read_b64_tr_b16 v[208:209], v250 offset:0\n"
    "v_cvt_pk_bf16_f32 v180, v196, v197\n"
    "v_mfma_f32_16x16x32_bf16 v[148:151], v[140:143], v[224:227], v[148:151]\n"
    "ds_read_b64_tr_b16 v[210:211], v250 offset:512\n"
    "s_add_u32 m0, s97, s73\n"
    "s_nop 0\n"
    "buffer_load_dwordx4 v242, s[40:43], s71 offen lds\n"
    "v_cvt_pk_bf16_f32 v181, v198, v199\n"
    "s_waitcnt lgkmcnt(6)\n"
    "v_mfma_f32_16x16x32_bf16 v[152:155], v[136:139], v[228:231], v[152:155]\n"
    "ds_read_b64_tr_b16 v[212:213], v251 offset:0\n"
    "v_cvt_pk_bf16_f32 v182, v204, v205\n"
    "v_mfma_f32_16x16x32_bf16 v[156:159], v[140:143], v[228:231], v[156:159]\n"
    "ds_read_b64_tr_b16 v[214:215], v251 offset:512\n"
    "s_waitcnt lgkmcnt(6)\n"
    "v_mfma_f32_16x16x32_bf16 v[160:163], v[136:139], v[232:235], v[160:163]\n"
    "ds_read_b64_tr_b16 v[216:217], v250 offset:8192\n"
    "v_cvt_pk_bf16_f32 v183, v206, v207\n"
    "v_mfma_f32_16x16x32_bf16 v[164:167], v[140:143], v[232:235], v[164:167]\n"
    "ds_read_b64_tr_b16 v[218:219], v250 offset:8704\n"
    "s_add_u32 m0, m0, 8192\n"
    "s_nop 0\n"
    "buffer_load_dwordx4 v242, s[44:47], s71 offen lds\n"
    "s_add_u32 s71, s71, s99\n"
    "s_add_u32 s97, s97, 16384\n"
    "s_cmp_eq_u32 s97, 163840\n"
    "s_cselect_b32 s97, 0, s97\n"
    "buffer_store_dwordx4 v[180:183], v252, s[56:59], s75 offen offset:1024 sc0 sc1\n"
    "s_waitcnt lgkmcnt(6)\n"
    "v_mfma_f32_16x16x32_bf16 v[168:171], v[136:139], v[236:239], v[168:171]\n"
    "ds_read_b64_tr_b16 v[220:221], v251 offset:8192\n"
    "s_add_u32 s75, s75, 16384\n"
    "v_mfma_f32_16x16x32_bf16 v[172:175], v[140:143], v[236:239], v[172:175]\n"
    "ds_read_b64_tr_b16 v[222:223], v251 offset:8704\n"
    "s_nop 7\n"
    "v_cvt_pk_bf16_f32 v144, v144, v145\n"
    "v_cvt_pk_bf16_f32 v145, v146, v147\n"
    "v_cvt_pk_bf16_f32 v146, v148, v149\n"
    "v_cvt_pk_bf16_f32 v147, v150, v151\n"
    "s_add_u32 s68, s79, 0\n"
    "buffer_store_dwordx4 v[144:147], v253, s[60:63], s68 offen sc0 sc1\n"
    "v_cvt_pk_bf16_f32 v152, v152, v153\n"
    "v_cvt_pk_bf16_f32 v153, v154, v155\n"
    "v_cvt_pk_bf16_f32 v154, v156, v157\n"
    "v_cvt_pk_bf16_f32 v155, v158, v159\n"
    "s_add_u32 s68, s79, 65536\n"
    "buffer_store_dwordx4 v[152:155], v253, s[60:63], s68 offen sc0 sc1\n"
    "v_cvt_pk_bf16_f32 v160, v160, v161\n"
    "v_cvt_pk_bf16_f32 v161, v162, v163\n"
    "v_cvt_pk_bf16_f32 v162, v164, v165\n"
    "v_cvt_pk_bf16_f32 v163, v166, v167\n"
    "s_add_u32 s68, s79, 262144\n"
    "buffer_store_dwordx4 v[160:163], v253, s[60:63], s68 offen sc0 sc1\n"
    "v_cvt_pk_bf16_f32 v168, v168, v169\n"
    "v_cvt_pk_bf16_f32 v169, v170, v171\n"
    "v_cvt_pk_bf16_f32 v170, v172, v173\n"
    "v_cvt_pk_bf16_f32 v171, v174, v175\n"
    "s_add_u32 s68, s79, 327680\n"
    "buffer_store_dwordx4 v[168:171], v253, s[60:63], s68 offen sc0 sc1\n"
    "s_branch L1END%=\n"
    "L1DRA%=:\n"
    "s_nop 7\n"
    "v_mul_f32 v160, v144, v160\n"
    "v_mul_f32 v161, v145, v161\n"
    "v_mul_f32 v162, v146, v162\n"
    "v_mul_f32 v163, v147, v163\n"
    "v_mul_f32 v144, 0xbfb8aa3b, v144\n"
    "v_mul_f32 v145, 0xbfb8aa3b, v145\n"
    "v_mul_f32 v146, 0xbfb8aa3b, v146\n"
    "v_mul_f32 v147, 0xbfb8aa3b, v147\n"
    "v_exp_f32 v144, v144\n"
    "v_exp_f32 v145, v145\n"
    "v_exp_f32 v146, v146\n"
    "v_exp_f32 v147, v147\n"
    "v_add_f32 v144, 1.0, v144\n"
    "v_add_f32 v145, 1.0, v145\n"
    "v_add_f32 v146, 1.0, v146\n"
    "v_add_f32 v147, 1.0, v147\n"
    "v_rcp_f32 v144, v144\n"
    "v_rcp_f32 v145, v145\n"
    "v_rcp_f32 v146, v146\n"
    "v_rcp_f32 v147, v147\n"
    "v_mul_f32 v160, v160, v144\n"
    "v_mul_f32 v161, v161, v145\n"
    "v_mul_f32 v162, v162, v146\n"
    "v_mul_f32 v163, v163, v147\n"
    "v_mul_f32 v168, v152, v168\n"
    "v_mul_f32 v169, v153, v169\n"
    "v_mul_f32 v170, v154, v170\n"
    "v_mul_f32 v171, v155, v171\n"
    "v_mul_f32 v152, 0xbfb8aa3b, v152\n"
    "v_mul_f32 v153, 0xbfb8aa3b, v153\n"
    "v_mul_f32 v154, 0xbfb8aa3b, v154\n"
    "v_mul_f32 v155, 0xbfb8aa3b, v155\n"
    "v_exp_f32 v152, v152\n"
    "v_exp_f32 v153, v153\n"
    "v_exp_f32 v154, v154\n"
    "v_exp_f32 v155, v155\n"
    "v_add_f32 v152, 1.0, v152\n"
    "v_add_f32 v153, 1.0, v153\n"
    "v_add_f32 v154, 1.0, v154\n"
    "v_add_f32 v155, 1.0, v155\n"
    "v_rcp_f32 v152, v152\n"
    "v_rcp_f32 v153, v153\n"
    "v_rcp_f32 v154, v154\n"
    "v_rcp_f32 v155, v155\n"
    "v_mul_f32 v168, v168, v152\n"
    "v_mul_f32 v169, v169, v153\n"
    "v_mul_f32 v170, v170, v154\n"
    "v_mul_f32 v171, v171, v155\n"
    "v_cvt_pk_bf16_f32 v144, v160, v161\n"
    "v_cvt_pk_bf16_f32 v145, v162, v163\n"
    "v_cvt_pk_bf16_f32 v146, v168, v169\n"
    "v_cvt_pk_bf16_f32 v147, v170, v171\n"
    "buffer_store_dwordx4 v[144:147], v252, s[56:59], s75 offen offset:0 sc0 sc1\n"
    "v_mul_f32 v164, v148, v164\n"
    "v_mul_f32 v165, v149, v165\n"
    "v_mul_f32 v166, v150, v166\n"
    "v_mul_f32 v167, v151, v167\n"
    "v_mul_f32 v148, 0xbfb8aa3b, v148\n"
    "v_mul_f32 v149, 0xbfb8aa3b, v149\n"
    "v_mul_f32 v150, 0xbfb8aa3b, v150\n"
    "v_mul_f32 v151, 0xbfb8aa3b, v151\n"
    "v_exp_f32 v148, v148\n"
    "v_exp_f32 v149, v149\n"
    "v_exp_f32 v150, v150\n"
    "v_exp_f32 v151, v151\n"
    "v_add_f32 v148, 1.0, v148\n"
    "v_add_f32 v149, 1.0, v149\n"
    "v_add_f32 v150, 1.0, v150\n"
    "v_add_f32 v151, 1.0, v151\n"
    "v_rcp_f32 v148, v148\n"
    "v_rcp_f32 v149, v149\n"
    "v_rcp_f32 v150, v150\n"
    "v_rcp_f32 v151, v151\n"
    "v_mul_f32 v164, v164, v148\n"
    "v_mul_f32 v165, v165, v149\n"
    "v_mul_f32 v166, v166, v150\n"
    "v_mul_f32 v167, v167, v151\n"
    "v_mul_f32 v172, v156, v172\n"
    "v_mul_f32 v173, v157, v173\n"
    "v_mul_f32 v174, v158, v174\n"
    "v_mul_f32 v175, v159, v175\n"
    "v_mul_f32 v156, 0xbfb8aa3b, v156\n"
    "v_mul_f32 v157, 0xbfb8aa3b, v157\n"
    "v_mul_f32 v158, 0xbfb8aa3b, v158\n"
    "v_mul_f32 v159, 0xbfb8aa3b, v159\n"
    "v_exp_f32 v156, v156\n"
    "v_exp_f32 v157, v157\n"
    "v_exp_f32 v158, v158\n"
    "v_exp_f32 v159, v159\n"
    "v_add_f32 v156, 1.0, v156\n"
    "v_add_f32 v157, 1.0, v157\n"
    "v_add_f32 v158, 1.0, v158\n"
    "v_add_f32 v159, 1.0, v159\n"
    "v_rcp_f32 v156, v156\n"
    "v_rcp_f32 v157, v157\n"
    "v_rcp_f32 v158, v158\n"
    "v_rcp_f32 v159, v159\n"
    "v_mul_f32 v172, v172, v156\n"
    "v_mul_f32 v173, v173, v157\n"
    "v_mul_f32 v174, v174, v158\n"
    "v_mul_f32 v175, v175, v159\n"
    "v_cvt_pk_bf16_f32 v148, v164, v165\n"
    "v_cvt_pk_bf16_f32 v149, v166, v167\n"
    "v_cvt_pk_bf16_f32 v150, v172, v173\n"
    "v_cvt_pk_bf16_f32 v151, v174, v175\n"
    "buffer_store_dwordx4 v[148:151], v252, s[56:59], s75 offen offset:1024 sc0 sc1\n"
    "s_add_u32 s75, s75, 16384\n"
    "s_branch L1END%=\n"
    "L1DRB%=:\n"
    "s_nop 7\n"
    "v_mul_f32 v192, v176, v192\n"
    "v_mul_f32 v193, v177, v193\n"
    "v_mul_f32 v194, v178, v194\n"
    "v_mul_f32 v195, v179, v195\n"
    "v_mul_f32 v176, 0xbfb8aa3b, v176\n"
    "v_mul_f32 v177, 0xbfb8aa3b, v177\n"
    "v_mul_f32 v178, 0xbfb8aa3b, v178\n"
    "v_mul_f32 v179, 0xbfb8aa3b, v179\n"
    "v_exp_f32 v176, v176\n"
    "v_exp_f32 v177, v177\n"
    "v_exp_f32 v178, v178\n"
    "v_exp_f32 v179, v179\n"
    "v_add_f32 v176, 1.0, v176\n"
    "v_add_f32 v177, 1.0, v177\n"
    "v_add_f32 v178, 1.0, v178\n"
    "v_add_f32 v179, 1.0, v179\n"
    "v_rcp_f32 v176, v176\n"
    "v_rcp_f32 v177, v177\n"
    "v_rcp_f32 v178, v178\n"
    "v_rcp_f32 v179, v179\n"
    "v_mul_f32 v192, v192, v176\n"
    "v_mul_f32 v193, v193, v177\n"
    "v_mul_f32 v194, v194, v178\n"
    "v_mul_f32 v195, v195, v179\n"
    "v_mul_f32 v200, v184, v200\n"
    "v_mul_f32 v201, v185, v201\n"
    "v_mul_f32 v202, v186, v202\n"
    "v_mul_f32 v203, v187, v203\n"
    "v_mul_f32 v184, 0xbfb8aa3b, v184\n"
    "v_mul_f32 v185, 0xbfb8aa3b, v185\n"
    "v_mul_f32 v186, 0xbfb8aa3b, v186\n"
    "v_mul_f32 v187, 0xbfb8aa3b, v187\n"
    "v_exp_f32 v184, v184\n"
    "v_exp_f32 v185, v185\n"
    "v_exp_f32 v186, v186\n"
    "v_exp_f32 v187, v187\n"
    "v_add_f32 v184, 1.0, v184\n"
    "v_add_f32 v185, 1.0, v185\n"
    "v_add_f32 v186, 1.0, v186\n"
    "v_add_f32 v187, 1.0, v187\n"
    "v_rcp_f32 v184, v184\n"
    "v_rcp_f32 v185, v185\n"
    "v_rcp_f32 v186, v186\n"
    "v_rcp_f32 v187, v187\n"
    "v_mul_f32 v200, v200, v184\n"
    "v_mul_f32 v201, v201, v185\n"
    "v_mul_f32 v202, v202, v186\n"
    "v_mul_f32 v203, v203, v187\n"
    "v_cvt_pk_bf16_f32 v176, v192, v193\n"
    "v_cvt_pk_bf16_f32 v177, v194, v195\n"
    "v_cvt_pk_bf16_f32 v178, v200, v201\n"
    "v_cvt_pk_bf16_f32 v179, v202, v203\n"
    "buffer_store_dwordx4 v[176:179], v252, s[56:59], s75 offen offset:0 sc0 sc1\n"
    "v_mul_f32 v196, v180, v196\n"
    "v_mul_f32 v197, v181, v197\n"
    "v_mul_f32 v198, v182, v198\n"
    "v_mul_f32 v199, v183, v199\n"
    "v_mul_f32 v180, 0xbfb8aa3b, v180\n"
    "v_mul_f32 v181, 0xbfb8aa3b, v181\n"
    "v_mul_f32 v182, 0xbfb8aa3b, v182\n"
    "v_mul_f32 v183, 0xbfb8aa3b, v183\n"
    "v_exp_f32 v180, v180\n"
    "v_exp_f32 v181, v181\n"
    "v_exp_f32 v182, v182\n"
    "v_exp_f32 v183, v183\n"
    "v_add_f32 v180, 1.0, v180\n"
    "v_add_f32 v181, 1.0, v181\n"
    "v_add_f32 v182, 1.0, v182\n"
    "v_add_f32 v183, 1.0, v183\n"
    "v_rcp_f32 v180, v180\n"
    "v_rcp_f32 v181, v181\n"
    "v_rcp_f32 v182, v182\n"
    "v_rcp_f32 v183, v183\n"
    "v_mul_f32 v196, v196, v180\n"
    "v_mul_f32 v197, v197, v181\n"
    "v_mul_f32 v198, v198, v182\n"
    "v_mul_f32 v199, v199, v183\n"
    "v_mul_f32 v204, v188, v204\n"
    "v_mul_f32 v205, v189, v205\n"
    "v_mul_f32 v206, v190, v206\n"
    "v_mul_f32 v207, v191, v207\n"
    "v_mul_f32 v188, 0xbfb8aa3b, v188\n"
    "v_mul_f32 v189, 0xbfb8aa3b, v189\n"
    "v_mul_f32 v190, 0xbfb8aa3b, v190\n"
    "v_mul_f32 v191, 0xbfb8aa3b, v191\n"
    "v_exp_f32 v188, v188\n"
    "v_exp_f32 v189, v189\n"
    "v_exp_f32 v190, v190\n"
    "v_exp_f32 v191, v191\n"
    "v_add_f32 v188, 1.0, v188\n"
    "v_add_f32 v189, 1.0, v189\n"
    "v_add_f32 v190, 1.0, v190\n"
    "v_add_f32 v191, 1.0, v191\n"
    "v_rcp_f32 v188, v188\n"
    "v_rcp_f32 v189, v189\n"
    "v_rcp_f32 v190, v190\n"
    "v_rcp_f32 v191, v191\n"
    "v_mul_f32 v204, v204, v188\n"
    "v_mul_f32 v205, v205, v189\n"
    "v_mul_f32 v206, v206, v190\n"
    "v_mul_f32 v207, v207, v191\n"
    "v_cvt_pk_bf16_f32 v180, v196, v197\n"
    "v_cvt_pk_bf16_f32 v181, v198, v199\n"
    "v_cvt_pk_bf16_f32 v182, v204, v205\n"
    "v_cvt_pk_bf16_f32 v183, v206, v207\n"
    "buffer_store_dwordx4 v[180:183], v252, s[56:59], s75 offen offset:1024 sc0 sc1\n"
    "s_add_u32 s75, s75, 16384\n"
    "L1END%=:\n"
    "s_waitcnt vmcnt(2) lgkmcnt(0)\n"
    "s_barrier\n"
    "s_mov_b32 %0, s95\n"
    "L1_CODE_END:\n"
    : "=s"(p0)
    : "s"(x),"s"(wg),"s"(wu),"s"(fda),"s"(ws),"s"(fdb),"s"(mt),"s"(w),"s"(nj),"s"(type),"s"(j0),"s"(f),"s"(bb),"s"(sg),"s"(su),"s"(srec),"s"(sstr),"s"(srec2),"s"(tptr),"s"(trec)
    : "memory","scc","vcc","v0","v1","v2","v3","v4","v5","v6","v7","v8","v9","v10","v11","v12","v13","v14","v15","v16","v17","v18","v19","v20","v21","v22","v23","v24","v25","v26","v27","v28","v29","v30","v31","v32","v33","v34","v35","v36","v37","v38","v39","v40","v41","v42","v43","v44","v45","v46","v47","v48","v49","v50","v51","v52","v53","v54","v55","v56","v57","v58","v59","v60","v61","v62","v63","v64","v65","v66","v67","v68","v69","v70","v71","v72","v73","v74","v75","v76","v77","v78","v79","v80","v81","v82","v83","v84","v85","v86","v87","v88","v89","v90","v91","v92","v93","v94","v95","v96","v97","v98","v99","v100","v101","v102","v103","v104","v105","v106","v107","v108","v109","v110","v111","v112","v113","v114","v115","v116","v117","v118","v119","v120","v121","v122","v123","v124","v125","v126","v127","v128","v129","v130","v131","v132","v133","v134","v135","v136","v137","v138","v139","v140","v141","v142","v143","v144","v145","v146","v147","v148","v149","v150","v151","v152","v153","v154","v155","v156","v157","v158","v159","v160","v161","v162","v163","v164","v165","v166","v167","v168","v169","v170","v171","v172","v173","v174","v175","v176","v177","v178","v179","v180","v181","v182","v183","v184","v185","v186","v187","v188","v189","v190","v191","v192","v193","v194","v195","v196","v197","v198","v199","v200","v201","v202","v203","v204","v205","v206","v207","v208","v209","v210","v211","v212","v213","v214","v215","v216","v217","v218","v219","v220","v221","v222","v223","v224","v225","v226","v227","v228","v229","v230","v231","v232","v233","v234","v235","v236","v237","v238","v239","v240","v241","v242","v243","v244","v245","v246","v247","v248","v249","v250","v251","v252","v253","v254","v255","s36","s37","s38","s39","s40","s41","s42","s43","s44","s45","s46","s47","s48","s49","s50","s51","s52","s53","s54","s55","s56","s57","s58","s59","s60","s61","s62","s63","s64","s65","s66","s67","s68","s69","s70","s71","s72","s73","s74","s75","s76","s77","s78","s79","s80","s81","s82","s83","s84","s85","s86","s87","s88","s89","s90","s91","s92","s93","s94","s95","s96","s97","s98","s99");
  typedef const __attribute__((address_space(4))) unsigned long long* kap;
  kap ka=(kap)__builtin_amdgcn_kernarg_segment_ptr(); asm volatile("":"+s"(ka),"+s"(bb));
  { unsigned lane; asm volatile("v_mbcnt_lo_u32_b32 %0, -1, 0\n v_mbcnt_hi_u32_b32 %0, -1, %0":"=v"(lane));
    char* ws2=(char*)ka[6];
    if(bb<144) wd_job(p0,bb,w,lane,ws2+WS_WD); else if(bb<208) wt_job(p0,bb-144,w,lane,ws2+WS_WT);
    const L1P p2=l1_params(bb);
    if(p2.nj==4) gate_job(__builtin_amdgcn_readfirstlane(p2.mt*3+(p2.f-5)),w,lane,(const u16*)ka[0],(const u16*)ka[7],ws2+WS_G); }

}

__global__ __launch_bounds__(512,2) void l2_kernel(char* ws, void* out){

  const int b=blockIdx.x, xcd=b&7, idx=b>>3;
  int kq, mt, nt=idx&3, a3=0;
  if(idx<24){ const int pp=6*xcd+(idx>>2); kq=pp>>4; mt=pp&15; }
  else { kq=3; mt=2*xcd+((idx-24)>>2); a3=(idx-24)*8+xcd; }
  int w=__builtin_amdgcn_readfirstlane(threadIdx.x>>6);
  const int r=w&3, kh=w>>2;
  const int s0 = kq<3 ? 48*kq : 0, s1 = kq<3 ? 48*kq+24 : 4;
  const int h0 = kq<3 ? s0 : 144, h1 = kq<3 ? s1 : 148;
  int nr = kq<3 ? 24 : 10;
  int hoff = (kh?h1:h0)*8192 + r*2048;
  int d[3], m[3];
  for(int i=0;i<2;++i){ int phi=w+8*i; int k2=phi>=9; int blk=phi-9*k2; d[i]=((k2?s1:s0)*9+blk)*1024; m[i]=phi*1024; }
  { int phi=16+(w>>2); int blk=phi-9; d[2]=(s1*9+blk)*1024+(w&3)*256; m[2]=phi*1024+(w&3)*256; }
  const char* Wp=ws+WS_WD+(size_t)nt*KS*9216; int wrec=KS*9216; const void* TB; const void* TWp; const char* GTp;
#if defined(__HIP_DEVICE_COMPILE__)
  GTp=(const char*)GTT+(size_t)nt*175104;
  if(kq==3){ Wp=GTp; wrec=19*9216; }
  TB=(const void*)&F2TT; TWp=(const char*)&F2TT+36864;
#endif
  int bb=b;
#define RF(x) x=__builtin_amdgcn_readfirstlane(x)
  RF(hoff);RF(a3);RF(nr);RF(d[0]);RF(d[1]);RF(d[2]);RF(m[0]);RF(m[1]);RF(m[2]);RF(mt);RF(nt);RF(kq);RF(wrec);
  asm volatile("":"+s"(w),"+s"(Wp),"+s"(TB),"+s"(TWp),"+s"(ws),"+s"(out),"+s"(bb),"+s"(GTp));
  asm volatile(
    "s_mov_b32 s63, %5\n"
    "s_mov_b32 s61, %7\n"
    "s_mov_b32 s64, %8\n"
    "s_mov_b32 s65, %9\n"
    "s_mov_b32 s66, %10\n"
    "s_mov_b32 s67, %11\n"
    "s_mov_b32 s68, %12\n"
    "s_mov_b32 s69, %13\n"
    "s_mov_b32 s77, %15\n"
    "s_mov_b32 s78, %16\n"
    "s_mov_b32 s62, %17\n"
    "s_mov_b32 s56, %18\n"
    "s_and_b32 s57, s56, 3\n"
    "s_lshr_b32 s58, s56, 2\n"
    "L2_CODE_BEG:\n"
    "s_cmp_lt_u32 s62, 3\n"
    "s_cbranch_scc0 L2ATYPE%=\n"
    "s_mul_i32 s59, s77, 1335296\n"
    "s_mov_b64 s[36:37], %0\n"
    "s_add_u32 s36, s36, s59\n"
    "s_addc_u32 s37, s37, 0\n"
    "s_and_b32 s37, s37, 0xffff\n"
    "s_mov_b32 s38, 1335296\n"
    "s_mov_b32 s39, 0x20000\n"
    "s_mov_b64 s[44:45], %0\n"
    "s_add_u32 s44, s44, 29360128\n"
    "s_addc_u32 s45, s45, 0\n"
    "s_and_b32 s45, s45, 0xffff\n"
    "s_mov_b32 s46, 7077888\n"
    "s_mov_b32 s47, 0x20000\n"
    "s_mov_b64 s[48:49], %2\n"
    "s_and_b32 s49, s49, 0xffff\n"
    "s_mov_b32 s50, 2359296\n"
    "s_mov_b32 s51, 0x20000\n"
    "s_mov_b64 s[52:53], %0\n"
    "s_add_u32 s52, s52, 46137344\n"
    "s_addc_u32 s53, s53, 0\n"
    "s_and_b32 s53, s53, 0xffff\n"
    "s_mov_b32 s54, 8192\n"
    "s_mov_b32 s55, 0x20000\n"
    "s_mov_b64 s[40:41], %1\n"
    "s_and_b32 s41, s41, 0xffff\n"
    "s_mov_b32 s42, %14\n"
    "s_mov_b32 s43, 0x20000\n"
    "v_mbcnt_lo_u32_b32 v206, -1, 0\n"
    "v_mbcnt_hi_u32_b32 v206, -1, v206\n"
    "v_lshlrev_b32 v200, 4, v206\n"
    "v_lshlrev_b32 v222, 2, v206\n"
    "s_mov_b32 s70, s63\n"
    "s_sub_u32 s64, s64, 9216\n"
    "s_sub_u32 s65, s65, 9216\n"
    "s_sub_u32 s66, s66, 9216\n"
    "s_sub_u32 s63, s63, 8192\n"
    "s_add_u32 m0, s67, 0\n"
    "s_add_u32 s64, s64, 9216\n"
    "buffer_load_dwordx4 v200, s[40:43], s64 offen lds\n"
    "s_add_u32 m0, s68, 0\n"
    "s_add_u32 s65, s65, 9216\n"
    "buffer_load_dwordx4 v200, s[40:43], s65 offen lds\n"
    "s_add_u32 m0, s69, 0\n"
    "s_add_u32 s66, s66, 9216\n"
    "buffer_load_dword v222, s[40:43], s66 offen lds\n"
    "s_add_u32 s63, s63, 8192\n"
    "buffer_load_dwordx4 v[108:111], v200, s[36:39], s63 offen offset:0\n"
    "buffer_load_dwordx4 v[112:115], v200, s[36:39], s63 offen offset:1024\n"
    "s_barrier\n"
    "s_add_u32 m0, s67, 18432\n"
    "s_add_u32 s64, s64, 9216\n"
    "buffer_load_dwordx4 v200, s[40:43], s64 offen lds\n"
    "s_add_u32 m0, s68, 18432\n"
    "s_add_u32 s65, s65, 9216\n"
    "buffer_load_dwordx4 v200, s[40:43], s65 offen lds\n"
    "s_add_u32 m0, s69, 18432\n"
    "s_add_u32 s66, s66, 9216\n"
    "buffer_load_dword v222, s[40:43], s66 offen lds\n"
    "s_add_u32 s63, s63, 8192\n"
    "buffer_load_dwordx4 v[116:119], v200, s[36:39], s63 offen offset:0\n"
    "buffer_load_dwordx4 v[120:123], v200, s[36:39], s63 offen offset:1024\n"
    "s_barrier\n"
    "s_add_u32 m0, s67, 36864\n"
    "s_add_u32 s64, s64, 9216\n"
    "buffer_load_dwordx4 v200, s[40:43], s64 offen lds\n"
    "s_add_u32 m0, s68, 36864\n"
    "s_add_u32 s65, s65, 9216\n"
    "buffer_load_dwordx4 v200, s[40:43], s65 offen lds\n"
    "s_add_u32 m0, s69, 36864\n"
    "s_add_u32 s66, s66, 9216\n"
    "buffer_load_dword v222, s[40:43], s66 offen lds\n"
    "s_add_u32 s63, s63, 8192\n"
    "buffer_load_dwordx4 v[124:127], v200, s[36:39], s63 offen offset:0\n"
    "buffer_load_dwordx4 v[128:131], v200, s[36:39], s63 offen offset:1024\n"
    "s_barrier\n"
    "s_add_u32 m0, s67, 55296\n"
    "s_add_u32 s64, s64, 9216\n"
    "buffer_load_dwordx4 v200, s[40:43], s64 offen lds\n"
    "s_add_u32 m0, s68, 55296\n"
    "s_add_u32 s65, s65, 9216\n"
    "buffer_load_dwordx4 v200, s[40:43], s65 offen lds\n"
    "s_add_u32 m0, s69, 55296\n"
    "s_add_u32 s66, s66, 9216\n"
    "buffer_load_dword v222, s[40:43], s66 offen lds\n"
    "s_add_u32 s63, s63, 8192\n"
    "buffer_load_dwordx4 v[132:135], v200, s[36:39], s63 offen offset:0\n"
    "buffer_load_dwordx4 v[136:139], v200, s[36:39], s63 offen offset:1024\n"
    "s_add_u32 m0, s67, 73728\n"
    "s_add_u32 s64, s64, 9216\n"
    "buffer_load_dwordx4 v200, s[40:43], s64 offen lds\n"
    "s_add_u32 m0, s68, 73728\n"
    "s_add_u32 s65, s65, 9216\n"
    "buffer_load_dwordx4 v200, s[40:43], s65 offen lds\n"
    "s_add_u32 m0, s69, 73728\n"
    "s_add_u32 s66, s66, 9216\n"
    "buffer_load_dword v222, s[40:43], s66 offen lds\n"
    "s_add_u32 s63, s63, 8192\n"
    "buffer_load_dwordx4 v[140:143], v200, s[36:39], s63 offen offset:0\n"
    "buffer_load_dwordx4 v[144:147], v200, s[36:39], s63 offen offset:1024\n"
    "s_add_u32 m0, s67, 92160\n"
    "s_add_u32 s64, s64, 9216\n"
    "buffer_load_dwordx4 v200, s[40:43], s64 offen lds\n"
    "s_add_u32 m0, s68, 92160\n"
    "s_add_u32 s65, s65, 9216\n"
    "buffer_load_dwordx4 v200, s[40:43], s65 offen lds\n"
    "s_add_u32 m0, s69, 92160\n"
    "s_add_u32 s66, s66, 9216\n"
    "buffer_load_dword v222, s[40:43], s66 offen lds\n"
    "v_and_b32 v207, 15, v206\n"
    "s_lshl_b32 s59, s77, 7\n"
    "s_lshl_b32 s60, s57, 5\n"
    "s_add_u32 s59, s59, s60\n"
    "v_add_u32 v207, s59, v207\n"
    "v_lshlrev_b32 v223, 2, v207\n"
    "s_mul_i32 s59, s58, 9216\n"
    "v_add_u32 v201, s59, v200\n"
    "v_add_u32 v202, 55296, v201\n"
    "v_add_u32 v205, 110592, v201\n"
    "s_waitcnt vmcnt(25)\n"
    "s_barrier\n"
    "ds_read_b128 v[72:75], v201 offset:0\n"
    "ds_read_b128 v[76:79], v201 offset:1024\n"
    "ds_read_b128 v[80:83], v201 offset:2048\n"
    "ds_read_b128 v[84:87], v201 offset:3072\n"
    "ds_read_b128 v[88:91], v201 offset:4096\n"
    "ds_read_b128 v[92:95], v201 offset:5120\n"
    "ds_read_b128 v[96:99], v201 offset:6144\n"
    "ds_read_b128 v[100:103], v201 offset:7168\n"
    "ds_read_b128 v[104:107], v201 offset:8192\n"
    "s_waitcnt vmcnt(15)\n"
    "s_barrier\n"
    "s_waitcnt lgkmcnt(8)\n"
    "v_mfma_f32_16x16x32_bf16 v[0:3], v[72:75], v[108:111], 0\n"
    "s_add_u32 m0, s67, 110592\n"
    "s_add_u32 s64, s64, 9216\n"
    "buffer_load_dwordx4 v200, s[40:43], s64 offen lds\n"
    "v_mfma_f32_16x16x32_bf16 v[4:7], v[72:75], v[112:115], 0\n"
    "ds_read_b128 v[72:75], v201 offset:18432\n"
    "s_waitcnt lgkmcnt(8)\n"
    "v_mfma_f32_16x16x32_bf16 v[8:11], v[76:79], v[108:111], 0\n"
    "s_add_u32 m0, s68, 110592\n"
    "s_add_u32 s65, s65, 9216\n"
    "buffer_load_dwordx4 v200, s[40:43], s65 offen lds\n"
    "v_mfma_f32_16x16x32_bf16 v[12:15], v[76:79], v[112:115], 0\n"
    "ds_read_b128 v[76:79], v201 offset:19456\n"
    "s_waitcnt lgkmcnt(8)\n"
    "v_mfma_f32_16x16x32_bf16 v[16:19], v[80:83], v[108:111], 0\n"
    "s_add_u32 m0, s69, 110592\n"
    "s_add_u32 s66, s66, 9216\n"
    "buffer_load_dword v222, s[40:43], s66 offen lds\n"
    "v_mfma_f32_16x16x32_bf16 v[20:23], v[80:83], v[112:115], 0\n"
    "ds_read_b128 v[80:83], v201 offset:20480\n"
    "s_waitcnt lgkmcnt(8)\n"
    "v_mfma_f32_16x16x32_bf16 v[24:27], v[84:87], v[108:111], 0\n"
    "s_add_u32 m0, s67, 129024\n"
    "s_add_u32 s64, s64, 9216\n"
    "buffer_load_dwordx4 v200, s[40:43], s64 offen lds\n"
    "v_mfma_f32_16x16x32_bf16 v[28:31], v[84:87], v[112:115], 0\n"
    "ds_read_b128 v[84:87], v201 offset:21504\n"
    "s_waitcnt lgkmcnt(8)\n"
    "v_mfma_f32_16x16x32_bf16 v[32:35], v[88:91], v[108:111], 0\n"
    "s_add_u32 m0, s68, 129024\n"
    "s_add_u32 s65, s65, 9216\n"
    "buffer_load_dwordx4 v200, s[40:43], s65 offen lds\n"
    "v_mfma_f32_16x16x32_bf16 v[36:39], v[88:91], v[112:115], 0\n"
    "ds_read_b128 v[88:91], v201 offset:22528\n"
    "s_waitcnt lgkmcnt(8)\n"
    "v_mfma_f32_16x16x32_bf16 v[40:43], v[92:95], v[108:111], 0\n"
    "s_add_u32 m0, s69, 129024\n"
    "s_add_u32 s66, s66, 9216\n"
    "buffer_load_dword v222, s[40:43], s66 offen lds\n"
    "v_mfma_f32_16x16x32_bf16 v[44:47], v[92:95], v[112:115], 0\n"
    "ds_read_b128 v[92:95], v201 offset:23552\n"
    "s_waitcnt lgkmcnt(8)\n"
    "v_mfma_f32_16x16x32_bf16 v[48:51], v[96:99], v[108:111], 0\n"
    "s_add_u32 s63, s63, 8192\n"
    "buffer_load_dwordx4 v[148:151], v200, s[36:39], s63 offen offset:0\n"
    "buffer_load_dwordx4 v[152:155], v200, s[36:39], s63 offen offset:1024\n"
    "v_mfma_f32_16x16x32_bf16 v[52:55], v[96:99], v[112:115], 0\n"
    "ds_read_b128 v[96:99], v201 offset:24576\n"
    "s_waitcnt lgkmcnt(8)\n"
    "v_mfma_f32_16x16x32_bf16 v[56:59], v[100:103], v[108:111], 0\n"
    "v_mfma_f32_16x16x32_bf16 v[60:63], v[100:103], v[112:115], 0\n"
    "ds_read_b128 v[100:103], v201 offset:25600\n"
    "s_waitcnt lgkmcnt(8)\n"
    "v_mfma_f32_16x16x32_bf16 v[64:67], v[104:107], v[108:111], 0\n"
    "v_mfma_f32_16x16x32_bf16 v[68:71], v[104:107], v[112:115], 0\n"
    "ds_read_b128 v[104:107], v201 offset:26624\n"
    "s_waitcnt lgkmcnt(8)\n"
    "v_mfma_f32_16x16x32_bf16 v[0:3], v[72:75], v[116:119], v[0:3]\n"
    "s_add_u32 s63, s63, 8192\n"
    "buffer_load_dwordx4 v[108:111], v200, s[36:39], s63 offen offset:0\n"
    "buffer_load_dwordx4 v[112:115], v200, s[36:39], s63 offen offset:1024\n"
    "v_mfma_f32_16x16x32_bf16 v[4:7], v[72:75], v[120:123], v[4:7]\n"
    "ds_read_b128 v[72:75], v201 offset:36864\n"
    "s_waitcnt lgkmcnt(8)\n"
    "v_mfma_f32_16x16x32_bf16 v[8:11], v[76:79], v[116:119], v[8:11]\n"
    "v_mfma_f32_16x16x32_bf16 v[12:15], v[76:79], v[120:123], v[12:15]\n"
    "ds_read_b128 v[76:79], v201 offset:37888\n"
    "s_waitcnt lgkmcnt(8)\n"
    "v_mfma_f32_16x16x32_bf16 v[16:19], v[80:83], v[116:119], v[16:19]\n"
    "v_mfma_f32_16x16x32_bf16 v[20:23], v[80:83], v[120:123], v[20:23]\n"
    "ds_read_b128 v[80:83], v201 offset:38912\n"
    "s_waitcnt lgkmcnt(8)\n"
    "v_mfma_f32_16x16x32_bf16 v[24:27], v[84:87], v[116:119], v[24:27]\n"
    "v_mfma_f32_16x16x32_bf16 v[28:31], v[84:87], v[120:123], v[28:31]\n"
    "ds_read_b128 v[84:87], v201 offset:39936\n"
    "s_waitcnt lgkmcnt(8)\n"
    "v_mfma_f32_16x16x32_bf16 v[32:35], v[88:91], v[116:119], v[32:35]\n"
    "v_mfma_f32_16x16x32_bf16 v[36:39], v[88:91], v[120:123], v[36:39]\n"
    "ds_read_b128 v[88:91], v201 offset:40960\n"
    "s_waitcnt lgkmcnt(8)\n"
    "v_mfma_f32_16x16x32_bf16 v[40:43], v[92:95], v[116:119], v[40:43]\n"
    "v_mfma_f32_16x16x32_bf16 v[44:47], v[92:95], v[120:123], v[44:47]\n"
    "ds_read_b128 v[92:95], v201 offset:41984\n"
    "s_waitcnt lgkmcnt(8)\n"
    "v_mfma_f32_16x16x32_bf16 v[48:51], v[96:99], v[116:119], v[48:51]\n"
    "v_mfma_f32_16x16x32_bf16 v[52:55], v[96:99], v[120:123], v[52:55]\n"
    "ds_read_b128 v[96:99], v201 offset:43008\n"
    "s_waitcnt lgkmcnt(8)\n"
    "v_mfma_f32_16x16x32_bf16 v[56:59], v[100:103], v[116:119], v[56:59]\n"
    "v_mfma_f32_16x16x32_bf16 v[60:63], v[100:103], v[120:123], v[60:63]\n"
    "ds_read_b128 v[100:103], v201 offset:44032\n"
    "s_waitcnt lgkmcnt(8)\n"
    "v_mfma_f32_16x16x32_bf16 v[64:67], v[104:107], v[116:119], v[64:67]\n"
    "v_mfma_f32_16x16x32_bf16 v[68:71], v[104:107], v[120:123], v[68:71]\n"
    "ds_read_b128 v[104:107], v201 offset:45056\n"
    "s_waitcnt vmcnt(15)\n"
    "s_barrier\n"
    "s_waitcnt lgkmcnt(8)\n"
    "v_mfma_f32_16x16x32_bf16 v[0:3], v[72:75], v[124:127], v[0:3]\n"
    "s_add_u32 m0, s67, 0\n"
    "s_add_u32 s64, s64, 9216\n"
    "buffer_load_dwordx4 v200, s[40:43], s64 offen lds\n"
    "v_mfma_f32_16x16x32_bf16 v[4:7], v[72:75], v[128:131], v[4:7]\n"
    "ds_read_b128 v[72:75], v202 offset:0\n"
    "s_waitcnt lgkmcnt(8)\n"
    "v_mfma_f32_16x16x32_bf16 v[8:11], v[76:79], v[124:127], v[8:11]\n"
    "s_add_u32 m0, s68, 0\n"
    "s_add_u32 s65, s65, 9216\n"
    "buffer_load_dwordx4 v200, s[40:43], s65 offen lds\n"
    "v_mfma_f32_16x16x32_bf16 v[12:15], v[76:79], v[128:131], v[12:15]\n"
    "ds_read_b128 v[76:79], v202 offset:1024\n"
    "s_waitcnt lgkmcnt(8)\n"
    "v_mfma_f32_16x16x32_bf16 v[16:19], v[80:83], v[124:127], v[16:19]\n"
    "s_add_u32 m0, s69, 0\n"
    "s_add_u32 s66, s66, 9216\n"
    "buffer_load_dword v222, s[40:43], s66 offen lds\n"
    "v_mfma_f32_16x16x32_bf16 v[20:23], v[80:83], v[128:131], v[20:23]\n"
    "ds_read_b128 v[80:83], v202 offset:2048\n"
    "s_waitcnt lgkmcnt(8)\n"
    "v_mfma_f32_16x16x32_bf16 v[24:27], v[84:87], v[124:127], v[24:27]\n"
    "s_add_u32 m0, s67, 18432\n"
    "s_add_u32 s64, s64, 9216\n"
    "buffer_load_dwordx4 v200, s[40:43], s64 offen lds\n"
    "v_mfma_f32_16x16x32_bf16 v[28:31], v[84:87], v[128:131], v[28:31]\n"
    "ds_read_b128 v[84:87], v202 offset:3072\n"
    "s_waitcnt lgkmcnt(8)\n"
    "v_mfma_f32_16x16x32_bf16 v[32:35], v[88:91], v[124:127], v[32:35]\n"
    "s_add_u32 m0, s68, 18432\n"
    "s_add_u32 s65, s65, 9216\n"
    "buffer_load_dwordx4 v200, s[40:43], s65 offen lds\n"
    "v_mfma_f32_16x16x32_bf16 v[36:39], v[88:91], v[128:131], v[36:39]\n"
    "ds_read_b128 v[88:91], v202 offset:4096\n"
    "s_waitcnt lgkmcnt(8)\n"
    "v_mfma_f32_16x16x32_bf16 v[40:43], v[92:95], v[124:127], v[40:43]\n"
    "s_add_u32 m0, s69, 18432\n"
    "s_add_u32 s66, s66, 9216\n"
    "buffer_load_dword v222, s[40:43], s66 offen lds\n"
    "v_mfma_f32_16x16x32_bf16 v[44:47], v[92:95], v[128:131], v[44:47]\n"
    "ds_read_b128 v[92:95], v202 offset:5120\n"
    "s_waitcnt lgkmcnt(8)\n"
    "v_mfma_f32_16x16x32_bf16 v[48:51], v[96:99], v[124:127], v[48:51]\n"
    "buffer_load_dword v203, v223, s[52:55], 0 offen\n"
    "buffer_load_dword v204, v223, s[52:55], 0 offen offset:64\n"
    "v_mfma_f32_16x16x32_bf16 v[52:55], v[96:99], v[128:131], v[52:55]\n"
    "ds_read_b128 v[96:99], v202 offset:6144\n"
    "s_waitcnt lgkmcnt(8)\n"
    "v_mfma_f32_16x16x32_bf16 v[56:59], v[100:103], v[124:127], v[56:59]\n"
    "s_add_u32 s63, s63, 8192\n"
    "buffer_load_dwordx4 v[116:119], v200, s[36:39], s63 offen offset:0\n"
    "buffer_load_dwordx4 v[120:123], v200, s[36:39], s63 offen offset:1024\n"
    "v_mfma_f32_16x16x32_bf16 v[60:63], v[100:103], v[128:131], v[60:63]\n"
    "ds_read_b128 v[100:103], v202 offset:7168\n"
    "s_waitcnt lgkmcnt(8)\n"
    "v_mfma_f32_16x16x32_bf16 v[64:67], v[104:107], v[124:127], v[64:67]\n"
    "v_mfma_f32_16x16x32_bf16 v[68:71], v[104:107], v[128:131], v[68:71]\n"
    "ds_read_b128 v[104:107], v202 offset:8192\n"
    "s_waitcnt lgkmcnt(8)\n"
    "v_mfma_f32_16x16x32_bf16 v[0:3], v[72:75], v[132:135], v[0:3]\n"
    "s_add_u32 s63, s63, 8192\n"
    "buffer_load_dwordx4 v[124:127], v200, s[36:39], s63 offen offset:0\n"
    "buffer_load_dwordx4 v[128:131], v200, s[36:39], s63 offen offset:1024\n"
    "v_mfma_f32_16x16x32_bf16 v[4:7], v[72:75], v[136:139], v[4:7]\n"
    "ds_read_b128 v[72:75], v202 offset:18432\n"
    "s_waitcnt lgkmcnt(8)\n"
    "v_mfma_f32_16x16x32_bf16 v[8:11], v[76:79], v[132:135], v[8:11]\n"
    "v_mfma_f32_16x16x32_bf16 v[12:15], v[76:79], v[136:139], v[12:15]\n"
    "ds_read_b128 v[76:79], v202 offset:19456\n"
    "s_waitcnt lgkmcnt(8)\n"
    "v_mfma_f32_16x16x32_bf16 v[16:19], v[80:83], v[132:135], v[16:19]\n"
    "v_mfma_f32_16x16x32_bf16 v[20:23], v[80:83], v[136:139], v[20:23]\n"
    "ds_read_b128 v[80:83], v202 offset:20480\n"
    "s_waitcnt lgkmcnt(8)\n"
    "v_mfma_f32_16x16x32_bf16 v[24:27], v[84:87], v[132:135], v[24:27]\n"
    "v_mfma_f32_16x16x32_bf16 v[28:31], v[84:87], v[136:139], v[28:31]\n"
    "ds_read_b128 v[84:87], v202 offset:21504\n"
    "s_waitcnt lgkmcnt(8)\n"
    "v_mfma_f32_16x16x32_bf16 v[32:35], v[88:91], v[132:135], v[32:35]\n"
    "v_mfma_f32_16x16x32_bf16 v[36:39], v[88:91], v[136:139], v[36:39]\n"
    "ds_read_b128 v[88:91], v202 offset:22528\n"
    "s_waitcnt lgkmcnt(8)\n"
    "v_mfma_f32_16x16x32_bf16 v[40:43], v[92:95], v[132:135], v[40:43]\n"
    "v_mfma_f32_16x16x32_bf16 v[44:47], v[92:95], v[136:139], v[44:47]\n"
    "ds_read_b128 v[92:95], v202 offset:23552\n"
    "s_waitcnt lgkmcnt(8)\n"
    "v_mfma_f32_16x16x32_bf16 v[48:51], v[96:99], v[132:135], v[48:51]\n"
    "v_mfma_f32_16x16x32_bf16 v[52:55], v[96:99], v[136:139], v[52:55]\n"
    "ds_read_b128 v[96:99], v202 offset:24576\n"
    "s_waitcnt lgkmcnt(8)\n"
    "v_mfma_f32_16x16x32_bf16 v[56:59], v[100:103], v[132:135], v[56:59]\n"
    "v_mfma_f32_16x16x32_bf16 v[60:63], v[100:103], v[136:139], v[60:63]\n"
    "ds_read_b128 v[100:103], v202 offset:25600\n"
    "s_waitcnt lgkmcnt(8)\n"
    "v_mfma_f32_16x16x32_bf16 v[64:67], v[104:107], v[132:135], v[64:67]\n"
    "v_mfma_f32_16x16x32_bf16 v[68:71], v[104:107], v[136:139], v[68:71]\n"
    "ds_read_b128 v[104:107], v202 offset:26624\n"
    "s_waitcnt vmcnt(19)\n"
    "s_barrier\n"
    "s_waitcnt lgkmcnt(8)\n"
    "v_mfma_f32_16x16x32_bf16 v[0:3], v[72:75], v[140:143], v[0:3]\n"
    "s_add_u32 m0, s67, 36864\n"
    "s_add_u32 s64, s64, 9216\n"
    "buffer_load_dwordx4 v200, s[40:43], s64 offen lds\n"
    "v_mfma_f32_16x16x32_bf16 v[4:7], v[72:75], v[144:147], v[4:7]\n"
    "ds_read_b128 v[72:75], v202 offset:36864\n"
    "s_waitcnt lgkmcnt(8)\n"
    "v_mfma_f32_16x16x32_bf16 v[8:11], v[76:79], v[140:143], v[8:11]\n"
    "s_add_u32 m0, s68, 36864\n"
    "s_add_u32 s65, s65, 9216\n"
    "buffer_load_dwordx4 v200, s[40:43], s65 offen lds\n"
    "v_mfma_f32_16x16x32_bf16 v[12:15], v[76:79], v[144:147], v[12:15]\n"
    "ds_read_b128 v[76:79], v202 offset:37888\n"
    "s_waitcnt lgkmcnt(8)\n"
    "v_mfma_f32_16x16x32_bf16 v[16:19], v[80:83], v[140:143], v[16:19]\n"
    "s_add_u32 m0, s69, 36864\n"
    "s_add_u32 s66, s66, 9216\n"
    "buffer_load_dword v222, s[40:43], s66 offen lds\n"
    "v_mfma_f32_16x16x32_bf16 v[20:23], v[80:83], v[144:147], v[20:23]\n"
    "ds_read_b128 v[80:83], v202 offset:38912\n"
    "s_waitcnt lgkmcnt(8)\n"
    "v_mfma_f32_16x16x32_bf16 v[24:27], v[84:87], v[140:143], v[24:27]\n"
    "s_add_u32 m0, s67, 55296\n"
    "s_add_u32 s64, s64, 9216\n"
    "buffer_load_dwordx4 v200, s[40:43], s64 offen lds\n"
    "v_mfma_f32_16x16x32_bf16 v[28:31], v[84:87], v[144:147], v[28:31]\n"
    "ds_read_b128 v[84:87], v202 offset:39936\n"
    "s_waitcnt lgkmcnt(8)\n"
    "v_mfma_f32_16x16x32_bf16 v[32:35], v[88:91], v[140:143], v[32:35]\n"
    "s_add_u32 m0, s68, 55296\n"
    "s_add_u32 s65, s65, 9216\n"
    "buffer_load_dwordx4 v200, s[40:43], s65 offen lds\n"
    "v_mfma_f32_16x16x32_bf16 v[36:39], v[88:91], v[144:147], v[36:39]\n"
    "ds_read_b128 v[88:91], v202 offset:40960\n"
    "s_waitcnt lgkmcnt(8)\n"
    "v_mfma_f32_16x16x32_bf16 v[40:43], v[92:95], v[140:143], v[40:43]\n"
    "s_add_u32 m0, s69, 55296\n"
    "s_add_u32 s66, s66, 9216\n"
    "buffer_load_dword v222, s[40:43], s66 offen lds\n"
    "v_mfma_f32_16x16x32_bf16 v[44:47], v[92:95], v[144:147], v[44:47]\n"
    "ds_read_b128 v[92:95], v202 offset:41984\n"
    "s_waitcnt lgkmcnt(8)\n"
    "v_mfma_f32_16x16x32_bf16 v[48:51], v[96:99], v[140:143], v[48:51]\n"
    "s_add_u32 s63, s63, 8192\n"
    "buffer_load_dwordx4 v[132:135], v200, s[36:39], s63 offen offset:0\n"
    "buffer_load_dwordx4 v[136:139], v200, s[36:39], s63 offen offset:1024\n"
    "v_mfma_f32_16x16x32_bf16 v[52:55], v[96:99], v[144:147], v[52:55]\n"
    "ds_read_b128 v[96:99], v202 offset:43008\n"
    "s_waitcnt lgkmcnt(8)\n"
    "v_mfma_f32_16x16x32_bf16 v[56:59], v[100:103], v[140:143], v[56:59]\n"
    "v_mfma_f32_16x16x32_bf16 v[60:63], v[100:103], v[144:147], v[60:63]\n"
    "ds_read_b128 v[100:103], v202 offset:44032\n"
    "s_waitcnt lgkmcnt(8)\n"
    "v_mfma_f32_16x16x32_bf16 v[64:67], v[104:107], v[140:143], v[64:67]\n"
    "v_mfma_f32_16x16x32_bf16 v[68:71], v[104:107], v[144:147], v[68:71]\n"
    "ds_read_b128 v[104:107], v202 offset:45056\n"
    "s_waitcnt vmcnt(22)\n"
    "s_waitcnt lgkmcnt(8)\n"
    "v_mfma_f32_16x16x32_bf16 v[0:3], v[72:75], v[148:151], v[0:3]\n"
    "s_add_u32 s63, s63, 8192\n"
    "buffer_load_dwordx4 v[140:143], v200, s[36:39], s63 offen offset:0\n"
    "buffer_load_dwordx4 v[144:147], v200, s[36:39], s63 offen offset:1024\n"
    "v_mfma_f32_16x16x32_bf16 v[4:7], v[72:75], v[152:155], v[4:7]\n"
    "ds_read_b128 v[72:75], v205 offset:0\n"
    "s_waitcnt lgkmcnt(8)\n"
    "v_mfma_f32_16x16x32_bf16 v[8:11], v[76:79], v[148:151], v[8:11]\n"
    "v_mfma_f32_16x16x32_bf16 v[12:15], v[76:79], v[152:155], v[12:15]\n"
    "ds_read_b128 v[76:79], v205 offset:1024\n"
    "s_waitcnt lgkmcnt(8)\n"
    "v_mfma_f32_16x16x32_bf16 v[16:19], v[80:83], v[148:151], v[16:19]\n"
    "v_mfma_f32_16x16x32_bf16 v[20:23], v[80:83], v[152:155], v[20:23]\n"
    "ds_read_b128 v[80:83], v205 offset:2048\n"
    "s_waitcnt lgkmcnt(8)\n"
    "v_mfma_f32_16x16x32_bf16 v[24:27], v[84:87], v[148:151], v[24:27]\n"
    "v_mfma_f32_16x16x32_bf16 v[28:31], v[84:87], v[152:155], v[28:31]\n"
    "ds_read_b128 v[84:87], v205 offset:3072\n"
    "s_waitcnt lgkmcnt(8)\n"
    "v_mfma_f32_16x16x32_bf16 v[32:35], v[88:91], v[148:151], v[32:35]\n"
    "v_mfma_f32_16x16x32_bf16 v[36:39], v[88:91], v[152:155], v[36:39]\n"
    "ds_read_b128 v[88:91], v205 offset:4096\n"
    "s_waitcnt lgkmcnt(8)\n"
    "v_mfma_f32_16x16x32_bf16 v[40:43], v[92:95], v[148:151], v[40:43]\n"
    "v_mfma_f32_16x16x32_bf16 v[44:47], v[92:95], v[152:155], v[44:47]\n"
    "ds_read_b128 v[92:95], v205 offset:5120\n"
    "s_waitcnt lgkmcnt(8)\n"
    "v_mfma_f32_16x16x32_bf16 v[48:51], v[96:99], v[148:151], v[48:51]\n"
    "v_mfma_f32_16x16x32_bf16 v[52:55], v[96:99], v[152:155], v[52:55]\n"
    "ds_read_b128 v[96:99], v205 offset:6144\n"
    "s_waitcnt lgkmcnt(8)\n"
    "v_mfma_f32_16x16x32_bf16 v[56:59], v[100:103], v[148:151], v[56:59]\n"
    "v_mfma_f32_16x16x32_bf16 v[60:63], v[100:103], v[152:155], v[60:63]\n"
    "ds_read_b128 v[100:103], v205 offset:7168\n"
    "s_waitcnt lgkmcnt(8)\n"
    "v_mfma_f32_16x16x32_bf16 v[64:67], v[104:107], v[148:151], v[64:67]\n"
    "v_mfma_f32_16x16x32_bf16 v[68:71], v[104:107], v[152:155], v[68:71]\n"
    "ds_read_b128 v[104:107], v205 offset:8192\n"
    "s_waitcnt vmcnt(19)\n"
    "s_barrier\n"
    "s_waitcnt lgkmcnt(8)\n"
    "v_mfma_f32_16x16x32_bf16 v[0:3], v[72:75], v[108:111], v[0:3]\n"
    "s_add_u32 m0, s67, 73728\n"
    "s_add_u32 s64, s64, 9216\n"
    "buffer_load_dwordx4 v200, s[40:43], s64 offen lds\n"
    "v_mfma_f32_16x16x32_bf16 v[4:7], v[72:75], v[112:115], v[4:7]\n"
    "ds_read_b128 v[72:75], v205 offset:18432\n"
    "s_waitcnt lgkmcnt(8)\n"
    "v_mfma_f32_16x16x32_bf16 v[8:11], v[76:79], v[108:111], v[8:11]\n"
    "s_add_u32 m0, s68, 73728\n"
    "s_add_u32 s65, s65, 9216\n"
    "buffer_load_dwordx4 v200, s[40:43], s65 offen lds\n"
    "v_mfma_f32_16x16x32_bf16 v[12:15], v[76:79], v[112:115], v[12:15]\n"
    "ds_read_b128 v[76:79], v205 offset:19456\n"
    "s_waitcnt lgkmcnt(8)\n"
    "v_mfma_f32_16x16x32_bf16 v[16:19], v[80:83], v[108:111], v[16:19]\n"
    "s_add_u32 m0, s69, 73728\n"
    "s_add_u32 s66, s66, 9216\n"
    "buffer_load_dword v222, s[40:43], s66 offen lds\n"
    "v_mfma_f32_16x16x32_bf16 v[20:23], v[80:83], v[112:115], v[20:23]\n"
    "ds_read_b128 v[80:83], v205 offset:20480\n"
    "s_waitcnt lgkmcnt(8)\n"
    "v_mfma_f32_16x16x32_bf16 v[24:27], v[84:87], v[108:111], v[24:27]\n"
    "s_add_u32 m0, s67, 92160\n"
    "s_add_u32 s64, s64, 9216\n"
    "buffer_load_dwordx4 v200, s[40:43], s64 offen lds\n"
    "v_mfma_f32_16x16x32_bf16 v[28:31], v[84:87], v[112:115], v[28:31]\n"
    "ds_read_b128 v[84:87], v205 offset:21504\n"
    "s_waitcnt lgkmcnt(8)\n"
    "v_mfma_f32_16x16x32_bf16 v[32:35], v[88:91], v[108:111], v[32:35]\n"
    "s_add_u32 m0, s68, 92160\n"
    "s_add_u32 s65, s65, 9216\n"
    "buffer_load_dwordx4 v200, s[40:43], s65 offen lds\n"
    "v_mfma_f32_16x16x32_bf16 v[36:39], v[88:91], v[112:115], v[36:39]\n"
    "ds_read_b128 v[88:91], v205 offset:22528\n"
    "s_waitcnt lgkmcnt(8)\n"
    "v_mfma_f32_16x16x32_bf16 v[40:43], v[92:95], v[108:111], v[40:43]\n"
    "s_add_u32 m0, s69, 92160\n"
    "s_add_u32 s66, s66, 9216\n"
    "buffer_load_dword v222, s[40:43], s66 offen lds\n"
    "v_mfma_f32_16x16x32_bf16 v[44:47], v[92:95], v[112:115], v[44:47]\n"
    "ds_read_b128 v[92:95], v205 offset:23552\n"
    "s_waitcnt lgkmcnt(8)\n"
    "v_mfma_f32_16x16x32_bf16 v[48:51], v[96:99], v[108:111], v[48:51]\n"
    "s_add_u32 s63, s63, 8192\n"
    "buffer_load_dwordx4 v[148:151], v200, s[36:39], s63 offen offset:0\n"
    "buffer_load_dwordx4 v[152:155], v200, s[36:39], s63 offen offset:1024\n"
    "v_mfma_f32_16x16x32_bf16 v[52:55], v[96:99], v[112:115], v[52:55]\n"
    "ds_read_b128 v[96:99], v205 offset:24576\n"
    "s_waitcnt lgkmcnt(8)\n"
    "v_mfma_f32_16x16x32_bf16 v[56:59], v[100:103], v[108:111], v[56:59]\n"
    "v_mfma_f32_16x16x32_bf16 v[60:63], v[100:103], v[112:115], v[60:63]\n"
    "ds_read_b128 v[100:103], v205 offset:25600\n"
    "s_waitcnt lgkmcnt(8)\n"
    "v_mfma_f32_16x16x32_bf16 v[64:67], v[104:107], v[108:111], v[64:67]\n"
    "v_mfma_f32_16x16x32_bf16 v[68:71], v[104:107], v[112:115], v[68:71]\n"
    "ds_read_b128 v[104:107], v205 offset:26624\n"
    "s_waitcnt vmcnt(20)\n"
    "s_waitcnt lgkmcnt(8)\n"
    "v_mfma_f32_16x16x32_bf16 v[0:3], v[72:75], v[116:119], v[0:3]\n"
    "s_add_u32 s63, s63, 8192\n"
    "buffer_load_dwordx4 v[108:111], v200, s[36:39], s63 offen offset:0\n"
    "buffer_load_dwordx4 v[112:115], v200, s[36:39], s63 offen offset:1024\n"
    "v_mfma_f32_16x16x32_bf16 v[4:7], v[72:75], v[120:123], v[4:7]\n"
    "ds_read_b128 v[72:75], v201 offset:0\n"
    "s_waitcnt lgkmcnt(8)\n"
    "v_mfma_f32_16x16x32_bf16 v[8:11], v[76:79], v[116:119], v[8:11]\n"
    "v_mfma_f32_16x16x32_bf16 v[12:15], v[76:79], v[120:123], v[12:15]\n"
    "ds_read_b128 v[76:79], v201 offset:1024\n"
    "s_waitcnt lgkmcnt(8)\n"
    "v_mfma_f32_16x16x32_bf16 v[16:19], v[80:83], v[116:119], v[16:19]\n"
    "v_mfma_f32_16x16x32_bf16 v[20:23], v[80:83], v[120:123], v[20:23]\n"
    "ds_read_b128 v[80:83], v201 offset:2048\n"
    "s_waitcnt lgkmcnt(8)\n"
    "v_mfma_f32_16x16x32_bf16 v[24:27], v[84:87], v[116:119], v[24:27]\n"
    "v_mfma_f32_16x16x32_bf16 v[28:31], v[84:87], v[120:123], v[28:31]\n"
    "ds_read_b128 v[84:87], v201 offset:3072\n"
    "s_waitcnt lgkmcnt(8)\n"
    "v_mfma_f32_16x16x32_bf16 v[32:35], v[88:91], v[116:119], v[32:35]\n"
    "v_mfma_f32_16x16x32_bf16 v[36:39], v[88:91], v[120:123], v[36:39]\n"
    "ds_read_b128 v[88:91], v201 offset:4096\n"
    "s_waitcnt lgkmcnt(8)\n"
    "v_mfma_f32_16x16x32_bf16 v[40:43], v[92:95], v[116:119], v[40:43]\n"
    "v_mfma_f32_16x16x32_bf16 v[44:47], v[92:95], v[120:123], v[44:47]\n"
    "ds_read_b128 v[92:95], v201 offset:5120\n"
    "s_waitcnt lgkmcnt(8)\n"
    "v_mfma_f32_16x16x32_bf16 v[48:51], v[96:99], v[116:119], v[48:51]\n"
    "v_mfma_f32_16x16x32_bf16 v[52:55], v[96:99], v[120:123], v[52:55]\n"
    "ds_read_b128 v[96:99], v201 offset:6144\n"
    "s_waitcnt lgkmcnt(8)\n"
    "v_mfma_f32_16x16x32_bf16 v[56:59], v[100:103], v[116:119], v[56:59]\n"
    "v_mfma_f32_16x16x32_bf16 v[60:63], v[100:103], v[120:123], v[60:63]\n"
    "ds_read_b128 v[100:103], v201 offset:7168\n"
    "s_waitcnt lgkmcnt(8)\n"
    "v_mfma_f32_16x16x32_bf16 v[64:67], v[104:107], v[116:119], v[64:67]\n"
    "v_mfma_f32_16x16x32_bf16 v[68:71], v[104:107], v[120:123], v[68:71]\n"
    "ds_read_b128 v[104:107], v201 offset:8192\n"
    "s_waitcnt vmcnt(17)\n"
    "s_barrier\n"
    "s_waitcnt lgkmcnt(8)\n"
    "v_mfma_f32_16x16x32_bf16 v[0:3], v[72:75], v[124:127], v[0:3]\n"
    "s_add_u32 m0, s67, 110592\n"
    "s_add_u32 s64, s64, 9216\n"
    "buffer_load_dwordx4 v200, s[40:43], s64 offen lds\n"
    "v_mfma_f32_16x16x32_bf16 v[4:7], v[72:75], v[128:131], v[4:7]\n"
    "ds_read_b128 v[72:75], v201 offset:18432\n"
    "s_waitcnt lgkmcnt(8)\n"
    "v_mfma_f32_16x16x32_bf16 v[8:11], v[76:79], v[124:127], v[8:11]\n"
    "s_add_u32 m0, s68, 110592\n"
    "s_add_u32 s65, s65, 9216\n"
    "buffer_load_dwordx4 v200, s[40:43], s65 offen lds\n"
    "v_mfma_f32_16x16x32_bf16 v[12:15], v[76:79], v[128:131], v[12:15]\n"
    "ds_read_b128 v[76:79], v201 offset:19456\n"
    "s_waitcnt lgkmcnt(8)\n"
    "v_mfma_f32_16x16x32_bf16 v[16:19], v[80:83], v[124:127], v[16:19]\n"
    "s_add_u32 m0, s69, 110592\n"
    "s_add_u32 s66, s66, 9216\n"
    "buffer_load_dword v222, s[40:43], s66 offen lds\n"
    "v_mfma_f32_16x16x32_bf16 v[20:23], v[80:83], v[128:131], v[20:23]\n"
    "ds_read_b128 v[80:83], v201 offset:20480\n"
    "s_waitcnt lgkmcnt(8)\n"
    "v_mfma_f32_16x16x32_bf16 v[24:27], v[84:87], v[124:127], v[24:27]\n"
    "s_add_u32 m0, s67, 129024\n"
    "s_add_u32 s64, s64, 9216\n"
    "buffer_load_dwordx4 v200, s[40:43], s64 offen lds\n"
    "v_mfma_f32_16x16x32_bf16 v[28:31], v[84:87], v[128:131], v[28:31]\n"
    "ds_read_b128 v[84:87], v201 offset:21504\n"
    "s_waitcnt lgkmcnt(8)\n"
    "v_mfma_f32_16x16x32_bf16 v[32:35], v[88:91], v[124:127], v[32:35]\n"
    "s_add_u32 m0, s68, 129024\n"
    "s_add_u32 s65, s65, 9216\n"
    "buffer_load_dwordx4 v200, s[40:43], s65 offen lds\n"
    "v_mfma_f32_16x16x32_bf16 v[36:39], v[88:91], v[128:131], v[36:39]\n"
    "ds_read_b128 v[88:91], v201 offset:22528\n"
    "s_waitcnt lgkmcnt(8)\n"
    "v_mfma_f32_16x16x32_bf16 v[40:43], v[92:95], v[124:127], v[40:43]\n"
    "s_add_u32 m0, s69, 129024\n"
    "s_add_u32 s66, s66, 9216\n"
    "buffer_load_dword v222, s[40:43], s66 offen lds\n"
    "v_mfma_f32_16x16x32_bf16 v[44:47], v[92:95], v[128:131], v[44:47]\n"
    "ds_read_b128 v[92:95], v201 offset:23552\n"
    "s_waitcnt lgkmcnt(8)\n"
    "v_mfma_f32_16x16x32_bf16 v[48:51], v[96:99], v[124:127], v[48:51]\n"
    "s_add_u32 s63, s63, 8192\n"
    "buffer_load_dwordx4 v[116:119], v200, s[36:39], s63 offen offset:0\n"
    "buffer_load_dwordx4 v[120:123], v200, s[36:39], s63 offen offset:1024\n"
    "v_mfma_f32_16x16x32_bf16 v[52:55], v[96:99], v[128:131], v[52:55]\n"
    "ds_read_b128 v[96:99], v201 offset:24576\n"
    "s_waitcnt lgkmcnt(8)\n"
    "v_mfma_f32_16x16x32_bf16 v[56:59], v[100:103], v[124:127], v[56:59]\n"
    "v_mfma_f32_16x16x32_bf16 v[60:63], v[100:103], v[128:131], v[60:63]\n"
    "ds_read_b128 v[100:103], v201 offset:25600\n"
    "s_waitcnt lgkmcnt(8)\n"
    "v_mfma_f32_16x16x32_bf16 v[64:67], v[104:107], v[124:127], v[64:67]\n"
    "v_mfma_f32_16x16x32_bf16 v[68:71], v[104:107], v[128:131], v[68:71]\n"
    "ds_read_b128 v[104:107], v201 offset:26624\n"
    "s_waitcnt vmcnt(20)\n"
    "s_waitcnt lgkmcnt(8)\n"
    "v_mfma_f32_16x16x32_bf16 v[0:3], v[72:75], v[132:135], v[0:3]\n"
    "s_add_u32 s63, s63, 8192\n"
    "buffer_load_dwordx4 v[124:127], v200, s[36:39], s63 offen offset:0\n"
    "buffer_load_dwordx4 v[128:131], v200, s[36:39], s63 offen offset:1024\n"
    "v_mfma_f32_16x16x32_bf16 v[4:7], v[72:75], v[136:139], v[4:7]\n"
    "ds_read_b128 v[72:75], v201 offset:36864\n"
    "s_waitcnt lgkmcnt(8)\n"
    "v_mfma_f32_16x16x32_bf16 v[8:11], v[76:79], v[132:135], v[8:11]\n"
    "v_mfma_f32_16x16x32_bf16 v[12:15], v[76:79], v[136:139], v[12:15]\n"
    "ds_read_b128 v[76:79], v201 offset:37888\n"
    "s_waitcnt lgkmcnt(8)\n"
    "v_mfma_f32_16x16x32_bf16 v[16:19], v[80:83], v[132:135], v[16:19]\n"
    "v_mfma_f32_16x16x32_bf16 v[20:23], v[80:83], v[136:139], v[20:23]\n"
    "ds_read_b128 v[80:83], v201 offset:38912\n"
    "s_waitcnt lgkmcnt(8)\n"
    "v_mfma_f32_16x16x32_bf16 v[24:27], v[84:87], v[132:135], v[24:27]\n"
    "v_mfma_f32_16x16x32_bf16 v[28:31], v[84:87], v[136:139], v[28:31]\n"
    "ds_read_b128 v[84:87], v201 offset:39936\n"
    "s_waitcnt lgkmcnt(8)\n"
    "v_mfma_f32_16x16x32_bf16 v[32:35], v[88:91], v[132:135], v[32:35]\n"
    "v_mfma_f32_16x16x32_bf16 v[36:39], v[88:91], v[136:139], v[36:39]\n"
    "ds_read_b128 v[88:91], v201 offset:40960\n"
    "s_waitcnt lgkmcnt(8)\n"
    "v_mfma_f32_16x16x32_bf16 v[40:43], v[92:95], v[132:135], v[40:43]\n"
    "v_mfma_f32_16x16x32_bf16 v[44:47], v[92:95], v[136:139], v[44:47]\n"
    "ds_read_b128 v[92:95], v201 offset:41984\n"
    "s_waitcnt lgkmcnt(8)\n"
    "v_mfma_f32_16x16x32_bf16 v[48:51], v[96:99], v[132:135], v[48:51]\n"
    "v_mfma_f32_16x16x32_bf16 v[52:55], v[96:99], v[136:139], v[52:55]\n"
    "ds_read_b128 v[96:99], v201 offset:43008\n"
    "s_waitcnt lgkmcnt(8)\n"
    "v_mfma_f32_16x16x32_bf16 v[56:59], v[100:103], v[132:135], v[56:59]\n"
    "v_mfma_f32_16x16x32_bf16 v[60:63], v[100:103], v[136:139], v[60:63]\n"
    "ds_read_b128 v[100:103], v201 offset:44032\n"
    "s_waitcnt lgkmcnt(8)\n"
    "v_mfma_f32_16x16x32_bf16 v[64:67], v[104:107], v[132:135], v[64:67]\n"
    "v_mfma_f32_16x16x32_bf16 v[68:71], v[104:107], v[136:139], v[68:71]\n"
    "ds_read_b128 v[104:107], v201 offset:45056\n"
    "s_waitcnt vmcnt(17)\n"
    "s_barrier\n"
    "s_waitcnt lgkmcnt(8)\n"
    "v_mfma_f32_16x16x32_bf16 v[0:3], v[72:75], v[140:143], v[0:3]\n"
    "s_add_u32 m0, s67, 0\n"
    "s_add_u32 s64, s64, 9216\n"
    "buffer_load_dwordx4 v200, s[40:43], s64 offen lds\n"
    "v_mfma_f32_16x16x32_bf16 v[4:7], v[72:75], v[144:147], v[4:7]\n"
    "ds_read_b128 v[72:75], v202 offset:0\n"
    "s_waitcnt lgkmcnt(8)\n"
    "v_mfma_f32_16x16x32_bf16 v[8:11], v[76:79], v[140:143], v[8:11]\n"
    "s_add_u32 m0, s68, 0\n"
    "s_add_u32 s65, s65, 9216\n"
    "buffer_load_dwordx4 v200, s[40:43], s65 offen lds\n"
    "v_mfma_f32_16x16x32_bf16 v[12:15], v[76:79], v[144:147], v[12:15]\n"
    "ds_read_b128 v[76:79], v202 offset:1024\n"
    "s_waitcnt lgkmcnt(8)\n"
    "v_mfma_f32_16x16x32_bf16 v[16:19], v[80:83], v[140:143], v[16:19]\n"
    "s_add_u32 m0, s69, 0\n"
    "s_add_u32 s66, s66, 9216\n"
    "buffer_load_dword v222, s[40:43], s66 offen lds\n"
    "v_mfma_f32_16x16x32_bf16 v[20:23], v[80:83], v[144:147], v[20:23]\n"
    "ds_read_b128 v[80:83], v202 offset:2048\n"
    "s_waitcnt lgkmcnt(8)\n"
    "v_mfma_f32_16x16x32_bf16 v[24:27], v[84:87], v[140:143], v[24:27]\n"
    "s_add_u32 m0, s67, 18432\n"
    "s_add_u32 s64, s64, 9216\n"
    "buffer_load_dwordx4 v200, s[40:43], s64 offen lds\n"
    "v_mfma_f32_16x16x32_bf16 v[28:31], v[84:87], v[144:147], v[28:31]\n"
    "ds_read_b128 v[84:87], v202 offset:3072\n"
    "s_waitcnt lgkmcnt(8)\n"
    "v_mfma_f32_16x16x32_bf16 v[32:35], v[88:91], v[140:143], v[32:35]\n"
    "s_add_u32 m0, s68, 18432\n"
    "s_add_u32 s65, s65, 9216\n"
    "buffer_load_dwordx4 v200, s[40:43], s65 offen lds\n"
    "v_mfma_f32_16x16x32_bf16 v[36:39], v[88:91], v[144:147], v[36:39]\n"
    "ds_read_b128 v[88:91], v202 offset:4096\n"
    "s_waitcnt lgkmcnt(8)\n"
    "v_mfma_f32_16x16x32_bf16 v[40:43], v[92:95], v[140:143], v[40:43]\n"
    "s_add_u32 m0, s69, 18432\n"
    "s_add_u32 s66, s66, 9216\n"
    "buffer_load_dword v222, s[40:43], s66 offen lds\n"
    "v_mfma_f32_16x16x32_bf16 v[44:47], v[92:95], v[144:147], v[44:47]\n"
    "ds_read_b128 v[92:95], v202 offset:5120\n"
    "s_waitcnt lgkmcnt(8)\n"
    "v_mfma_f32_16x16x32_bf16 v[48:51], v[96:99], v[140:143], v[48:51]\n"
    "s_add_u32 s63, s63, 8192\n"
    "buffer_load_dwordx4 v[132:135], v200, s[36:39], s63 offen offset:0\n"
    "buffer_load_dwordx4 v[136:139], v200, s[36:39], s63 offen offset:1024\n"
    "v_mfma_f32_16x16x32_bf16 v[52:55], v[96:99], v[144:147], v[52:55]\n"
    "ds_read_b128 v[96:99], v202 offset:6144\n"
    "s_waitcnt lgkmcnt(8)\n"
    "v_mfma_f32_16x16x32_bf16 v[56:59], v[100:103], v[140:143], v[56:59]\n"
    "v_mfma_f32_16x16x32_bf16 v[60:63], v[100:103], v[144:147], v[60:63]\n"
    "ds_read_b128 v[100:103], v202 offset:7168\n"
    "s_waitcnt lgkmcnt(8)\n"
    "v_mfma_f32_16x16x32_bf16 v[64:67], v[104:107], v[140:143], v[64:67]\n"
    "v_mfma_f32_16x16x32_bf16 v[68:71], v[104:107], v[144:147], v[68:71]\n"
    "ds_read_b128 v[104:107], v202 offset:8192\n"
    "s_waitcnt vmcnt(20)\n"
    "s_waitcnt lgkmcnt(8)\n"
    "v_mfma_f32_16x16x32_bf16 v[0:3], v[72:75], v[148:151], v[0:3]\n"
    "s_add_u32 s63, s63, 8192\n"
    "buffer_load_dwordx4 v[140:143], v200, s[36:39], s63 offen offset:0\n"
    "buffer_load_dwordx4 v[144:147], v200, s[36:39], s63 offen offset:1024\n"
    "v_mfma_f32_16x16x32_bf16 v[4:7], v[72:75], v[152:155], v[4:7]\n"
    "ds_read_b128 v[72:75], v202 offset:18432\n"
    "s_waitcnt lgkmcnt(8)\n"
    "v_mfma_f32_16x16x32_bf16 v[8:11], v[76:79], v[148:151], v[8:11]\n"
    "v_mfma_f32_16x16x32_bf16 v[12:15], v[76:79], v[152:155], v[12:15]\n"
    "ds_read_b128 v[76:79], v202 offset:19456\n"
    "s_waitcnt lgkmcnt(8)\n"
    "v_mfma_f32_16x16x32_bf16 v[16:19], v[80:83], v[148:151], v[16:19]\n"
    "v_mfma_f32_16x16x32_bf16 v[20:23], v[80:83], v[152:155], v[20:23]\n"
    "ds_read_b128 v[80:83], v202 offset:20480\n"
    "s_waitcnt lgkmcnt(8)\n"
    "v_mfma_f32_16x16x32_bf16 v[24:27], v[84:87], v[148:151], v[24:27]\n"
    "v_mfma_f32_16x16x32_bf16 v[28:31], v[84:87], v[152:155], v[28:31]\n"
    "ds_read_b128 v[84:87], v202 offset:21504\n"
    "s_waitcnt lgkmcnt(8)\n"
    "v_mfma_f32_16x16x32_bf16 v[32:35], v[88:91], v[148:151], v[32:35]\n"
    "v_mfma_f32_16x16x32_bf16 v[36:39], v[88:91], v[152:155], v[36:39]\n"
    "ds_read_b128 v[88:91], v202 offset:22528\n"
    "s_waitcnt lgkmcnt(8)\n"
    "v_mfma_f32_16x16x32_bf16 v[40:43], v[92:95], v[148:151], v[40:43]\n"
    "v_mfma_f32_16x16x32_bf16 v[44:47], v[92:95], v[152:155], v[44:47]\n"
    "ds_read_b128 v[92:95], v202 offset:23552\n"
    "s_waitcnt lgkmcnt(8)\n"
    "v_mfma_f32_16x16x32_bf16 v[48:51], v[96:99], v[148:151], v[48:51]\n"
    "v_mfma_f32_16x16x32_bf16 v[52:55], v[96:99], v[152:155], v[52:55]\n"
    "ds_read_b128 v[96:99], v202 offset:24576\n"
    "s_waitcnt lgkmcnt(8)\n"
    "v_mfma_f32_16x16x32_bf16 v[56:59], v[100:103], v[148:151], v[56:59]\n"
    "v_mfma_f32_16x16x32_bf16 v[60:63], v[100:103], v[152:155], v[60:63]\n"
    "ds_read_b128 v[100:103], v202 offset:25600\n"
    "s_waitcnt lgkmcnt(8)\n"
    "v_mfma_f32_16x16x32_bf16 v[64:67], v[104:107], v[148:151], v[64:67]\n"
    "v_mfma_f32_16x16x32_bf16 v[68:71], v[104:107], v[152:155], v[68:71]\n"
    "ds_read_b128 v[104:107], v202 offset:26624\n"
    "s_waitcnt vmcnt(17)\n"
    "s_barrier\n"
    "s_waitcnt lgkmcnt(8)\n"
    "v_mfma_f32_16x16x32_bf16 v[0:3], v[72:75], v[108:111], v[0:3]\n"
    "s_add_u32 m0, s67, 36864\n"
    "s_add_u32 s64, s64, 9216\n"
    "buffer_load_dwordx4 v200, s[40:43], s64 offen lds\n"
    "v_mfma_f32_16x16x32_bf16 v[4:7], v[72:75], v[112:115], v[4:7]\n"
    "ds_read_b128 v[72:75], v202 offset:36864\n"
    "s_waitcnt lgkmcnt(8)\n"
    "v_mfma_f32_16x16x32_bf16 v[8:11], v[76:79], v[108:111], v[8:11]\n"
    "s_add_u32 m0, s68, 36864\n"
    "s_add_u32 s65, s65, 9216\n"
    "buffer_load_dwordx4 v200, s[40:43], s65 offen lds\n"
    "v_mfma_f32_16x16x32_bf16 v[12:15], v[76:79], v[112:115], v[12:15]\n"
    "ds_read_b128 v[76:79], v202 offset:37888\n"
    "s_waitcnt lgkmcnt(8)\n"
    "v_mfma_f32_16x16x32_bf16 v[16:19], v[80:83], v[108:111], v[16:19]\n"
    "s_add_u32 m0, s69, 36864\n"
    "s_add_u32 s66, s66, 9216\n"
    "buffer_load_dword v222, s[40:43], s66 offen lds\n"
    "v_mfma_f32_16x16x32_bf16 v[20:23], v[80:83], v[112:115], v[20:23]\n"
    "ds_read_b128 v[80:83], v202 offset:38912\n"
    "s_waitcnt lgkmcnt(8)\n"
    "v_mfma_f32_16x16x32_bf16 v[24:27], v[84:87], v[108:111], v[24:27]\n"
    "s_add_u32 m0, s67, 55296\n"
    "s_add_u32 s64, s64, 9216\n"
    "buffer_load_dwordx4 v200, s[40:43], s64 offen lds\n"
    "v_mfma_f32_16x16x32_bf16 v[28:31], v[84:87], v[112:115], v[28:31]\n"
    "ds_read_b128 v[84:87], v202 offset:39936\n"
    "s_waitcnt lgkmcnt(8)\n"
    "v_mfma_f32_16x16x32_bf16 v[32:35], v[88:91], v[108:111], v[32:35]\n"
    "s_add_u32 m0, s68, 55296\n"
    "s_add_u32 s65, s65, 9216\n"
    "buffer_load_dwordx4 v200, s[40:43], s65 offen lds\n"
    "v_mfma_f32_16x16x32_bf16 v[36:39], v[88:91], v[112:115], v[36:39]\n"
    "ds_read_b128 v[88:91], v202 offset:40960\n"
    "s_waitcnt lgkmcnt(8)\n"
    "v_mfma_f32_16x16x32_bf16 v[40:43], v[92:95], v[108:111], v[40:43]\n"
    "s_add_u32 m0, s69, 55296\n"
    "s_add_u32 s66, s66, 9216\n"
    "buffer_load_dword v222, s[40:43], s66 offen lds\n"
    "v_mfma_f32_16x16x32_bf16 v[44:47], v[92:95], v[112:115], v[44:47]\n"
    "ds_read_b128 v[92:95], v202 offset:41984\n"
    "s_waitcnt lgkmcnt(8)\n"
    "v_mfma_f32_16x16x32_bf16 v[48:51], v[96:99], v[108:111], v[48:51]\n"
    "s_add_u32 s63, s63, 8192\n"
    "buffer_load_dwordx4 v[148:151], v200, s[36:39], s63 offen offset:0\n"
    "buffer_load_dwordx4 v[152:155], v200, s[36:39], s63 offen offset:1024\n"
    "v_mfma_f32_16x16x32_bf16 v[52:55], v[96:99], v[112:115], v[52:55]\n"
    "ds_read_b128 v[96:99], v202 offset:43008\n"
    "s_waitcnt lgkmcnt(8)\n"
    "v_mfma_f32_16x16x32_bf16 v[56:59], v[100:103], v[108:111], v[56:59]\n"
    "v_mfma_f32_16x16x32_bf16 v[60:63], v[100:103], v[112:115], v[60:63]\n"
    "ds_read_b128 v[100:103], v202 offset:44032\n"
    "s_waitcnt lgkmcnt(8)\n"
    "v_mfma_f32_16x16x32_bf16 v[64:67], v[104:107], v[108:111], v[64:67]\n"
    "v_mfma_f32_16x16x32_bf16 v[68:71], v[104:107], v[112:115], v[68:71]\n"
    "ds_read_b128 v[104:107], v202 offset:45056\n"
    "s_waitcnt vmcnt(20)\n"
    "s_waitcnt lgkmcnt(8)\n"
    "v_mfma_f32_16x16x32_bf16 v[0:3], v[72:75], v[116:119], v[0:3]\n"
    "s_add_u32 s63, s63, 8192\n"
    "buffer_load_dwordx4 v[108:111], v200, s[36:39], s63 offen offset:0\n"
    "buffer_load_dwordx4 v[112:115], v200, s[36:39], s63 offen offset:1024\n"
    "v_mfma_f32_16x16x32_bf16 v[4:7], v[72:75], v[120:123], v[4:7]\n"
    "ds_read_b128 v[72:75], v205 offset:0\n"
    "s_waitcnt lgkmcnt(8)\n"
    "v_mfma_f32_16x16x32_bf16 v[8:11], v[76:79], v[116:119], v[8:11]\n"
    "v_mfma_f32_16x16x32_bf16 v[12:15], v[76:79], v[120:123], v[12:15]\n"
    "ds_read_b128 v[76:79], v205 offset:1024\n"
    "s_waitcnt lgkmcnt(8)\n"
    "v_mfma_f32_16x16x32_bf16 v[16:19], v[80:83], v[116:119], v[16:19]\n"
    "v_mfma_f32_16x16x32_bf16 v[20:23], v[80:83], v[120:123], v[20:23]\n"
    "ds_read_b128 v[80:83], v205 offset:2048\n"
    "s_waitcnt lgkmcnt(8)\n"
    "v_mfma_f32_16x16x32_bf16 v[24:27], v[84:87], v[116:119], v[24:27]\n"
    "v_mfma_f32_16x16x32_bf16 v[28:31], v[84:87], v[120:123], v[28:31]\n"
    "ds_read_b128 v[84:87], v205 offset:3072\n"
    "s_waitcnt lgkmcnt(8)\n"
    "v_mfma_f32_16x16x32_bf16 v[32:35], v[88:91], v[116:119], v[32:35]\n"
    "v_mfma_f32_16x16x32_bf16 v[36:39], v[88:91], v[120:123], v[36:39]\n"
    "ds_read_b128 v[88:91], v205 offset:4096\n"
    "s_waitcnt lgkmcnt(8)\n"
    "v_mfma_f32_16x16x32_bf16 v[40:43], v[92:95], v[116:119], v[40:43]\n"
    "v_mfma_f32_16x16x32_bf16 v[44:47], v[92:95], v[120:123], v[44:47]\n"
    "ds_read_b128 v[92:95], v205 offset:5120\n"
    "s_waitcnt lgkmcnt(8)\n"
    "v_mfma_f32_16x16x32_bf16 v[48:51], v[96:99], v[116:119], v[48:51]\n"
    "v_mfma_f32_16x16x32_bf16 v[52:55], v[96:99], v[120:123], v[52:55]\n"
    "ds_read_b128 v[96:99], v205 offset:6144\n"
    "s_waitcnt lgkmcnt(8)\n"
    "v_mfma_f32_16x16x32_bf16 v[56:59], v[100:103], v[116:119], v[56:59]\n"
    "v_mfma_f32_16x16x32_bf16 v[60:63], v[100:103], v[120:123], v[60:63]\n"
    "ds_read_b128 v[100:103], v205 offset:7168\n"
    "s_waitcnt lgkmcnt(8)\n"
    "v_mfma_f32_16x16x32_bf16 v[64:67], v[104:107], v[116:119], v[64:67]\n"
    "v_mfma_f32_16x16x32_bf16 v[68:71], v[104:107], v[120:123], v[68:71]\n"
    "ds_read_b128 v[104:107], v205 offset:8192\n"
    "s_waitcnt vmcnt(17)\n"
    "s_barrier\n"
    "s_waitcnt lgkmcnt(8)\n"
    "v_mfma_f32_16x16x32_bf16 v[0:3], v[72:75], v[124:127], v[0:3]\n"
    "s_add_u32 m0, s67, 73728\n"
    "s_add_u32 s64, s64, 9216\n"
    "buffer_load_dwordx4 v200, s[40:43], s64 offen lds\n"
    "v_mfma_f32_16x16x32_bf16 v[4:7], v[72:75], v[128:131], v[4:7]\n"
    "ds_read_b128 v[72:75], v205 offset:18432\n"
    "s_waitcnt lgkmcnt(8)\n"
    "v_mfma_f32_16x16x32_bf16 v[8:11], v[76:79], v[124:127], v[8:11]\n"
    "s_add_u32 m0, s68, 73728\n"
    "s_add_u32 s65, s65, 9216\n"
    "buffer_load_dwordx4 v200, s[40:43], s65 offen lds\n"
    "v_mfma_f32_16x16x32_bf16 v[12:15], v[76:79], v[128:131], v[12:15]\n"
    "ds_read_b128 v[76:79], v205 offset:19456\n"
    "s_waitcnt lgkmcnt(8)\n"
    "v_mfma_f32_16x16x32_bf16 v[16:19], v[80:83], v[124:127], v[16:19]\n"
    "s_add_u32 m0, s69, 73728\n"
    "s_add_u32 s66, s66, 9216\n"
    "buffer_load_dword v222, s[40:43], s66 offen lds\n"
    "v_mfma_f32_16x16x32_bf16 v[20:23], v[80:83], v[128:131], v[20:23]\n"
    "ds_read_b128 v[80:83], v205 offset:20480\n"
    "s_waitcnt lgkmcnt(8)\n"
    "v_mfma_f32_16x16x32_bf16 v[24:27], v[84:87], v[124:127], v[24:27]\n"
    "s_add_u32 m0, s67, 92160\n"
    "s_add_u32 s64, s64, 9216\n"
    "buffer_load_dwordx4 v200, s[40:43], s64 offen lds\n"
    "v_mfma_f32_16x16x32_bf16 v[28:31], v[84:87], v[128:131], v[28:31]\n"
    "ds_read_b128 v[84:87], v205 offset:21504\n"
    "s_waitcnt lgkmcnt(8)\n"
    "v_mfma_f32_16x16x32_bf16 v[32:35], v[88:91], v[124:127], v[32:35]\n"
    "s_add_u32 m0, s68, 92160\n"
    "s_add_u32 s65, s65, 9216\n"
    "buffer_load_dwordx4 v200, s[40:43], s65 offen lds\n"
    "v_mfma_f32_16x16x32_bf16 v[36:39], v[88:91], v[128:131], v[36:39]\n"
    "ds_read_b128 v[88:91], v205 offset:22528\n"
    "s_waitcnt lgkmcnt(8)\n"
    "v_mfma_f32_16x16x32_bf16 v[40:43], v[92:95], v[124:127], v[40:43]\n"
    "s_add_u32 m0, s69, 92160\n"
    "s_add_u32 s66, s66, 9216\n"
    "buffer_load_dword v222, s[40:43], s66 offen lds\n"
    "v_mfma_f32_16x16x32_bf16 v[44:47], v[92:95], v[128:131], v[44:47]\n"
    "ds_read_b128 v[92:95], v205 offset:23552\n"
    "s_waitcnt lgkmcnt(8)\n"
    "v_mfma_f32_16x16x32_bf16 v[48:51], v[96:99], v[124:127], v[48:51]\n"
    "s_add_u32 s63, s63, 8192\n"
    "buffer_load_dwordx4 v[116:119], v200, s[36:39], s63 offen offset:0\n"
    "buffer_load_dwordx4 v[120:123], v200, s[36:39], s63 offen offset:1024\n"
    "v_mfma_f32_16x16x32_bf16 v[52:55], v[96:99], v[128:131], v[52:55]\n"
    "ds_read_b128 v[96:99], v205 offset:24576\n"
    "s_waitcnt lgkmcnt(8)\n"
    "v_mfma_f32_16x16x32_bf16 v[56:59], v[100:103], v[124:127], v[56:59]\n"
    "v_mfma_f32_16x16x32_bf16 v[60:63], v[100:103], v[128:131], v[60:63]\n"
    "ds_read_b128 v[100:103], v205 offset:25600\n"
    "s_waitcnt lgkmcnt(8)\n"
    "v_mfma_f32_16x16x32_bf16 v[64:67], v[104:107], v[124:127], v[64:67]\n"
    "v_mfma_f32_16x16x32_bf16 v[68:71], v[104:107], v[128:131], v[68:71]\n"
    "ds_read_b128 v[104:107], v205 offset:26624\n"
    "s_waitcnt vmcnt(20)\n"
    "s_waitcnt lgkmcnt(8)\n"
    "v_mfma_f32_16x16x32_bf16 v[0:3], v[72:75], v[132:135], v[0:3]\n"
    "s_add_u32 s63, s63, 8192\n"
    "buffer_load_dwordx4 v[124:127], v200, s[36:39], s63 offen offset:0\n"
    "buffer_load_dwordx4 v[128:131], v200, s[36:39], s63 offen offset:1024\n"
    "v_mfma_f32_16x16x32_bf16 v[4:7], v[72:75], v[136:139], v[4:7]\n"
    "ds_read_b128 v[72:75], v201 offset:0\n"
    "s_waitcnt lgkmcnt(8)\n"
    "v_mfma_f32_16x16x32_bf16 v[8:11], v[76:79], v[132:135], v[8:11]\n"
    "v_mfma_f32_16x16x32_bf16 v[12:15], v[76:79], v[136:139], v[12:15]\n"
    "ds_read_b128 v[76:79], v201 offset:1024\n"
    "s_waitcnt lgkmcnt(8)\n"
    "v_mfma_f32_16x16x32_bf16 v[16:19], v[80:83], v[132:135], v[16:19]\n"
    "v_mfma_f32_16x16x32_bf16 v[20:23], v[80:83], v[136:139], v[20:23]\n"
    "ds_read_b128 v[80:83], v201 offset:2048\n"
    "s_waitcnt lgkmcnt(8)\n"
    "v_mfma_f32_16x16x32_bf16 v[24:27], v[84:87], v[132:135], v[24:27]\n"
    "v_mfma_f32_16x16x32_bf16 v[28:31], v[84:87], v[136:139], v[28:31]\n"
    "ds_read_b128 v[84:87], v201 offset:3072\n"
    "s_waitcnt lgkmcnt(8)\n"
    "v_mfma_f32_16x16x32_bf16 v[32:35], v[88:91], v[132:135], v[32:35]\n"
    "v_mfma_f32_16x16x32_bf16 v[36:39], v[88:91], v[136:139], v[36:39]\n"
    "ds_read_b128 v[88:91], v201 offset:4096\n"
    "s_waitcnt lgkmcnt(8)\n"
    "v_mfma_f32_16x16x32_bf16 v[40:43], v[92:95], v[132:135], v[40:43]\n"
    "v_mfma_f32_16x16x32_bf16 v[44:47], v[92:95], v[136:139], v[44:47]\n"
    "ds_read_b128 v[92:95], v201 offset:5120\n"
    "s_waitcnt lgkmcnt(8)\n"
    "v_mfma_f32_16x16x32_bf16 v[48:51], v[96:99], v[132:135], v[48:51]\n"
    "v_mfma_f32_16x16x32_bf16 v[52:55], v[96:99], v[136:139], v[52:55]\n"
    "ds_read_b128 v[96:99], v201 offset:6144\n"
    "s_waitcnt lgkmcnt(8)\n"
    "v_mfma_f32_16x16x32_bf16 v[56:59], v[100:103], v[132:135], v[56:59]\n"
    "v_mfma_f32_16x16x32_bf16 v[60:63], v[100:103], v[136:139], v[60:63]\n"
    "ds_read_b128 v[100:103], v201 offset:7168\n"
    "s_waitcnt lgkmcnt(8)\n"
    "v_mfma_f32_16x16x32_bf16 v[64:67], v[104:107], v[132:135], v[64:67]\n"
    "v_mfma_f32_16x16x32_bf16 v[68:71], v[104:107], v[136:139], v[68:71]\n"
    "ds_read_b128 v[104:107], v201 offset:8192\n"
    "s_waitcnt vmcnt(17)\n"
    "s_barrier\n"
    "s_waitcnt lgkmcnt(8)\n"
    "v_mfma_f32_16x16x32_bf16 v[0:3], v[72:75], v[140:143], v[0:3]\n"
    "s_add_u32 m0, s67, 110592\n"
    "s_add_u32 s64, s64, 9216\n"
    "buffer_load_dwordx4 v200, s[40:43], s64 offen lds\n"
    "v_mfma_f32_16x16x32_bf16 v[4:7], v[72:75], v[144:147], v[4:7]\n"
    "ds_read_b128 v[72:75], v201 offset:18432\n"
    "s_waitcnt lgkmcnt(8)\n"
    "v_mfma_f32_16x16x32_bf16 v[8:11], v[76:79], v[140:143], v[8:11]\n"
    "s_add_u32 m0, s68, 110592\n"
    "s_add_u32 s65, s65, 9216\n"
    "buffer_load_dwordx4 v200, s[40:43], s65 offen lds\n"
    "v_mfma_f32_16x16x32_bf16 v[12:15], v[76:79], v[144:147], v[12:15]\n"
    "ds_read_b128 v[76:79], v201 offset:19456\n"
    "s_waitcnt lgkmcnt(8)\n"
    "v_mfma_f32_16x16x32_bf16 v[16:19], v[80:83], v[140:143], v[16:19]\n"
    "s_add_u32 m0, s69, 110592\n"
    "s_add_u32 s66, s66, 9216\n"
    "buffer_load_dword v222, s[40:43], s66 offen lds\n"
    "v_mfma_f32_16x16x32_bf16 v[20:23], v[80:83], v[144:147], v[20:23]\n"
    "ds_read_b128 v[80:83], v201 offset:20480\n"
    "s_waitcnt lgkmcnt(8)\n"
    "v_mfma_f32_16x16x32_bf16 v[24:27], v[84:87], v[140:143], v[24:27]\n"
    "s_add_u32 m0, s67, 129024\n"
    "s_add_u32 s64, s64, 9216\n"
    "buffer_load_dwordx4 v200, s[40:43], s64 offen lds\n"
    "v_mfma_f32_16x16x32_bf16 v[28:31], v[84:87], v[144:147], v[28:31]\n"
    "ds_read_b128 v[84:87], v201 offset:21504\n"
    "s_waitcnt lgkmcnt(8)\n"
    "v_mfma_f32_16x16x32_bf16 v[32:35], v[88:91], v[140:143], v[32:35]\n"
    "s_add_u32 m0, s68, 129024\n"
    "s_add_u32 s65, s65, 9216\n"
    "buffer_load_dwordx4 v200, s[40:43], s65 offen lds\n"
    "v_mfma_f32_16x16x32_bf16 v[36:39], v[88:91], v[144:147], v[36:39]\n"
    "ds_read_b128 v[88:91], v201 offset:22528\n"
    "s_waitcnt lgkmcnt(8)\n"
    "v_mfma_f32_16x16x32_bf16 v[40:43], v[92:95], v[140:143], v[40:43]\n"
    "s_add_u32 m0, s69, 129024\n"
    "s_add_u32 s66, s66, 9216\n"
    "buffer_load_dword v222, s[40:43], s66 offen lds\n"
    "v_mfma_f32_16x16x32_bf16 v[44:47], v[92:95], v[144:147], v[44:47]\n"
    "ds_read_b128 v[92:95], v201 offset:23552\n"
    "s_waitcnt lgkmcnt(8)\n"
    "v_mfma_f32_16x16x32_bf16 v[48:51], v[96:99], v[140:143], v[48:51]\n"
    "s_add_u32 s63, s63, 8192\n"
    "buffer_load_dwordx4 v[132:135], v200, s[36:39], s63 offen offset:0\n"
    "buffer_load_dwordx4 v[136:139], v200, s[36:39], s63 offen offset:1024\n"
    "v_mfma_f32_16x16x32_bf16 v[52:55], v[96:99], v[144:147], v[52:55]\n"
    "ds_read_b128 v[96:99], v201 offset:24576\n"
    "s_waitcnt lgkmcnt(8)\n"
    "v_mfma_f32_16x16x32_bf16 v[56:59], v[100:103], v[140:143], v[56:59]\n"
    "v_mfma_f32_16x16x32_bf16 v[60:63], v[100:103], v[144:147], v[60:63]\n"
    "ds_read_b128 v[100:103], v201 offset:25600\n"
    "s_waitcnt lgkmcnt(8)\n"
    "v_mfma_f32_16x16x32_bf16 v[64:67], v[104:107], v[140:143], v[64:67]\n"
    "v_mfma_f32_16x16x32_bf16 v[68:71], v[104:107], v[144:147], v[68:71]\n"
    "ds_read_b128 v[104:107], v201 offset:26624\n"
    "s_waitcnt vmcnt(20)\n"
    "s_waitcnt lgkmcnt(8)\n"
    "v_mfma_f32_16x16x32_bf16 v[0:3], v[72:75], v[148:151], v[0:3]\n"
    "s_add_u32 s63, s63, 8192\n"
    "buffer_load_dwordx4 v[140:143], v200, s[36:39], s63 offen offset:0\n"
    "buffer_load_dwordx4 v[144:147], v200, s[36:39], s63 offen offset:1024\n"
    "v_mfma_f32_16x16x32_bf16 v[4:7], v[72:75], v[152:155], v[4:7]\n"
    "ds_read_b128 v[72:75], v201 offset:36864\n"
    "s_waitcnt lgkmcnt(8)\n"
    "v_mfma_f32_16x16x32_bf16 v[8:11], v[76:79], v[148:151], v[8:11]\n"
    "v_mfma_f32_16x16x32_bf16 v[12:15], v[76:79], v[152:155], v[12:15]\n"
    "ds_read_b128 v[76:79], v201 offset:37888\n"
    "s_waitcnt lgkmcnt(8)\n"
    "v_mfma_f32_16x16x32_bf16 v[16:19], v[80:83], v[148:151], v[16:19]\n"
    "v_mfma_f32_16x16x32_bf16 v[20:23], v[80:83], v[152:155], v[20:23]\n"
    "ds_read_b128 v[80:83], v201 offset:38912\n"
    "s_waitcnt lgkmcnt(8)\n"
    "v_mfma_f32_16x16x32_bf16 v[24:27], v[84:87], v[148:151], v[24:27]\n"
    "v_mfma_f32_16x16x32_bf16 v[28:31], v[84:87], v[152:155], v[28:31]\n"
    "ds_read_b128 v[84:87], v201 offset:39936\n"
    "s_waitcnt lgkmcnt(8)\n"
    "v_mfma_f32_16x16x32_bf16 v[32:35], v[88:91], v[148:151], v[32:35]\n"
    "v_mfma_f32_16x16x32_bf16 v[36:39], v[88:91], v[152:155], v[36:39]\n"
    "ds_read_b128 v[88:91], v201 offset:40960\n"
    "s_waitcnt lgkmcnt(8)\n"
    "v_mfma_f32_16x16x32_bf16 v[40:43], v[92:95], v[148:151], v[40:43]\n"
    "v_mfma_f32_16x16x32_bf16 v[44:47], v[92:95], v[152:155], v[44:47]\n"
    "ds_read_b128 v[92:95], v201 offset:41984\n"
    "s_waitcnt lgkmcnt(8)\n"
    "v_mfma_f32_16x16x32_bf16 v[48:51], v[96:99], v[148:151], v[48:51]\n"
    "v_mfma_f32_16x16x32_bf16 v[52:55], v[96:99], v[152:155], v[52:55]\n"
    "ds_read_b128 v[96:99], v201 offset:43008\n"
    "s_waitcnt lgkmcnt(8)\n"
    "v_mfma_f32_16x16x32_bf16 v[56:59], v[100:103], v[148:151], v[56:59]\n"
    "v_mfma_f32_16x16x32_bf16 v[60:63], v[100:103], v[152:155], v[60:63]\n"
    "ds_read_b128 v[100:103], v201 offset:44032\n"
    "s_waitcnt lgkmcnt(8)\n"
    "v_mfma_f32_16x16x32_bf16 v[64:67], v[104:107], v[148:151], v[64:67]\n"
    "v_mfma_f32_16x16x32_bf16 v[68:71], v[104:107], v[152:155], v[68:71]\n"
    "ds_read_b128 v[104:107], v201 offset:45056\n"
    "s_waitcnt vmcnt(17)\n"
    "s_barrier\n"
    "s_waitcnt lgkmcnt(8)\n"
    "v_mfma_f32_16x16x32_bf16 v[0:3], v[72:75], v[108:111], v[0:3]\n"
    "s_mov_b64 s[40:41], %20\n"
    "s_and_b32 s41, s41, 0xffff\n"
    "s_mov_b32 s42, 175104\n"
    "s_mul_i32 s59, s62, 405504\n"
    "s_add_u32 s59, s59, 156672\n"
    "s_sub_u32 s64, s64, s59\n"
    "s_add_u32 s60, s59, 202752\n"
    "s_sub_u32 s66, s66, s60\n"
    "s_cmp_eq_u32 s56, 0\n"
    "s_cselect_b32 s59, s59, s60\n"
    "s_sub_u32 s65, s65, s59\n"
    "s_add_u32 m0, s67, 0\n"
    "s_add_u32 s64, s64, 9216\n"
    "buffer_load_dwordx4 v200, s[40:43], s64 offen lds\n"
    "v_mfma_f32_16x16x32_bf16 v[4:7], v[72:75], v[112:115], v[4:7]\n"
    "ds_read_b128 v[72:75], v202 offset:0\n"
    "s_waitcnt lgkmcnt(8)\n"
    "v_mfma_f32_16x16x32_bf16 v[8:11], v[76:79], v[108:111], v[8:11]\n"
    "s_add_u32 m0, s68, 0\n"
    "s_add_u32 s65, s65, 9216\n"
    "buffer_load_dwordx4 v200, s[40:43], s65 offen lds\n"
    "v_mfma_f32_16x16x32_bf16 v[12:15], v[76:79], v[112:115], v[12:15]\n"
    "ds_read_b128 v[76:79], v202 offset:1024\n"
    "s_waitcnt lgkmcnt(8)\n"
    "v_mfma_f32_16x16x32_bf16 v[16:19], v[80:83], v[108:111], v[16:19]\n"
    "s_add_u32 m0, s69, 0\n"
    "s_add_u32 s66, s66, 9216\n"
    "buffer_load_dword v222, s[40:43], s66 offen lds\n"
    "v_mfma_f32_16x16x32_bf16 v[20:23], v[80:83], v[112:115], v[20:23]\n"
    "ds_read_b128 v[80:83], v202 offset:2048\n"
    "s_waitcnt lgkmcnt(8)\n"
    "v_mfma_f32_16x16x32_bf16 v[24:27], v[84:87], v[108:111], v[24:27]\n"
    "s_add_u32 m0, s67, 18432\n"
    "s_add_u32 s64, s64, 9216\n"
    "buffer_load_dwordx4 v200, s[40:43], s64 offen lds\n"
    "v_mfma_f32_16x16x32_bf16 v[28:31], v[84:87], v[112:115], v[28:31]\n"
    "ds_read_b128 v[84:87], v202 offset:3072\n"
    "s_waitcnt lgkmcnt(8)\n"
    "v_mfma_f32_16x16x32_bf16 v[32:35], v[88:91], v[108:111], v[32:35]\n"
    "s_add_u32 m0, s68, 18432\n"
    "s_add_u32 s65, s65, 9216\n"
    "buffer_load_dwordx4 v200, s[40:43], s65 offen lds\n"
    "v_mfma_f32_16x16x32_bf16 v[36:39], v[88:91], v[112:115], v[36:39]\n"
    "ds_read_b128 v[88:91], v202 offset:4096\n"
    "s_waitcnt lgkmcnt(8)\n"
    "v_mfma_f32_16x16x32_bf16 v[40:43], v[92:95], v[108:111], v[40:43]\n"
    "s_add_u32 m0, s69, 18432\n"
    "s_add_u32 s66, s66, 9216\n"
    "buffer_load_dword v222, s[40:43], s66 offen lds\n"
    "v_mfma_f32_16x16x32_bf16 v[44:47], v[92:95], v[112:115], v[44:47]\n"
    "ds_read_b128 v[92:95], v202 offset:5120\n"
    "s_waitcnt lgkmcnt(8)\n"
    "v_mfma_f32_16x16x32_bf16 v[48:51], v[96:99], v[108:111], v[48:51]\n"
    "s_add_u32 s63, s63, 8192\n"
    "buffer_load_dwordx4 v[148:151], v200, s[36:39], s63 offen offset:0\n"
    "buffer_load_dwordx4 v[152:155], v200, s[36:39], s63 offen offset:1024\n"
    "v_mfma_f32_16x16x32_bf16 v[52:55], v[96:99], v[112:115], v[52:55]\n"
    "ds_read_b128 v[96:99], v202 offset:6144\n"
    "s_waitcnt lgkmcnt(8)\n"
    "v_mfma_f32_16x16x32_bf16 v[56:59], v[100:103], v[108:111], v[56:59]\n"
    "v_mfma_f32_16x16x32_bf16 v[60:63], v[100:103], v[112:115], v[60:63]\n"
    "ds_read_b128 v[100:103], v202 offset:7168\n"
    "s_waitcnt lgkmcnt(8)\n"
    "v_mfma_f32_16x16x32_bf16 v[64:67], v[104:107], v[108:111], v[64:67]\n"
    "v_mfma_f32_16x16x32_bf16 v[68:71], v[104:107], v[112:115], v[68:71]\n"
    "ds_read_b128 v[104:107], v202 offset:8192\n"
    "s_waitcnt vmcnt(20)\n"
    "s_waitcnt lgkmcnt(8)\n"
    "v_mfma_f32_16x16x32_bf16 v[0:3], v[72:75], v[116:119], v[0:3]\n"
    "v_mfma_f32_16x16x32_bf16 v[4:7], v[72:75], v[120:123], v[4:7]\n"
    "ds_read_b128 v[72:75], v202 offset:18432\n"
    "s_waitcnt lgkmcnt(8)\n"
    "v_mfma_f32_16x16x32_bf16 v[8:11], v[76:79], v[116:119], v[8:11]\n"
    "v_mfma_f32_16x16x32_bf16 v[12:15], v[76:79], v[120:123], v[12:15]\n"
    "ds_read_b128 v[76:79], v202 offset:19456\n"
    "s_waitcnt lgkmcnt(8)\n"
    "v_mfma_f32_16x16x32_bf16 v[16:19], v[80:83], v[116:119], v[16:19]\n"
    "v_mfma_f32_16x16x32_bf16 v[20:23], v[80:83], v[120:123], v[20:23]\n"
    "ds_read_b128 v[80:83], v202 offset:20480\n"
    "s_waitcnt lgkmcnt(8)\n"
    "v_mfma_f32_16x16x32_bf16 v[24:27], v[84:87], v[116:119], v[24:27]\n"
    "v_mfma_f32_16x16x32_bf16 v[28:31], v[84:87], v[120:123], v[28:31]\n"
    "ds_read_b128 v[84:87], v202 offset:21504\n"
    "s_waitcnt lgkmcnt(8)\n"
    "v_mfma_f32_16x16x32_bf16 v[32:35], v[88:91], v[116:119], v[32:35]\n"
    "v_mfma_f32_16x16x32_bf16 v[36:39], v[88:91], v[120:123], v[36:39]\n"
    "ds_read_b128 v[88:91], v202 offset:22528\n"
    "s_waitcnt lgkmcnt(8)\n"
    "v_mfma_f32_16x16x32_bf16 v[40:43], v[92:95], v[116:119], v[40:43]\n"
    "v_mfma_f32_16x16x32_bf16 v[44:47], v[92:95], v[120:123], v[44:47]\n"
    "ds_read_b128 v[92:95], v202 offset:23552\n"
    "s_waitcnt lgkmcnt(8)\n"
    "v_mfma_f32_16x16x32_bf16 v[48:51], v[96:99], v[116:119], v[48:51]\n"
    "v_mfma_f32_16x16x32_bf16 v[52:55], v[96:99], v[120:123], v[52:55]\n"
    "ds_read_b128 v[96:99], v202 offset:24576\n"
    "s_waitcnt lgkmcnt(8)\n"
    "v_mfma_f32_16x16x32_bf16 v[56:59], v[100:103], v[116:119], v[56:59]\n"
    "v_mfma_f32_16x16x32_bf16 v[60:63], v[100:103], v[120:123], v[60:63]\n"
    "ds_read_b128 v[100:103], v202 offset:25600\n"
    "s_waitcnt lgkmcnt(8)\n"
    "v_mfma_f32_16x16x32_bf16 v[64:67], v[104:107], v[116:119], v[64:67]\n"
    "v_mfma_f32_16x16x32_bf16 v[68:71], v[104:107], v[120:123], v[68:71]\n"
    "ds_read_b128 v[104:107], v202 offset:26624\n"
    "s_waitcnt vmcnt(15)\n"
    "s_barrier\n"
    "s_waitcnt lgkmcnt(8)\n"
    "v_mfma_f32_16x16x32_bf16 v[0:3], v[72:75], v[124:127], v[0:3]\n"
    "v_mfma_f32_16x16x32_bf16 v[4:7], v[72:75], v[128:131], v[4:7]\n"
    "ds_read_b128 v[72:75], v202 offset:36864\n"
    "s_waitcnt lgkmcnt(8)\n"
    "v_mfma_f32_16x16x32_bf16 v[8:11], v[76:79], v[124:127], v[8:11]\n"
    "v_mfma_f32_16x16x32_bf16 v[12:15], v[76:79], v[128:131], v[12:15]\n"
    "ds_read_b128 v[76:79], v202 offset:37888\n"
    "s_waitcnt lgkmcnt(8)\n"
    "v_mfma_f32_16x16x32_bf16 v[16:19], v[80:83], v[124:127], v[16:19]\n"
    "v_mfma_f32_16x16x32_bf16 v[20:23], v[80:83], v[128:131], v[20:23]\n"
    "ds_read_b128 v[80:83], v202 offset:38912\n"
    "s_waitcnt lgkmcnt(8)\n"
    "v_mfma_f32_16x16x32_bf16 v[24:27], v[84:87], v[124:127], v[24:27]\n"
    "v_mfma_f32_16x16x32_bf16 v[28:31], v[84:87], v[128:131], v[28:31]\n"
    "ds_read_b128 v[84:87], v202 offset:39936\n"
    "s_waitcnt lgkmcnt(8)\n"
    "v_mfma_f32_16x16x32_bf16 v[32:35], v[88:91], v[124:127], v[32:35]\n"
    "v_mfma_f32_16x16x32_bf16 v[36:39], v[88:91], v[128:131], v[36:39]\n"
    "ds_read_b128 v[88:91], v202 offset:40960\n"
    "s_waitcnt lgkmcnt(8)\n"
    "v_mfma_f32_16x16x32_bf16 v[40:43], v[92:95], v[124:127], v[40:43]\n"
    "v_mfma_f32_16x16x32_bf16 v[44:47], v[92:95], v[128:131], v[44:47]\n"
    "ds_read_b128 v[92:95], v202 offset:41984\n"
    "s_waitcnt lgkmcnt(8)\n"
    "v_mfma_f32_16x16x32_bf16 v[48:51], v[96:99], v[124:127], v[48:51]\n"
    "v_mfma_f32_16x16x32_bf16 v[52:55], v[96:99], v[128:131], v[52:55]\n"
    "ds_read_b128 v[96:99], v202 offset:43008\n"
    "s_waitcnt lgkmcnt(8)\n"
    "v_mfma_f32_16x16x32_bf16 v[56:59], v[100:103], v[124:127], v[56:59]\n"
    "v_mfma_f32_16x16x32_bf16 v[60:63], v[100:103], v[128:131], v[60:63]\n"
    "ds_read_b128 v[100:103], v202 offset:44032\n"
    "s_waitcnt lgkmcnt(8)\n"
    "v_mfma_f32_16x16x32_bf16 v[64:67], v[104:107], v[124:127], v[64:67]\n"
    "v_mfma_f32_16x16x32_bf16 v[68:71], v[104:107], v[128:131], v[68:71]\n"
    "ds_read_b128 v[104:107], v202 offset:45056\n"
    "s_waitcnt vmcnt(10)\n"
    "s_waitcnt lgkmcnt(8)\n"
    "v_mfma_f32_16x16x32_bf16 v[0:3], v[72:75], v[132:135], v[0:3]\n"
    "v_mfma_f32_16x16x32_bf16 v[4:7], v[72:75], v[136:139], v[4:7]\n"
    "ds_read_b128 v[72:75], v205 offset:0\n"
    "s_waitcnt lgkmcnt(8)\n"
    "v_mfma_f32_16x16x32_bf16 v[8:11], v[76:79], v[132:135], v[8:11]\n"
    "v_mfma_f32_16x16x32_bf16 v[12:15], v[76:79], v[136:139], v[12:15]\n"
    "ds_read_b128 v[76:79], v205 offset:1024\n"
    "s_waitcnt lgkmcnt(8)\n"
    "v_mfma_f32_16x16x32_bf16 v[16:19], v[80:83], v[132:135], v[16:19]\n"
    "v_mfma_f32_16x16x32_bf16 v[20:23], v[80:83], v[136:139], v[20:23]\n"
    "ds_read_b128 v[80:83], v205 offset:2048\n"
    "s_waitcnt lgkmcnt(8)\n"
    "v_mfma_f32_16x16x32_bf16 v[24:27], v[84:87], v[132:135], v[24:27]\n"
    "v_mfma_f32_16x16x32_bf16 v[28:31], v[84:87], v[136:139], v[28:31]\n"
    "ds_read_b128 v[84:87], v205 offset:3072\n"
    "s_waitcnt lgkmcnt(8)\n"
    "v_mfma_f32_16x16x32_bf16 v[32:35], v[88:91], v[132:135], v[32:35]\n"
    "v_mfma_f32_16x16x32_bf16 v[36:39], v[88:91], v[136:139], v[36:39]\n"
    "ds_read_b128 v[88:91], v205 offset:4096\n"
    "s_waitcnt lgkmcnt(8)\n"
    "v_mfma_f32_16x16x32_bf16 v[40:43], v[92:95], v[132:135], v[40:43]\n"
    "v_mfma_f32_16x16x32_bf16 v[44:47], v[92:95], v[136:139], v[44:47]\n"
    "ds_read_b128 v[92:95], v205 offset:5120\n"
    "s_waitcnt lgkmcnt(8)\n"
    "v_mfma_f32_16x16x32_bf16 v[48:51], v[96:99], v[132:135], v[48:51]\n"
    "v_mfma_f32_16x16x32_bf16 v[52:55], v[96:99], v[136:139], v[52:55]\n"
    "ds_read_b128 v[96:99], v205 offset:6144\n"
    "s_waitcnt lgkmcnt(8)\n"
    "v_mfma_f32_16x16x32_bf16 v[56:59], v[100:103], v[132:135], v[56:59]\n"
    "v_mfma_f32_16x16x32_bf16 v[60:63], v[100:103], v[136:139], v[60:63]\n"
    "ds_read_b128 v[100:103], v205 offset:7168\n"
    "s_waitcnt lgkmcnt(8)\n"
    "v_mfma_f32_16x16x32_bf16 v[64:67], v[104:107], v[132:135], v[64:67]\n"
    "v_mfma_f32_16x16x32_bf16 v[68:71], v[104:107], v[136:139], v[68:71]\n"
    "ds_read_b128 v[104:107], v205 offset:8192\n"
    "s_waitcnt vmcnt(5)\n"
    "s_barrier\n"
    "s_waitcnt lgkmcnt(8)\n"
    "v_mfma_f32_16x16x32_bf16 v[0:3], v[72:75], v[140:143], v[0:3]\n"
    "v_mfma_f32_16x16x32_bf16 v[4:7], v[72:75], v[144:147], v[4:7]\n"
    "ds_read_b128 v[72:75], v205 offset:18432\n"
    "s_waitcnt lgkmcnt(8)\n"
    "v_mfma_f32_16x16x32_bf16 v[8:11], v[76:79], v[140:143], v[8:11]\n"
    "v_mfma_f32_16x16x32_bf16 v[12:15], v[76:79], v[144:147], v[12:15]\n"
    "ds_read_b128 v[76:79], v205 offset:19456\n"
    "s_waitcnt lgkmcnt(8)\n"
    "v_mfma_f32_16x16x32_bf16 v[16:19], v[80:83], v[140:143], v[16:19]\n"
    "v_mfma_f32_16x16x32_bf16 v[20:23], v[80:83], v[144:147], v[20:23]\n"
    "ds_read_b128 v[80:83], v205 offset:20480\n"
    "s_waitcnt lgkmcnt(8)\n"
    "v_mfma_f32_16x16x32_bf16 v[24:27], v[84:87], v[140:143], v[24:27]\n"
    "v_mfma_f32_16x16x32_bf16 v[28:31], v[84:87], v[144:147], v[28:31]\n"
    "ds_read_b128 v[84:87], v205 offset:21504\n"
    "s_waitcnt lgkmcnt(8)\n"
    "v_mfma_f32_16x16x32_bf16 v[32:35], v[88:91], v[140:143], v[32:35]\n"
    "v_mfma_f32_16x16x32_bf16 v[36:39], v[88:91], v[144:147], v[36:39]\n"
    "ds_read_b128 v[88:91], v205 offset:22528\n"
    "s_waitcnt lgkmcnt(8)\n"
    "v_mfma_f32_16x16x32_bf16 v[40:43], v[92:95], v[140:143], v[40:43]\n"
    "v_mfma_f32_16x16x32_bf16 v[44:47], v[92:95], v[144:147], v[44:47]\n"
    "ds_read_b128 v[92:95], v205 offset:23552\n"
    "s_waitcnt lgkmcnt(8)\n"
    "v_mfma_f32_16x16x32_bf16 v[48:51], v[96:99], v[140:143], v[48:51]\n"
    "v_mfma_f32_16x16x32_bf16 v[52:55], v[96:99], v[144:147], v[52:55]\n"
    "ds_read_b128 v[96:99], v205 offset:24576\n"
    "s_waitcnt lgkmcnt(8)\n"
    "v_mfma_f32_16x16x32_bf16 v[56:59], v[100:103], v[140:143], v[56:59]\n"
    "v_mfma_f32_16x16x32_bf16 v[60:63], v[100:103], v[144:147], v[60:63]\n"
    "ds_read_b128 v[100:103], v205 offset:25600\n"
    "s_waitcnt lgkmcnt(8)\n"
    "v_mfma_f32_16x16x32_bf16 v[64:67], v[104:107], v[140:143], v[64:67]\n"
    "v_mfma_f32_16x16x32_bf16 v[68:71], v[104:107], v[144:147], v[68:71]\n"
    "ds_read_b128 v[104:107], v205 offset:26624\n"
    "s_waitcnt vmcnt(0)\n"
    "s_waitcnt lgkmcnt(8)\n"
    "v_mfma_f32_16x16x32_bf16 v[0:3], v[72:75], v[148:151], v[0:3]\n"
    "v_mfma_f32_16x16x32_bf16 v[4:7], v[72:75], v[152:155], v[4:7]\n"
    "ds_read_b128 v[72:75], v201 offset:0\n"
    "s_waitcnt lgkmcnt(8)\n"
    "v_mfma_f32_16x16x32_bf16 v[8:11], v[76:79], v[148:151], v[8:11]\n"
    "v_mfma_f32_16x16x32_bf16 v[12:15], v[76:79], v[152:155], v[12:15]\n"
    "ds_read_b128 v[76:79], v201 offset:1024\n"
    "s_waitcnt lgkmcnt(8)\n"
    "v_mfma_f32_16x16x32_bf16 v[16:19], v[80:83], v[148:151], v[16:19]\n"
    "v_mfma_f32_16x16x32_bf16 v[20:23], v[80:83], v[152:155], v[20:23]\n"
    "ds_read_b128 v[80:83], v201 offset:2048\n"
    "s_waitcnt lgkmcnt(8)\n"
    "v_mfma_f32_16x16x32_bf16 v[24:27], v[84:87], v[148:151], v[24:27]\n"
    "v_mfma_f32_16x16x32_bf16 v[28:31], v[84:87], v[152:155], v[28:31]\n"
    "ds_read_b128 v[84:87], v201 offset:3072\n"
    "s_waitcnt lgkmcnt(8)\n"
    "v_mfma_f32_16x16x32_bf16 v[32:35], v[88:91], v[148:151], v[32:35]\n"
    "v_mfma_f32_16x16x32_bf16 v[36:39], v[88:91], v[152:155], v[36:39]\n"
    "ds_read_b128 v[88:91], v201 offset:4096\n"
    "s_waitcnt lgkmcnt(8)\n"
    "v_mfma_f32_16x16x32_bf16 v[40:43], v[92:95], v[148:151], v[40:43]\n"
    "s_lshl_b32 s59, s62, 2\n"
    "s_lshl_b32 s60, s58, 1\n"
    "s_add_u32 s59, s59, s60\n"
    "s_add_u32 s59, s59, 151\n"
    "s_lshl_b32 s59, s59, 13\n"
    "s_lshl_b32 s60, s57, 11\n"
    "s_add_u32 s74, s59, s60\n"
    "s_add_u32 s59, s74, 0\n"
    "buffer_load_dwordx4 v[108:111], v200, s[36:39], s59 offen offset:0 sc1\n"
    "buffer_load_dwordx4 v[112:115], v200, s[36:39], s59 offen offset:1024 sc1\n"
    "s_add_u32 s59, s74, 8192\n"
    "buffer_load_dwordx4 v[116:119], v200, s[36:39], s59 offen offset:0 sc1\n"
    "buffer_load_dwordx4 v[120:123], v200, s[36:39], s59 offen offset:1024 sc1\n"
    "v_mfma_f32_16x16x32_bf16 v[44:47], v[92:95], v[152:155], v[44:47]\n"
    "ds_read_b128 v[92:95], v201 offset:5120\n"
    "s_waitcnt lgkmcnt(8)\n"
    "v_mfma_f32_16x16x32_bf16 v[48:51], v[96:99], v[148:151], v[48:51]\n"
    "v_mfma_f32_16x16x32_bf16 v[52:55], v[96:99], v[152:155], v[52:55]\n"
    "ds_read_b128 v[96:99], v201 offset:6144\n"
    "s_waitcnt lgkmcnt(8)\n"
    "v_mfma_f32_16x16x32_bf16 v[56:59], v[100:103], v[148:151], v[56:59]\n"
    "v_mfma_f32_16x16x32_bf16 v[60:63], v[100:103], v[152:155], v[60:63]\n"
    "ds_read_b128 v[100:103], v201 offset:7168\n"
    "s_waitcnt lgkmcnt(8)\n"
    "v_mfma_f32_16x16x32_bf16 v[64:67], v[104:107], v[148:151], v[64:67]\n"
    "v_mfma_f32_16x16x32_bf16 v[68:71], v[104:107], v[152:155], v[68:71]\n"
    "ds_read_b128 v[104:107], v201 offset:8192\n"
    "s_barrier\n"
    "v_sub_f32 v203, 1.0, v203\n"
    "v_sub_f32 v204, 1.0, v204\n"
    "v_mul_f32 v0, v0, v203\n"
    "v_mul_f32 v1, v1, v203\n"
    "v_mul_f32 v2, v2, v203\n"
    "v_mul_f32 v3, v3, v203\n"
    "v_mul_f32 v4, v4, v204\n"
    "v_mul_f32 v5, v5, v204\n"
    "v_mul_f32 v6, v6, v204\n"
    "v_mul_f32 v7, v7, v204\n"
    "v_mul_f32 v8, v8, v203\n"
    "v_mul_f32 v9, v9, v203\n"
    "v_mul_f32 v10, v10, v203\n"
    "v_mul_f32 v11, v11, v203\n"
    "v_mul_f32 v12, v12, v204\n"
    "v_mul_f32 v13, v13, v204\n"
    "v_mul_f32 v14, v14, v204\n"
    "v_mul_f32 v15, v15, v204\n"
    "v_mul_f32 v16, v16, v203\n"
    "v_mul_f32 v17, v17, v203\n"
    "v_mul_f32 v18, v18, v203\n"
    "v_mul_f32 v19, v19, v203\n"
    "v_mul_f32 v20, v20, v204\n"
    "v_mul_f32 v21, v21, v204\n"
    "v_mul_f32 v22, v22, v204\n"
    "v_mul_f32 v23, v23, v204\n"
    "v_mul_f32 v24, v24, v203\n"
    "v_mul_f32 v25, v25, v203\n"
    "v_mul_f32 v26, v26, v203\n"
    "v_mul_f32 v27, v27, v203\n"
    "v_mul_f32 v28, v28, v204\n"
    "v_mul_f32 v29, v29, v204\n"
    "v_mul_f32 v30, v30, v204\n"
    "v_mul_f32 v31, v31, v204\n"
    "v_mul_f32 v32, v32, v203\n"
    "v_mul_f32 v33, v33, v203\n"
    "v_mul_f32 v34, v34, v203\n"
    "v_mul_f32 v35, v35, v203\n"
    "v_mul_f32 v36, v36, v204\n"
    "v_mul_f32 v37, v37, v204\n"
    "v_mul_f32 v38, v38, v204\n"
    "v_mul_f32 v39, v39, v204\n"
    "v_mul_f32 v40, v40, v203\n"
    "v_mul_f32 v41, v41, v203\n"
    "v_mul_f32 v42, v42, v203\n"
    "v_mul_f32 v43, v43, v203\n"
    "v_mul_f32 v44, v44, v204\n"
    "v_mul_f32 v45, v45, v204\n"
    "v_mul_f32 v46, v46, v204\n"
    "v_mul_f32 v47, v47, v204\n"
    "v_mul_f32 v48, v48, v203\n"
    "v_mul_f32 v49, v49, v203\n"
    "v_mul_f32 v50, v50, v203\n"
    "v_mul_f32 v51, v51, v203\n"
    "v_mul_f32 v52, v52, v204\n"
    "v_mul_f32 v53, v53, v204\n"
    "v_mul_f32 v54, v54, v204\n"
    "v_mul_f32 v55, v55, v204\n"
    "v_mul_f32 v56, v56, v203\n"
    "v_mul_f32 v57, v57, v203\n"
    "v_mul_f32 v58, v58, v203\n"
    "v_mul_f32 v59, v59, v203\n"
    "v_mul_f32 v60, v60, v204\n"
    "v_mul_f32 v61, v61, v204\n"
    "v_mul_f32 v62, v62, v204\n"
    "v_mul_f32 v63, v63, v204\n"
    "v_mul_f32 v64, v64, v203\n"
    "v_mul_f32 v65, v65, v203\n"
    "v_mul_f32 v66, v66, v203\n"
    "v_mul_f32 v67, v67, v203\n"
    "v_mul_f32 v68, v68, v204\n"
    "v_mul_f32 v69, v69, v204\n"
    "v_mul_f32 v70, v70, v204\n"
    "v_mul_f32 v71, v71, v204\n"
    "s_waitcnt vmcnt(2)\n"
    "s_mov_b32 s71, 0\n"
    "L2CKB24%=:\n"
    "v_max3_u32 v208, v108, v109, v110\n"
    "v_max3_u32 v208, v208, v111, v112\n"
    "v_max3_u32 v208, v208, v113, v114\n"
    "v_max_u32 v208, v208, v115\n"
    "v_cmp_eq_u32 vcc, -1, v208\n"
    "s_cbranch_vccz L2OKB24%=\n"
    "s_add_u32 s71, s71, 1\n"
    "s_cmp_lt_u32 s71, 0x40000\n"
    "s_cbranch_scc0 L2OKB24%=\n"
    "s_sleep 4\n"
    "s_add_u32 s59, s74, 0\n"
    "buffer_load_dwordx4 v[108:111], v200, s[36:39], s59 offen offset:0 sc1\n"
    "buffer_load_dwordx4 v[112:115], v200, s[36:39], s59 offen offset:1024 sc1\n"
    "s_waitcnt vmcnt(0)\n"
    "s_branch L2CKB24%=\n"
    "L2OKB24%=:\n"
    "s_waitcnt lgkmcnt(8)\n"
    "v_mfma_f32_16x16x32_bf16 v[0:3], v[72:75], v[108:111], v[0:3]\n"
    "v_mfma_f32_16x16x32_bf16 v[4:7], v[72:75], v[112:115], v[4:7]\n"
    "ds_read_b128 v[72:75], v201 offset:18432\n"
    "s_waitcnt lgkmcnt(8)\n"
    "v_mfma_f32_16x16x32_bf16 v[8:11], v[76:79], v[108:111], v[8:11]\n"
    "v_mfma_f32_16x16x32_bf16 v[12:15], v[76:79], v[112:115], v[12:15]\n"
    "ds_read_b128 v[76:79], v201 offset:19456\n"
    "s_waitcnt lgkmcnt(8)\n"
    "v_mfma_f32_16x16x32_bf16 v[16:19], v[80:83], v[108:111], v[16:19]\n"
    "v_mfma_f32_16x16x32_bf16 v[20:23], v[80:83], v[112:115], v[20:23]\n"
    "ds_read_b128 v[80:83], v201 offset:20480\n"
    "s_waitcnt lgkmcnt(8)\n"
    "v_mfma_f32_16x16x32_bf16 v[24:27], v[84:87], v[108:111], v[24:27]\n"
    "v_mfma_f32_16x16x32_bf16 v[28:31], v[84:87], v[112:115], v[28:31]\n"
    "ds_read_b128 v[84:87], v201 offset:21504\n"
    "s_waitcnt lgkmcnt(8)\n"
    "v_mfma_f32_16x16x32_bf16 v[32:35], v[88:91], v[108:111], v[32:35]\n"
    "v_mfma_f32_16x16x32_bf16 v[36:39], v[88:91], v[112:115], v[36:39]\n"
    "ds_read_b128 v[88:91], v201 offset:22528\n"
    "s_waitcnt lgkmcnt(8)\n"
    "v_mfma_f32_16x16x32_bf16 v[40:43], v[92:95], v[108:111], v[40:43]\n"
    "v_mfma_f32_16x16x32_bf16 v[44:47], v[92:95], v[112:115], v[44:47]\n"
    "ds_read_b128 v[92:95], v201 offset:23552\n"
    "s_waitcnt lgkmcnt(8)\n"
    "v_mfma_f32_16x16x32_bf16 v[48:51], v[96:99], v[108:111], v[48:51]\n"
    "v_mfma_f32_16x16x32_bf16 v[52:55], v[96:99], v[112:115], v[52:55]\n"
    "ds_read_b128 v[96:99], v201 offset:24576\n"
    "s_waitcnt lgkmcnt(8)\n"
    "v_mfma_f32_16x16x32_bf16 v[56:59], v[100:103], v[108:111], v[56:59]\n"
    "v_mfma_f32_16x16x32_bf16 v[60:63], v[100:103], v[112:115], v[60:63]\n"
    "ds_read_b128 v[100:103], v201 offset:25600\n"
    "s_waitcnt lgkmcnt(8)\n"
    "v_mfma_f32_16x16x32_bf16 v[64:67], v[104:107], v[108:111], v[64:67]\n"
    "v_mfma_f32_16x16x32_bf16 v[68:71], v[104:107], v[112:115], v[68:71]\n"
    "ds_read_b128 v[104:107], v201 offset:26624\n"
    "s_waitcnt vmcnt(0)\n"
    "s_mov_b32 s71, 0\n"
    "L2CKB25%=:\n"
    "v_max3_u32 v208, v116, v117, v118\n"
    "v_max3_u32 v208, v208, v119, v120\n"
    "v_max3_u32 v208, v208, v121, v122\n"
    "v_max_u32 v208, v208, v123\n"
    "v_cmp_eq_u32 vcc, -1, v208\n"
    "s_cbranch_vccz L2OKB25%=\n"
    "s_add_u32 s71, s71, 1\n"
    "s_cmp_lt_u32 s71, 0x40000\n"
    "s_cbranch_scc0 L2OKB25%=\n"
    "s_sleep 4\n"
    "s_add_u32 s59, s74, 8192\n"
    "buffer_load_dwordx4 v[116:119], v200, s[36:39], s59 offen offset:0 sc1\n"
    "buffer_load_dwordx4 v[120:123], v200, s[36:39], s59 offen offset:1024 sc1\n"
    "s_waitcnt vmcnt(0)\n"
    "s_branch L2CKB25%=\n"
    "L2OKB25%=:\n"
    "s_waitcnt lgkmcnt(8)\n"
    "v_mfma_f32_16x16x32_bf16 v[0:3], v[72:75], v[116:119], v[0:3]\n"
    "v_mfma_f32_16x16x32_bf16 v[4:7], v[72:75], v[120:123], v[4:7]\n"
    "s_waitcnt lgkmcnt(7)\n"
    "v_mfma_f32_16x16x32_bf16 v[8:11], v[76:79], v[116:119], v[8:11]\n"
    "v_mfma_f32_16x16x32_bf16 v[12:15], v[76:79], v[120:123], v[12:15]\n"
    "s_waitcnt lgkmcnt(6)\n"
    "v_mfma_f32_16x16x32_bf16 v[16:19], v[80:83], v[116:119], v[16:19]\n"
    "v_mfma_f32_16x16x32_bf16 v[20:23], v[80:83], v[120:123], v[20:23]\n"
    "s_waitcnt lgkmcnt(5)\n"
    "v_mfma_f32_16x16x32_bf16 v[24:27], v[84:87], v[116:119], v[24:27]\n"
    "v_mfma_f32_16x16x32_bf16 v[28:31], v[84:87], v[120:123], v[28:31]\n"
    "s_waitcnt lgkmcnt(4)\n"
    "v_mfma_f32_16x16x32_bf16 v[32:35], v[88:91], v[116:119], v[32:35]\n"
    "v_mfma_f32_16x16x32_bf16 v[36:39], v[88:91], v[120:123], v[36:39]\n"
    "s_waitcnt lgkmcnt(3)\n"
    "v_mfma_f32_16x16x32_bf16 v[40:43], v[92:95], v[116:119], v[40:43]\n"
    "v_mfma_f32_16x16x32_bf16 v[44:47], v[92:95], v[120:123], v[44:47]\n"
    "s_waitcnt lgkmcnt(2)\n"
    "v_mfma_f32_16x16x32_bf16 v[48:51], v[96:99], v[116:119], v[48:51]\n"
    "v_mfma_f32_16x16x32_bf16 v[52:55], v[96:99], v[120:123], v[52:55]\n"
    "s_waitcnt lgkmcnt(1)\n"
    "v_mfma_f32_16x16x32_bf16 v[56:59], v[100:103], v[116:119], v[56:59]\n"
    "v_mfma_f32_16x16x32_bf16 v[60:63], v[100:103], v[120:123], v[60:63]\n"
    "s_waitcnt lgkmcnt(0)\n"
    "v_mfma_f32_16x16x32_bf16 v[64:67], v[104:107], v[116:119], v[64:67]\n"
    "v_mfma_f32_16x16x32_bf16 v[68:71], v[104:107], v[120:123], v[68:71]\n"
    "s_branch L2DONE%=\n"
    "L2ATYPE%=:\n"
    "s_cmp_lt_u32 %6, 38\n"
    "s_cbranch_scc1 L2F2%=\n"
    "s_mov_b64 s[40:41], %1\n"
    "s_and_b32 s41, s41, 0xffff\n"
    "s_mov_b32 s42, %14\n"
    "s_mov_b32 s43, 0x20000\n"
    "v_mbcnt_lo_u32_b32 v211, -1, 0\n"
    "v_mbcnt_hi_u32_b32 v211, -1, v211\n"
    "v_lshlrev_b32 v210, 4, v211\n"
    "v_lshlrev_b32 v211, 2, v211\n"
    "s_sub_u32 s64, s64, 9216\n"
    "s_sub_u32 s65, s65, 9216\n"
    "s_sub_u32 s66, s66, 9216\n"
    "s_add_u32 m0, s67, 0\n"
    "s_add_u32 s64, s64, 9216\n"
    "buffer_load_dwordx4 v210, s[40:43], s64 offen lds\n"
    "s_add_u32 m0, s68, 0\n"
    "s_add_u32 s65, s65, 9216\n"
    "buffer_load_dwordx4 v210, s[40:43], s65 offen lds\n"
    "s_add_u32 m0, s69, 0\n"
    "s_add_u32 s66, s66, 9216\n"
    "buffer_load_dword v211, s[40:43], s66 offen lds\n"
    "s_branch L2NOF2%=\n"
    "L2F2%=:\n"
    "s_mov_b64 s[44:45], %0\n"
    "s_add_u32 s44, s44, 39845888\n"
    "s_addc_u32 s45, s45, 0\n"
    "s_and_b32 s45, s45, 0xffff\n"
    "s_mov_b32 s46, 2621440\n"
    "s_mov_b32 s47, 0x20000\n"
    "s_mov_b64 s[48:49], %0\n"
    "s_add_u32 s48, s48, 42991616\n"
    "s_addc_u32 s49, s49, 0\n"
    "s_and_b32 s49, s49, 0xffff\n"
    "s_mov_b32 s50, 2490368\n"
    "s_mov_b32 s51, 0x20000\n"
    "s_mov_b64 s[52:53], %3\n"
    "s_and_b32 s53, s53, 0xffff\n"
    "s_mov_b32 s54, 36864\n"
    "s_mov_b32 s55, 0x20000\n"
    "s_mov_b64 s[96:97], %0\n"
    "s_and_b32 s97, s97, 0xffff\n"
    "s_mov_b32 s98, 21364736\n"
    "s_mov_b32 s99, 0x20000\n"
    "s_mov_b64 s[36:37], %4\n"
    "s_and_b32 s37, s37, 0xffff\n"
    "s_mov_b32 s38, 24576\n"
    "s_mov_b32 s39, 0x20000\n"
    "s_mov_b64 s[40:41], %0\n"
    "s_add_u32 s40, s40, 46145536\n"
    "s_addc_u32 s41, s41, 0\n"
    "s_and_b32 s41, s41, 0xffff\n"
    "s_mov_b32 s42, 8192\n"
    "s_mov_b32 s43, 0x20000\n"
    "s_lshl_b32 s72, %6, 3\n"
    "s_add_u32 s72, s72, s56\n"
    "s_lshr_b32 s59, %6, 1\n"
    "s_add_u32 s59, s59, 144\n"
    "s_lshl_b32 s73, s59, 13\n"
    "s_and_b32 s59, %6, 1\n"
    "s_lshl_b32 s59, s59, 9\n"
    "s_add_u32 s73, s73, s59\n"
    "v_mbcnt_lo_u32_b32 v223, -1, 0\n"
    "v_mbcnt_hi_u32_b32 v223, -1, v223\n"
    "v_lshlrev_b32 v226, 4, v223\n"
    "s_lshl_b32 s59, s72, 13\n"
    "s_lshl_b32 s60, s56, 13\n"
    "s_add_u32 m0, s60, 18432\n"
    "s_nop 0\n"
    "buffer_load_dwordx4 v226, s[44:47], s59 offen lds\n"
    "s_add_u32 s59, s59, 1024\n"
    "s_add_u32 m0, s60, 19456\n"
    "s_nop 0\n"
    "buffer_load_dwordx4 v226, s[44:47], s59 offen lds\n"
    "s_add_u32 s59, s59, 1024\n"
    "s_add_u32 m0, s60, 20480\n"
    "s_nop 0\n"
    "buffer_load_dwordx4 v226, s[44:47], s59 offen lds\n"
    "s_add_u32 s59, s59, 1024\n"
    "s_add_u32 m0, s60, 21504\n"
    "s_nop 0\n"
    "buffer_load_dwordx4 v226, s[44:47], s59 offen lds\n"
    "s_add_u32 s59, s59, 1024\n"
    "s_add_u32 m0, s60, 22528\n"
    "s_nop 0\n"
    "buffer_load_dwordx4 v226, s[44:47], s59 offen lds\n"
    "s_add_u32 s59, s59, 1024\n"
    "s_add_u32 m0, s60, 23552\n"
    "s_nop 0\n"
    "buffer_load_dwordx4 v226, s[44:47], s59 offen lds\n"
    "s_add_u32 s59, s59, 1024\n"
    "s_add_u32 m0, s60, 24576\n"
    "s_nop 0\n"
    "buffer_load_dwordx4 v226, s[44:47], s59 offen lds\n"
    "s_add_u32 s59, s59, 1024\n"
    "s_add_u32 m0, s60, 25600\n"
    "s_nop 0\n"
    "buffer_load_dwordx4 v226, s[44:47], s59 offen lds\n"
    "s_lshl_b32 s59, s56, 10\n"
    "s_add_u32 m0, s59, 84480\n"
    "s_add_u32 s60, s59, 0\n"
    "buffer_load_dwordx4 v226, s[52:55], s60 offen lds\n"
    "s_add_u32 m0, s59, 92672\n"
    "s_add_u32 s60, s59, 8192\n"
    "buffer_load_dwordx4 v226, s[52:55], s60 offen lds\n"
    "s_add_u32 m0, s59, 100864\n"
    "s_add_u32 s60, s59, 16384\n"
    "buffer_load_dwordx4 v226, s[52:55], s60 offen lds\n"
    "s_add_u32 m0, s59, 109056\n"
    "s_add_u32 s60, s59, 24576\n"
    "buffer_load_dwordx4 v226, s[52:55], s60 offen lds\n"
    "v_lshlrev_b32 v227, 2, v223\n"
    "s_lshl_b32 s60, s56, 9\n"
    "s_add_u32 m0, s60, 117248\n"
    "s_add_u32 s59, s60, 32768\n"
    "buffer_load_dword v227, s[52:55], s59 offen lds\n"
    "s_add_u32 m0, s60, 117504\n"
    "s_add_u32 s59, s60, 33024\n"
    "buffer_load_dword v227, s[52:55], s59 offen lds\n"
    "s_lshl_b32 s59, s56, 10\n"
    "s_add_u32 m0, s59, 121344\n"
    "s_add_u32 s60, s59, 0\n"
    "buffer_load_dwordx4 v226, s[36:39], s60 offen lds\n"
    "s_add_u32 m0, s59, 129536\n"
    "s_add_u32 s60, s59, 8192\n"
    "buffer_load_dwordx4 v226, s[36:39], s60 offen lds\n"
    "s_add_u32 m0, s59, 137728\n"
    "s_add_u32 s60, s59, 16384\n"
    "buffer_load_dwordx4 v226, s[36:39], s60 offen lds\n"
    "v_lshrrev_b32 v227, 4, v223\n"
    "s_lshl_b32 s59, s56, 2\n"
    "v_add_u32 v227, s59, v227\n"
    "v_xor_b32 v228, v227, v223\n"
    "v_and_b32 v228, 15, v228\n"
    "v_lshlrev_b32 v228, 4, v228\n"
    "v_lshlrev_b32 v227, 8, v227\n"
    "v_add_u32 v227, v227, v228\n"
    "s_lshl_b32 s59, s56, 10\n"
    "s_add_u32 m0, s59, 145920\n"
    "s_nop 0\n"
    "buffer_load_dwordx4 v227, s[40:43], 0 offen lds\n"
    "v_and_b32 v227, 31, v223\n"
    "v_lshrrev_b32 v228, 5, v223\n"
    "v_lshlrev_b32 v222, 2, v227\n"
    "v_lshlrev_b32 v229, 9, v228\n"
    "v_add_u32 v222, v222, v229\n"
    "s_lshl_b32 s59, s72, 13\n"
    "s_add_u32 s60, s59, 4096\n"
    "buffer_load_dword v172, v222, s[48:51], s59 offen offset:0\n"
    "buffer_load_dword v173, v222, s[48:51], s59 offen offset:128\n"
    "buffer_load_dword v174, v222, s[48:51], s59 offen offset:256\n"
    "buffer_load_dword v175, v222, s[48:51], s59 offen offset:384\n"
    "buffer_load_dword v176, v222, s[48:51], s59 offen offset:1024\n"
    "buffer_load_dword v177, v222, s[48:51], s59 offen offset:1152\n"
    "buffer_load_dword v178, v222, s[48:51], s59 offen offset:1280\n"
    "buffer_load_dword v179, v222, s[48:51], s59 offen offset:1408\n"
    "buffer_load_dword v180, v222, s[48:51], s59 offen offset:2048\n"
    "buffer_load_dword v181, v222, s[48:51], s59 offen offset:2176\n"
    "buffer_load_dword v182, v222, s[48:51], s59 offen offset:2304\n"
    "buffer_load_dword v183, v222, s[48:51], s59 offen offset:2432\n"
    "buffer_load_dword v184, v222, s[48:51], s59 offen offset:3072\n"
    "buffer_load_dword v185, v222, s[48:51], s59 offen offset:3200\n"
    "buffer_load_dword v186, v222, s[48:51], s59 offen offset:3328\n"
    "buffer_load_dword v187, v222, s[48:51], s59 offen offset:3456\n"
    "buffer_load_dword v188, v222, s[48:51], s60 offen offset:0\n"
    "buffer_load_dword v189, v222, s[48:51], s60 offen offset:128\n"
    "buffer_load_dword v190, v222, s[48:51], s60 offen offset:256\n"
    "buffer_load_dword v191, v222, s[48:51], s60 offen offset:384\n"
    "buffer_load_dword v192, v222, s[48:51], s60 offen offset:1024\n"
    "buffer_load_dword v193, v222, s[48:51], s60 offen offset:1152\n"
    "buffer_load_dword v194, v222, s[48:51], s60 offen offset:1280\n"
    "buffer_load_dword v195, v222, s[48:51], s60 offen offset:1408\n"
    "buffer_load_dword v196, v222, s[48:51], s60 offen offset:2048\n"
    "buffer_load_dword v197, v222, s[48:51], s60 offen offset:2176\n"
    "buffer_load_dword v198, v222, s[48:51], s60 offen offset:2304\n"
    "buffer_load_dword v199, v222, s[48:51], s60 offen offset:2432\n"
    "buffer_load_dword v200, v222, s[48:51], s60 offen offset:3072\n"
    "buffer_load_dword v201, v222, s[48:51], s60 offen offset:3200\n"
    "buffer_load_dword v202, v222, s[48:51], s60 offen offset:3328\n"
    "buffer_load_dword v203, v222, s[48:51], s60 offen offset:3456\n"
    "s_mov_b64 s[40:41], %1\n"
    "s_and_b32 s41, s41, 0xffff\n"
    "s_mov_b32 s42, %14\n"
    "s_mov_b32 s43, 0x20000\n"
    "v_mbcnt_lo_u32_b32 v241, -1, 0\n"
    "v_mbcnt_hi_u32_b32 v241, -1, v241\n"
    "v_lshlrev_b32 v240, 4, v241\n"
    "v_lshlrev_b32 v241, 2, v241\n"
    "s_sub_u32 s64, s64, 9216\n"
    "s_sub_u32 s65, s65, 9216\n"
    "s_sub_u32 s66, s66, 9216\n"
    "s_add_u32 m0, s67, 0\n"
    "s_add_u32 s64, s64, 9216\n"
    "buffer_load_dwordx4 v240, s[40:43], s64 offen lds\n"
    "s_add_u32 m0, s68, 0\n"
    "s_add_u32 s65, s65, 9216\n"
    "buffer_load_dwordx4 v240, s[40:43], s65 offen lds\n"
    "s_add_u32 m0, s69, 0\n"
    "s_add_u32 s66, s66, 9216\n"
    "buffer_load_dword v241, s[40:43], s66 offen lds\n"
    "v_bfe_u32 v230, v223, 2, 2\n"
    "v_lshlrev_b32 v231, 3, v228\n"
    "v_add_u32 v230, v230, v231\n"
    "v_lshlrev_b32 v230, 7, v230\n"
    "v_and_b32 v231, 3, v223\n"
    "v_lshlrev_b32 v231, 4, v231\n"
    "v_add_u32 v230, v230, v231\n"
    "v_bfe_u32 v231, v223, 4, 1\n"
    "v_lshlrev_b32 v231, 3, v231\n"
    "v_add_u32 v230, v230, v231\n"
    "s_lshl_b32 s59, s56, 13\n"
    "s_add_u32 s59, s59, 18432\n"
    "v_add_u32 v220, s59, v230\n"
    "v_lshlrev_b32 v221, 4, v223\n"
    "v_add_u32 v224, 121344, v221\n"
    "v_add_u32 v221, 84480, v221\n"
    "v_and_b32 v230, 15, v227\n"
    "v_xor_b32 v230, v230, v228\n"
    "v_lshlrev_b32 v230, 3, v230\n"
    "v_lshlrev_b32 v231, 7, v227\n"
    "v_add_u32 v230, v230, v231\n"
    "s_mul_i32 s59, s56, 8256\n"
    "s_add_u32 s59, s59, 18432\n"
    "v_xor_b32 v242, 0, v230\n"
    "v_add_u32 v242, s59, v242\n"
    "v_xor_b32 v243, 16, v230\n"
    "v_add_u32 v243, s59, v243\n"
    "v_xor_b32 v244, 32, v230\n"
    "v_add_u32 v244, s59, v244\n"
    "v_xor_b32 v245, 48, v230\n"
    "v_add_u32 v245, s59, v245\n"
    "v_xor_b32 v246, 64, v230\n"
    "v_add_u32 v246, s59, v246\n"
    "v_xor_b32 v247, 80, v230\n"
    "v_add_u32 v247, s59, v247\n"
    "v_xor_b32 v248, 96, v230\n"
    "v_add_u32 v248, s59, v248\n"
    "v_xor_b32 v249, 112, v230\n"
    "v_add_u32 v249, s59, v249\n"
    "v_and_b32 v230, 15, v227\n"
    "v_xor_b32 v230, v230, v228\n"
    "v_lshlrev_b32 v230, 4, v230\n"
    "v_lshlrev_b32 v231, 8, v227\n"
    "v_add_u32 v230, v230, v231\n"
    "v_add_u32 v225, 145920, v230\n"
    "s_waitcnt vmcnt(44)\n"
    "s_barrier\n"
    "ds_read_b128 v[148:151], v221 offset:0\n"
    "ds_read_b128 v[152:155], v221 offset:1024\n"
    "ds_read_b128 v[156:159], v221 offset:2048\n"
    "ds_read_b128 v[160:163], v221 offset:3072\n"
    "ds_read_b128 v[164:167], v221 offset:4096\n"
    "ds_read_b128 v[168:171], v221 offset:5120\n"
    "ds_read_b64_tr_b16 v[112:113], v220 offset:0\n"
    "ds_read_b64_tr_b16 v[114:115], v220 offset:512\n"
    "ds_read_b64_tr_b16 v[116:117], v220 offset:2048\n"
    "ds_read_b64_tr_b16 v[118:119], v220 offset:2560\n"
    "ds_read_b64_tr_b16 v[120:121], v220 offset:64\n"
    "ds_read_b64_tr_b16 v[122:123], v220 offset:576\n"
    "ds_read_b64_tr_b16 v[124:125], v220 offset:2112\n"
    "ds_read_b64_tr_b16 v[126:127], v220 offset:2624\n"
    "ds_read_b64_tr_b16 v[128:129], v220 offset:4096\n"
    "ds_read_b64_tr_b16 v[130:131], v220 offset:4608\n"
    "ds_read_b64_tr_b16 v[132:133], v220 offset:6144\n"
    "ds_read_b64_tr_b16 v[134:135], v220 offset:6656\n"
    "ds_read_b64_tr_b16 v[136:137], v220 offset:4160\n"
    "ds_read_b64_tr_b16 v[138:139], v220 offset:4672\n"
    "ds_read_b64_tr_b16 v[140:141], v220 offset:6208\n"
    "ds_read_b64_tr_b16 v[142:143], v220 offset:6720\n"
    "s_waitcnt lgkmcnt(0)\n"
    "v_mfma_f32_32x32x16_bf16 v[0:15], v[112:115], v[148:151], 0\n"
    "v_mfma_f32_32x32x16_bf16 v[32:47], v[128:131], v[148:151], 0\n"
    "v_mfma_f32_32x32x16_bf16 v[0:15], v[128:131], v[156:159], v[0:15]\n"
    "v_mfma_f32_32x32x16_bf16 v[32:47], v[112:115], v[164:167], v[32:47]\n"
    "v_mfma_f32_32x32x16_bf16 v[0:15], v[116:119], v[152:155], v[0:15]\n"
    "v_mfma_f32_32x32x16_bf16 v[32:47], v[132:135], v[152:155], v[32:47]\n"
    "v_mfma_f32_32x32x16_bf16 v[0:15], v[132:135], v[160:163], v[0:15]\n"
    "v_mfma_f32_32x32x16_bf16 v[32:47], v[116:119], v[168:171], v[32:47]\n"
    "s_waitcnt vmcnt(35)\n"
    "s_barrier\n"
    "ds_read_b128 v[204:207], v224 offset:0\n"
    "ds_read_b128 v[208:211], v224 offset:1024\n"
    "ds_read_b128 v[212:215], v224 offset:2048\n"
    "ds_read_b128 v[216:219], v224 offset:3072\n"
    "v_mfma_f32_32x32x16_bf16 v[16:31], v[120:123], v[148:151], 0\n"
    "v_mfma_f32_32x32x16_bf16 v[48:63], v[136:139], v[148:151], 0\n"
    "v_mfma_f32_32x32x16_bf16 v[16:31], v[136:139], v[156:159], v[16:31]\n"
    "s_waitcnt lgkmcnt(3)\n"
    "s_nop 1\n"
    "v_mul_f32 v226, v32, v205\n"
    "v_mul_f32 v227, v0, v205\n"
    "v_mul_f32 v228, v33, v207\n"
    "v_mul_f32 v229, v1, v207\n"
    "v_fma_f32 v0, v0, v204, v226\n"
    "v_fma_f32 v32, v32, v204, -v227\n"
    "v_fma_f32 v1, v1, v206, v228\n"
    "v_fma_f32 v33, v33, v206, -v229\n"
    "ds_read_b128 v[204:207], v224 offset:4096\n"
    "s_waitcnt lgkmcnt(3)\n"
    "v_mul_f32 v230, v34, v209\n"
    "v_mul_f32 v231, v2, v209\n"
    "v_mul_f32 v232, v35, v211\n"
    "v_mul_f32 v233, v3, v211\n"
    "v_fma_f32 v2, v2, v208, v230\n"
    "v_fma_f32 v34, v34, v208, -v231\n"
    "v_mfma_f32_32x32x16_bf16 v[48:63], v[120:123], v[164:167], v[48:63]\n"
    "v_fma_f32 v3, v3, v210, v232\n"
    "v_fma_f32 v35, v35, v210, -v233\n"
    "ds_read_b128 v[208:211], v224 offset:5120\n"
    "s_waitcnt lgkmcnt(3)\n"
    "v_mul_f32 v226, v36, v213\n"
    "v_mul_f32 v227, v4, v213\n"
    "v_mul_f32 v228, v37, v215\n"
    "v_mul_f32 v229, v5, v215\n"
    "v_fma_f32 v4, v4, v212, v226\n"
    "v_fma_f32 v36, v36, v212, -v227\n"
    "v_fma_f32 v5, v5, v214, v228\n"
    "v_fma_f32 v37, v37, v214, -v229\n"
    "ds_read_b128 v[212:215], v224 offset:6144\n"
    "s_waitcnt lgkmcnt(3)\n"
    "v_mul_f32 v230, v38, v217\n"
    "v_mul_f32 v231, v6, v217\n"
    "v_mul_f32 v232, v39, v219\n"
    "v_mul_f32 v233, v7, v219\n"
    "v_mfma_f32_32x32x16_bf16 v[16:31], v[124:127], v[152:155], v[16:31]\n"
    "v_fma_f32 v6, v6, v216, v230\n"
    "v_fma_f32 v38, v38, v216, -v231\n"
    "v_fma_f32 v7, v7, v218, v232\n"
    "v_fma_f32 v39, v39, v218, -v233\n"
    "ds_read_b128 v[216:219], v224 offset:7168\n"
    "s_waitcnt lgkmcnt(3)\n"
    "v_mul_f32 v226, v40, v205\n"
    "v_mul_f32 v227, v8, v205\n"
    "v_mul_f32 v228, v41, v207\n"
    "v_mul_f32 v229, v9, v207\n"
    "v_fma_f32 v8, v8, v204, v226\n"
    "v_fma_f32 v40, v40, v204, -v227\n"
    "v_fma_f32 v9, v9, v206, v228\n"
    "v_fma_f32 v41, v41, v206, -v229\n"
    "ds_read_b128 v[204:207], v224 offset:8192\n"
    "s_waitcnt lgkmcnt(3)\n"
    "v_mul_f32 v230, v42, v209\n"
    "v_mfma_f32_32x32x16_bf16 v[48:63], v[140:143], v[152:155], v[48:63]\n"
    "v_mul_f32 v231, v10, v209\n"
    "v_mul_f32 v232, v43, v211\n"
    "v_mul_f32 v233, v11, v211\n"
    "v_fma_f32 v10, v10, v208, v230\n"
    "v_fma_f32 v42, v42, v208, -v231\n"
    "v_fma_f32 v11, v11, v210, v232\n"
    "v_fma_f32 v43, v43, v210, -v233\n"
    "ds_read_b128 v[208:211], v224 offset:9216\n"
    "s_waitcnt lgkmcnt(3)\n"
    "v_mul_f32 v226, v44, v213\n"
    "v_mul_f32 v227, v12, v213\n"
    "v_mul_f32 v228, v45, v215\n"
    "v_mul_f32 v229, v13, v215\n"
    "v_fma_f32 v12, v12, v212, v226\n"
    "v_fma_f32 v44, v44, v212, -v227\n"
    "v_mfma_f32_32x32x16_bf16 v[16:31], v[140:143], v[160:163], v[16:31]\n"
    "v_fma_f32 v13, v13, v214, v228\n"
    "v_fma_f32 v45, v45, v214, -v229\n"
    "ds_read_b128 v[212:215], v224 offset:10240\n"
    "s_waitcnt lgkmcnt(3)\n"
    "v_mul_f32 v230, v46, v217\n"
    "v_mul_f32 v231, v14, v217\n"
    "v_mul_f32 v232, v47, v219\n"
    "v_mul_f32 v233, v15, v219\n"
    "v_fma_f32 v14, v14, v216, v230\n"
    "v_fma_f32 v46, v46, v216, -v231\n"
    "v_fma_f32 v15, v15, v218, v232\n"
    "v_fma_f32 v47, v47, v218, -v233\n"
    "ds_read_b128 v[216:219], v224 offset:11264\n"
    "v_cvt_pk_bf16_f32 v64, v0, v1\n"
    "v_cvt_pk_bf16_f32 v65, v2, v3\n"
    "v_cvt_pk_bf16_f32 v66, v4, v5\n"
    "v_mfma_f32_32x32x16_bf16 v[48:63], v[124:127], v[168:171], v[48:63]\n"
    "v_cvt_pk_bf16_f32 v67, v6, v7\n"
    "v_cvt_pk_bf16_f32 v68, v8, v9\n"
    "v_cvt_pk_bf16_f32 v69, v10, v11\n"
    "v_cvt_pk_bf16_f32 v70, v12, v13\n"
    "v_cvt_pk_bf16_f32 v71, v14, v15\n"
    "v_cvt_pk_bf16_f32 v80, v32, v33\n"
    "v_cvt_pk_bf16_f32 v81, v34, v35\n"
    "v_cvt_pk_bf16_f32 v82, v36, v37\n"
    "v_cvt_pk_bf16_f32 v83, v38, v39\n"
    "v_cvt_pk_bf16_f32 v84, v40, v41\n"
    "v_cvt_pk_bf16_f32 v85, v42, v43\n"
    "v_cvt_pk_bf16_f32 v86, v44, v45\n"
    "v_cvt_pk_bf16_f32 v87, v46, v47\n"
    "ds_read_b128 v[112:115], v221 offset:12288\n"
    "ds_read_b128 v[116:119], v221 offset:20480\n"
    "ds_read_b128 v[120:123], v221 offset:28672\n"
    "ds_read_b128 v[124:127], v221 offset:13312\n"
    "ds_read_b128 v[128:131], v221 offset:21504\n"
    "ds_read_b128 v[132:135], v221 offset:29696\n"
    "ds_read_b128 v[136:139], v221 offset:14336\n"
    "ds_read_b128 v[140:143], v221 offset:22528\n"
    "ds_read_b128 v[144:147], v221 offset:30720\n"
    "s_waitcnt lgkmcnt(6)\n"
    "v_mfma_f32_32x32x16_bf16 v[0:15], v[112:115], v[64:67], 0\n"
    "v_mul_f32 v226, v48, v205\n"
    "v_mul_f32 v227, v16, v205\n"
    "v_mul_f32 v228, v49, v207\n"
    "v_mul_f32 v229, v17, v207\n"
    "v_fma_f32 v16, v16, v204, v226\n"
    "v_fma_f32 v48, v48, v204, -v227\n"
    "v_fma_f32 v17, v17, v206, v228\n"
    "v_fma_f32 v49, v49, v206, -v229\n"
    "ds_read_b128 v[204:207], v224 offset:12288\n"
    "v_mul_f32 v230, v50, v209\n"
    "v_mul_f32 v231, v18, v209\n"
    "v_mfma_f32_32x32x16_bf16 v[32:47], v[112:115], v[80:83], 0\n"
    "v_mul_f32 v232, v51, v211\n"
    "v_mul_f32 v233, v19, v211\n"
    "v_fma_f32 v18, v18, v208, v230\n"
    "v_fma_f32 v50, v50, v208, -v231\n"
    "v_fma_f32 v19, v19, v210, v232\n"
    "v_fma_f32 v51, v51, v210, -v233\n"
    "ds_read_b128 v[208:211], v224 offset:13312\n"
    "v_mul_f32 v226, v52, v213\n"
    "v_mul_f32 v227, v20, v213\n"
    "v_mul_f32 v228, v53, v215\n"
    "v_mul_f32 v229, v21, v215\n"
    "v_mfma_f32_32x32x16_bf16 v[0:15], v[116:119], v[80:83], v[0:15]\n"
    "v_fma_f32 v20, v20, v212, v226\n"
    "v_fma_f32 v52, v52, v212, -v227\n"
    "v_fma_f32 v21, v21, v214, v228\n"
    "v_fma_f32 v53, v53, v214, -v229\n"
    "ds_read_b128 v[212:215], v224 offset:14336\n"
    "v_mul_f32 v230, v54, v217\n"
    "v_mul_f32 v231, v22, v217\n"
    "v_mul_f32 v232, v55, v219\n"
    "v_mul_f32 v233, v23, v219\n"
    "v_fma_f32 v22, v22, v216, v230\n"
    "v_fma_f32 v54, v54, v216, -v231\n"
    "v_mfma_f32_32x32x16_bf16 v[32:47], v[120:123], v[64:67], v[32:47]\n"
    "ds_read_b128 v[112:115], v221 offset:15360\n"
    "ds_read_b128 v[116:119], v221 offset:23552\n"
    "ds_read_b128 v[120:123], v221 offset:31744\n"
    "v_fma_f32 v23, v23, v218, v232\n"
    "v_fma_f32 v55, v55, v218, -v233\n"
    "ds_read_b128 v[216:219], v224 offset:15360\n"
    "s_waitcnt lgkmcnt(6)\n"
    "v_mul_f32 v226, v56, v205\n"
    "v_mul_f32 v227, v24, v205\n"
    "v_mul_f32 v228, v57, v207\n"
    "v_mul_f32 v229, v25, v207\n"
    "v_fma_f32 v24, v24, v204, v226\n"
    "v_fma_f32 v56, v56, v204, -v227\n"
    "v_fma_f32 v25, v25, v206, v228\n"
    "v_fma_f32 v57, v57, v206, -v229\n"
    "ds_read_b128 v[204:207], v224 offset:0\n"
    "s_waitcnt lgkmcnt(6)\n"
    "v_mfma_f32_32x32x16_bf16 v[0:15], v[124:127], v[68:71], v[0:15]\n"
    "v_mul_f32 v230, v58, v209\n"
    "v_mul_f32 v231, v26, v209\n"
    "v_mul_f32 v232, v59, v211\n"
    "v_mul_f32 v233, v27, v211\n"
    "v_fma_f32 v26, v26, v208, v230\n"
    "v_fma_f32 v58, v58, v208, -v231\n"
    "v_fma_f32 v27, v27, v210, v232\n"
    "v_fma_f32 v59, v59, v210, -v233\n"
    "ds_read_b128 v[208:211], v224 offset:1024\n"
    "s_waitcnt lgkmcnt(6)\n"
    "v_mul_f32 v226, v60, v213\n"
    "v_mul_f32 v227, v28, v213\n"
    "v_mfma_f32_32x32x16_bf16 v[32:47], v[124:127], v[84:87], v[32:47]\n"
    "v_mul_f32 v228, v61, v215\n"
    "v_mul_f32 v229, v29, v215\n"
    "v_fma_f32 v28, v28, v212, v226\n"
    "v_fma_f32 v60, v60, v212, -v227\n"
    "v_fma_f32 v29, v29, v214, v228\n"
    "v_fma_f32 v61, v61, v214, -v229\n"
    "ds_read_b128 v[212:215], v224 offset:2048\n"
    "s_waitcnt lgkmcnt(3)\n"
    "v_mul_f32 v230, v62, v217\n"
    "v_mul_f32 v231, v30, v217\n"
    "v_mul_f32 v232, v63, v219\n"
    "v_mul_f32 v233, v31, v219\n"
    "v_mfma_f32_32x32x16_bf16 v[0:15], v[128:131], v[84:87], v[0:15]\n"
    "v_fma_f32 v30, v30, v216, v230\n"
    "v_fma_f32 v62, v62, v216, -v231\n"
    "v_fma_f32 v31, v31, v218, v232\n"
    "v_fma_f32 v63, v63, v218, -v233\n"
    "ds_read_b128 v[216:219], v224 offset:3072\n"
    "v_cvt_pk_bf16_f32 v72, v16, v17\n"
    "v_cvt_pk_bf16_f32 v73, v18, v19\n"
    "v_cvt_pk_bf16_f32 v74, v20, v21\n"
    "v_cvt_pk_bf16_f32 v75, v22, v23\n"
    "v_cvt_pk_bf16_f32 v76, v24, v25\n"
    "v_cvt_pk_bf16_f32 v77, v26, v27\n"
    "v_mfma_f32_32x32x16_bf16 v[32:47], v[132:135], v[68:71], v[32:47]\n"
    "ds_read_b128 v[124:127], v221 offset:16384\n"
    "ds_read_b128 v[128:131], v221 offset:24576\n"
    "ds_read_b128 v[132:135], v221 offset:32768\n"
    "v_cvt_pk_bf16_f32 v78, v28, v29\n"
    "v_cvt_pk_bf16_f32 v79, v30, v31\n"
    "v_cvt_pk_bf16_f32 v88, v48, v49\n"
    "v_cvt_pk_bf16_f32 v89, v50, v51\n"
    "v_cvt_pk_bf16_f32 v90, v52, v53\n"
    "v_cvt_pk_bf16_f32 v91, v54, v55\n"
    "v_cvt_pk_bf16_f32 v92, v56, v57\n"
    "v_cvt_pk_bf16_f32 v93, v58, v59\n"
    "v_cvt_pk_bf16_f32 v94, v60, v61\n"
    "v_cvt_pk_bf16_f32 v95, v62, v63\n"
    "v_mfma_f32_32x32x16_bf16 v[0:15], v[136:139], v[72:75], v[0:15]\n"
    "v_mfma_f32_32x32x16_bf16 v[32:47], v[136:139], v[88:91], v[32:47]\n"
    "v_mfma_f32_32x32x16_bf16 v[0:15], v[140:143], v[88:91], v[0:15]\n"
    "v_mfma_f32_32x32x16_bf16 v[32:47], v[144:147], v[72:75], v[32:47]\n"
    "ds_read_b128 v[136:139], v221 offset:17408\n"
    "ds_read_b128 v[140:143], v221 offset:25600\n"
    "ds_read_b128 v[144:147], v221 offset:33792\n"
    "v_mfma_f32_32x32x16_bf16 v[0:15], v[112:115], v[76:79], v[0:15]\n"
    "v_mfma_f32_32x32x16_bf16 v[32:47], v[112:115], v[92:95], v[32:47]\n"
    "v_mfma_f32_32x32x16_bf16 v[0:15], v[116:119], v[92:95], v[0:15]\n"
    "v_mfma_f32_32x32x16_bf16 v[32:47], v[120:123], v[76:79], v[32:47]\n"
    "ds_read_b128 v[112:115], v221 offset:18432\n"
    "ds_read_b128 v[116:119], v221 offset:26624\n"
    "ds_read_b128 v[120:123], v221 offset:34816\n"
    "s_waitcnt vmcnt(3)\n"
    "s_waitcnt lgkmcnt(6)\n"
    "v_mfma_f32_32x32x16_bf16 v[16:31], v[124:127], v[64:67], 0\n"
    "v_mfma_f32_32x32x16_bf16 v[48:63], v[124:127], v[80:83], 0\n"
    "v_mfma_f32_32x32x16_bf16 v[16:31], v[128:131], v[80:83], v[16:31]\n"
    "v_lshlrev_b32 v226, 16, v172\n"
    "v_and_b32 v228, 0xffff0000, v172\n"
    "v_lshlrev_b32 v227, 16, v173\n"
    "v_and_b32 v229, 0xffff0000, v173\n"
    "v_mul_f32 v230, v32, v228\n"
    "v_mul_f32 v232, v0, v228\n"
    "v_mul_f32 v231, v33, v229\n"
    "v_mul_f32 v233, v1, v229\n"
    "v_mfma_f32_32x32x16_bf16 v[48:63], v[132:135], v[64:67], v[48:63]\n"
    "ds_read_b128 v[124:127], v221 offset:19456\n"
    "ds_read_b128 v[128:131], v221 offset:27648\n"
    "ds_read_b128 v[132:135], v221 offset:35840\n"
    "v_fma_f32 v0, v0, v226, -v230\n"
    "v_fma_f32 v32, v32, v226, v232\n"
    "v_fma_f32 v1, v1, v227, -v231\n"
    "v_fma_f32 v33, v33, v227, v233\n"
    "v_lshlrev_b32 v234, 16, v174\n"
    "v_and_b32 v236, 0xffff0000, v174\n"
    "v_lshlrev_b32 v235, 16, v175\n"
    "v_and_b32 v237, 0xffff0000, v175\n"
    "s_waitcnt lgkmcnt(6)\n"
    "v_mfma_f32_32x32x16_bf16 v[16:31], v[136:139], v[68:71], v[16:31]\n"
    "v_mul_f32 v238, v34, v236\n"
    "v_mul_f32 v240, v2, v236\n"
    "v_mul_f32 v239, v35, v237\n"
    "v_mul_f32 v241, v3, v237\n"
    "v_fma_f32 v2, v2, v234, -v238\n"
    "v_fma_f32 v34, v34, v234, v240\n"
    "v_fma_f32 v3, v3, v235, -v239\n"
    "v_fma_f32 v35, v35, v235, v241\n"
    "v_mfma_f32_32x32x16_bf16 v[48:63], v[136:139], v[84:87], v[48:63]\n"
    "v_lshlrev_b32 v226, 16, v176\n"
    "v_and_b32 v228, 0xffff0000, v176\n"
    "v_lshlrev_b32 v227, 16, v177\n"
    "v_and_b32 v229, 0xffff0000, v177\n"
    "v_mul_f32 v230, v36, v228\n"
    "v_mul_f32 v232, v4, v228\n"
    "v_mul_f32 v231, v37, v229\n"
    "v_mul_f32 v233, v5, v229\n"
    "v_mfma_f32_32x32x16_bf16 v[16:31], v[140:143], v[84:87], v[16:31]\n"
    "v_fma_f32 v4, v4, v226, -v230\n"
    "v_fma_f32 v36, v36, v226, v232\n"
    "v_fma_f32 v5, v5, v227, -v231\n"
    "v_fma_f32 v37, v37, v227, v233\n"
    "v_lshlrev_b32 v234, 16, v178\n"
    "v_and_b32 v236, 0xffff0000, v178\n"
    "v_lshlrev_b32 v235, 16, v179\n"
    "v_and_b32 v237, 0xffff0000, v179\n"
    "v_mfma_f32_32x32x16_bf16 v[48:63], v[144:147], v[68:71], v[48:63]\n"
    "ds_read_b128 v[136:139], v221 offset:12288\n"
    "ds_read_b128 v[140:143], v221 offset:20480\n"
    "ds_read_b128 v[144:147], v221 offset:28672\n"
    "v_mul_f32 v238, v38, v236\n"
    "v_mul_f32 v240, v6, v236\n"
    "v_mul_f32 v239, v39, v237\n"
    "v_mul_f32 v241, v7, v237\n"
    "v_fma_f32 v6, v6, v234, -v238\n"
    "v_fma_f32 v38, v38, v234, v240\n"
    "v_fma_f32 v7, v7, v235, -v239\n"
    "v_fma_f32 v39, v39, v235, v241\n"
    "s_waitcnt lgkmcnt(6)\n"
    "v_mfma_f32_32x32x16_bf16 v[16:31], v[112:115], v[72:75], v[16:31]\n"
    "v_lshlrev_b32 v226, 16, v180\n"
    "v_and_b32 v228, 0xffff0000, v180\n"
    "v_lshlrev_b32 v227, 16, v181\n"
    "v_and_b32 v229, 0xffff0000, v181\n"
    "v_mul_f32 v230, v40, v228\n"
    "v_mul_f32 v232, v8, v228\n"
    "v_mul_f32 v231, v41, v229\n"
    "v_mul_f32 v233, v9, v229\n"
    "v_mfma_f32_32x32x16_bf16 v[48:63], v[112:115], v[88:91], v[48:63]\n"
    "v_fma_f32 v8, v8, v226, -v230\n"
    "v_fma_f32 v40, v40, v226, v232\n"
    "v_fma_f32 v9, v9, v227, -v231\n"
    "v_fma_f32 v41, v41, v227, v233\n"
    "v_lshlrev_b32 v234, 16, v182\n"
    "v_and_b32 v236, 0xffff0000, v182\n"
    "v_lshlrev_b32 v235, 16, v183\n"
    "v_and_b32 v237, 0xffff0000, v183\n"
    "v_mfma_f32_32x32x16_bf16 v[16:31], v[116:119], v[88:91], v[16:31]\n"
    "v_mul_f32 v238, v42, v236\n"
    "v_mul_f32 v240, v10, v236\n"
    "v_mul_f32 v239, v43, v237\n"
    "v_mul_f32 v241, v11, v237\n"
    "v_fma_f32 v10, v10, v234, -v238\n"
    "v_fma_f32 v42, v42, v234, v240\n"
    "v_fma_f32 v11, v11, v235, -v239\n"
    "v_fma_f32 v43, v43, v235, v241\n"
    "v_mfma_f32_32x32x16_bf16 v[48:63], v[120:123], v[72:75], v[48:63]\n"
    "ds_read_b128 v[112:115], v221 offset:13312\n"
    "ds_read_b128 v[116:119], v221 offset:21504\n"
    "ds_read_b128 v[120:123], v221 offset:29696\n"
    "v_lshlrev_b32 v226, 16, v184\n"
    "v_and_b32 v228, 0xffff0000, v184\n"
    "v_lshlrev_b32 v227, 16, v185\n"
    "v_and_b32 v229, 0xffff0000, v185\n"
    "v_mul_f32 v230, v44, v228\n"
    "v_mul_f32 v232, v12, v228\n"
    "v_mul_f32 v231, v45, v229\n"
    "v_mul_f32 v233, v13, v229\n"
    "s_waitcnt lgkmcnt(6)\n"
    "v_mfma_f32_32x32x16_bf16 v[16:31], v[124:127], v[76:79], v[16:31]\n"
    "v_fma_f32 v12, v12, v226, -v230\n"
    "v_fma_f32 v44, v44, v226, v232\n"
    "v_fma_f32 v13, v13, v227, -v231\n"
    "v_fma_f32 v45, v45, v227, v233\n"
    "v_lshlrev_b32 v234, 16, v186\n"
    "v_and_b32 v236, 0xffff0000, v186\n"
    "v_lshlrev_b32 v235, 16, v187\n"
    "v_and_b32 v237, 0xffff0000, v187\n"
    "v_mfma_f32_32x32x16_bf16 v[48:63], v[124:127], v[92:95], v[48:63]\n"
    "v_mul_f32 v238, v46, v236\n"
    "v_mul_f32 v240, v14, v236\n"
    "v_mul_f32 v239, v47, v237\n"
    "v_mul_f32 v241, v15, v237\n"
    "v_fma_f32 v14, v14, v234, -v238\n"
    "v_fma_f32 v46, v46, v234, v240\n"
    "v_fma_f32 v15, v15, v235, -v239\n"
    "v_fma_f32 v47, v47, v235, v241\n"
    "v_mfma_f32_32x32x16_bf16 v[16:31], v[128:131], v[92:95], v[16:31]\n"
    "v_cvt_pk_bf16_f32 v96, v0, v1\n"
    "v_cvt_pk_bf16_f32 v97, v2, v3\n"
    "v_cvt_pk_bf16_f32 v98, v4, v5\n"
    "v_cvt_pk_bf16_f32 v99, v6, v7\n"
    "v_cvt_pk_bf16_f32 v100, v8, v9\n"
    "v_cvt_pk_bf16_f32 v101, v10, v11\n"
    "v_cvt_pk_bf16_f32 v102, v12, v13\n"
    "v_cvt_pk_bf16_f32 v103, v14, v15\n"
    "v_mfma_f32_32x32x16_bf16 v[48:63], v[132:135], v[76:79], v[48:63]\n"
    "ds_read_b128 v[124:127], v221 offset:14336\n"
    "ds_read_b128 v[128:131], v221 offset:22528\n"
    "ds_read_b128 v[132:135], v221 offset:30720\n"
    "v_cvt_pk_bf16_f32 v104, v32, v33\n"
    "v_cvt_pk_bf16_f32 v105, v34, v35\n"
    "v_cvt_pk_bf16_f32 v106, v36, v37\n"
    "v_cvt_pk_bf16_f32 v107, v38, v39\n"
    "v_cvt_pk_bf16_f32 v108, v40, v41\n"
    "v_cvt_pk_bf16_f32 v109, v42, v43\n"
    "v_cvt_pk_bf16_f32 v110, v44, v45\n"
    "v_cvt_pk_bf16_f32 v111, v46, v47\n"
    "s_waitcnt lgkmcnt(6)\n"
    "v_mfma_f32_32x32x16_bf16 v[0:15], v[96:99], v[136:139], 0\n"
    "v_lshlrev_b32 v226, 16, v188\n"
    "v_and_b32 v228, 0xffff0000, v188\n"
    "v_lshlrev_b32 v227, 16, v189\n"
    "v_and_b32 v229, 0xffff0000, v189\n"
    "v_mul_f32 v230, v48, v228\n"
    "v_mul_f32 v232, v16, v228\n"
    "v_mul_f32 v231, v49, v229\n"
    "v_mul_f32 v233, v17, v229\n"
    "v_fma_f32 v16, v16, v226, -v230\n"
    "v_fma_f32 v48, v48, v226, v232\n"
    "v_fma_f32 v17, v17, v227, -v231\n"
    "v_fma_f32 v49, v49, v227, v233\n"
    "v_lshlrev_b32 v234, 16, v190\n"
    "v_and_b32 v236, 0xffff0000, v190\n"
    "v_mfma_f32_32x32x16_bf16 v[32:47], v[104:107], v[136:139], 0\n"
    "v_lshlrev_b32 v235, 16, v191\n"
    "v_and_b32 v237, 0xffff0000, v191\n"
    "v_mul_f32 v238, v50, v236\n"
    "v_mul_f32 v240, v18, v236\n"
    "v_mul_f32 v239, v51, v237\n"
    "v_mul_f32 v241, v19, v237\n"
    "v_fma_f32 v18, v18, v234, -v238\n"
    "v_fma_f32 v50, v50, v234, v240\n"
    "v_fma_f32 v19, v19, v235, -v239\n"
    "v_fma_f32 v51, v51, v235, v241\n"
    "v_lshlrev_b32 v226, 16, v192\n"
    "v_and_b32 v228, 0xffff0000, v192\n"
    "v_lshlrev_b32 v227, 16, v193\n"
    "v_and_b32 v229, 0xffff0000, v193\n"
    "v_mfma_f32_32x32x16_bf16 v[0:15], v[104:107], v[144:147], v[0:15]\n"
    "v_mul_f32 v230, v52, v228\n"
    "v_mul_f32 v232, v20, v228\n"
    "v_mul_f32 v231, v53, v229\n"
    "v_mul_f32 v233, v21, v229\n"
    "v_fma_f32 v20, v20, v226, -v230\n"
    "v_fma_f32 v52, v52, v226, v232\n"
    "v_fma_f32 v21, v21, v227, -v231\n"
    "v_fma_f32 v53, v53, v227, v233\n"
    "v_lshlrev_b32 v234, 16, v194\n"
    "v_and_b32 v236, 0xffff0000, v194\n"
    "v_lshlrev_b32 v235, 16, v195\n"
    "v_and_b32 v237, 0xffff0000, v195\n"
    "v_mul_f32 v238, v54, v236\n"
    "v_mul_f32 v240, v22, v236\n"
    "v_mfma_f32_32x32x16_bf16 v[32:47], v[96:99], v[140:143], v[32:47]\n"
    "ds_read_b128 v[136:139], v221 offset:15360\n"
    "ds_read_b128 v[140:143], v221 offset:23552\n"
    "ds_read_b128 v[144:147], v221 offset:31744\n"
    "v_mul_f32 v239, v55, v237\n"
    "v_mul_f32 v241, v23, v237\n"
    "v_fma_f32 v22, v22, v234, -v238\n"
    "v_fma_f32 v54, v54, v234, v240\n"
    "v_fma_f32 v23, v23, v235, -v239\n"
    "v_fma_f32 v55, v55, v235, v241\n"
    "v_lshlrev_b32 v226, 16, v196\n"
    "v_and_b32 v228, 0xffff0000, v196\n"
    "v_lshlrev_b32 v227, 16, v197\n"
    "v_and_b32 v229, 0xffff0000, v197\n"
    "v_mul_f32 v230, v56, v228\n"
    "v_mul_f32 v232, v24, v228\n"
    "v_mul_f32 v231, v57, v229\n"
    "v_mul_f32 v233, v25, v229\n"
    "s_waitcnt lgkmcnt(6)\n"
    "v_mfma_f32_32x32x16_bf16 v[0:15], v[100:103], v[112:115], v[0:15]\n"
    "v_fma_f32 v24, v24, v226, -v230\n"
    "v_fma_f32 v56, v56, v226, v232\n"
    "v_fma_f32 v25, v25, v227, -v231\n"
    "v_fma_f32 v57, v57, v227, v233\n"
    "v_lshlrev_b32 v234, 16, v198\n"
    "v_and_b32 v236, 0xffff0000, v198\n"
    "v_lshlrev_b32 v235, 16, v199\n"
    "v_and_b32 v237, 0xffff0000, v199\n"
    "v_mul_f32 v238, v58, v236\n"
    "v_mul_f32 v240, v26, v236\n"
    "v_mul_f32 v239, v59, v237\n"
    "v_mul_f32 v241, v27, v237\n"
    "v_fma_f32 v26, v26, v234, -v238\n"
    "v_fma_f32 v58, v58, v234, v240\n"
    "v_mfma_f32_32x32x16_bf16 v[32:47], v[108:111], v[112:115], v[32:47]\n"
    "v_fma_f32 v27, v27, v235, -v239\n"
    "v_fma_f32 v59, v59, v235, v241\n"
    "v_lshlrev_b32 v226, 16, v200\n"
    "v_and_b32 v228, 0xffff0000, v200\n"
    "v_lshlrev_b32 v227, 16, v201\n"
    "v_and_b32 v229, 0xffff0000, v201\n"
    "v_mul_f32 v230, v60, v228\n"
    "v_mul_f32 v232, v28, v228\n"
    "v_mul_f32 v231, v61, v229\n"
    "v_mul_f32 v233, v29, v229\n"
    "v_fma_f32 v28, v28, v226, -v230\n"
    "v_fma_f32 v60, v60, v226, v232\n"
    "v_fma_f32 v29, v29, v227, -v231\n"
    "v_fma_f32 v61, v61, v227, v233\n"
    "v_mfma_f32_32x32x16_bf16 v[0:15], v[108:111], v[120:123], v[0:15]\n"
    "v_lshlrev_b32 v234, 16, v202\n"
    "v_and_b32 v236, 0xffff0000, v202\n"
    "v_lshlrev_b32 v235, 16, v203\n"
    "v_and_b32 v237, 0xffff0000, v203\n"
    "v_mul_f32 v238, v62, v236\n"
    "v_mul_f32 v240, v30, v236\n"
    "v_mul_f32 v239, v63, v237\n"
    "v_mul_f32 v241, v31, v237\n"
    "v_fma_f32 v30, v30, v234, -v238\n"
    "v_fma_f32 v62, v62, v234, v240\n"
    "v_fma_f32 v31, v31, v235, -v239\n"
    "v_fma_f32 v63, v63, v235, v241\n"
    "v_cvt_pk_bf16_f32 v72, v16, v17\n"
    "v_cvt_pk_bf16_f32 v73, v18, v19\n"
    "v_mfma_f32_32x32x16_bf16 v[32:47], v[100:103], v[116:119], v[32:47]\n"
    "ds_read_b128 v[112:115], v221 offset:16384\n"
    "ds_read_b128 v[116:119], v221 offset:24576\n"
    "ds_read_b128 v[120:123], v221 offset:32768\n"
    "v_cvt_pk_bf16_f32 v74, v20, v21\n"
    "v_cvt_pk_bf16_f32 v75, v22, v23\n"
    "v_cvt_pk_bf16_f32 v76, v24, v25\n"
    "v_cvt_pk_bf16_f32 v77, v26, v27\n"
    "v_cvt_pk_bf16_f32 v78, v28, v29\n"
    "v_cvt_pk_bf16_f32 v79, v30, v31\n"
    "v_cvt_pk_bf16_f32 v88, v48, v49\n"
    "v_cvt_pk_bf16_f32 v89, v50, v51\n"
    "v_cvt_pk_bf16_f32 v90, v52, v53\n"
    "v_cvt_pk_bf16_f32 v91, v54, v55\n"
    "v_cvt_pk_bf16_f32 v92, v56, v57\n"
    "v_cvt_pk_bf16_f32 v93, v58, v59\n"
    "v_cvt_pk_bf16_f32 v94, v60, v61\n"
    "v_cvt_pk_bf16_f32 v95, v62, v63\n"
    "ds_read_b128 v[148:151], v221 offset:6144\n"
    "ds_read_b128 v[152:155], v221 offset:7168\n"
    "ds_read_b128 v[156:159], v221 offset:8192\n"
    "ds_read_b128 v[160:163], v221 offset:9216\n"
    "ds_read_b128 v[164:167], v221 offset:10240\n"
    "ds_read_b128 v[168:171], v221 offset:11264\n"
    "s_waitcnt lgkmcnt(12)\n"
    "v_mfma_f32_32x32x16_bf16 v[0:15], v[72:75], v[124:127], v[0:15]\n"
    "v_mfma_f32_32x32x16_bf16 v[32:47], v[88:91], v[124:127], v[32:47]\n"
    "v_mfma_f32_32x32x16_bf16 v[0:15], v[88:91], v[132:135], v[0:15]\n"
    "v_mfma_f32_32x32x16_bf16 v[32:47], v[72:75], v[128:131], v[32:47]\n"
    "ds_read_b128 v[124:127], v221 offset:17408\n"
    "ds_read_b128 v[128:131], v221 offset:25600\n"
    "ds_read_b128 v[132:135], v221 offset:33792\n"
    "s_waitcnt lgkmcnt(12)\n"
    "v_mfma_f32_32x32x16_bf16 v[0:15], v[76:79], v[136:139], v[0:15]\n"
    "v_mfma_f32_32x32x16_bf16 v[32:47], v[92:95], v[136:139], v[32:47]\n"
    "v_mfma_f32_32x32x16_bf16 v[0:15], v[92:95], v[144:147], v[0:15]\n"
    "v_mfma_f32_32x32x16_bf16 v[32:47], v[76:79], v[140:143], v[32:47]\n"
    "ds_read_b128 v[136:139], v221 offset:18432\n"
    "ds_read_b128 v[140:143], v221 offset:26624\n"
    "ds_read_b128 v[144:147], v221 offset:34816\n"
    "s_waitcnt lgkmcnt(12)\n"
    "v_mfma_f32_32x32x16_bf16 v[16:31], v[96:99], v[112:115], 0\n"
    "v_mfma_f32_32x32x16_bf16 v[48:63], v[104:107], v[112:115], 0\n"
    "v_mfma_f32_32x32x16_bf16 v[16:31], v[104:107], v[120:123], v[16:31]\n"
    "s_nop 4\n"
    "v_mul_f32 v226, v32, v205\n"
    "v_mul_f32 v227, v0, v205\n"
    "v_mul_f32 v228, v33, v207\n"
    "v_mul_f32 v229, v1, v207\n"
    "v_fma_f32 v0, v0, v204, -v226\n"
    "v_fma_f32 v32, v32, v204, v227\n"
    "v_mfma_f32_32x32x16_bf16 v[48:63], v[96:99], v[116:119], v[48:63]\n"
    "ds_read_b128 v[112:115], v221 offset:19456\n"
    "ds_read_b128 v[116:119], v221 offset:27648\n"
    "ds_read_b128 v[120:123], v221 offset:35840\n"
    "v_fma_f32 v1, v1, v206, -v228\n"
    "v_fma_f32 v33, v33, v206, v229\n"
    "ds_read_b128 v[204:207], v224 offset:4096\n"
    "v_mul_f32 v230, v34, v209\n"
    "v_mul_f32 v231, v2, v209\n"
    "v_mul_f32 v232, v35, v211\n"
    "v_mul_f32 v233, v3, v211\n"
    "s_waitcnt lgkmcnt(7)\n"
    "v_mfma_f32_32x32x16_bf16 v[16:31], v[100:103], v[124:127], v[16:31]\n"
    "v_fma_f32 v2, v2, v208, -v230\n"
    "v_fma_f32 v34, v34, v208, v231\n"
    "v_fma_f32 v3, v3, v210, -v232\n"
    "v_fma_f32 v35, v35, v210, v233\n"
    "ds_read_b128 v[208:211], v224 offset:5120\n"
    "v_mul_f32 v226, v36, v213\n"
    "v_mul_f32 v227, v4, v213\n"
    "v_mfma_f32_32x32x16_bf16 v[48:63], v[108:111], v[124:127], v[48:63]\n"
    "v_mul_f32 v228, v37, v215\n"
    "v_mul_f32 v229, v5, v215\n"
    "v_fma_f32 v4, v4, v212, -v226\n"
    "v_fma_f32 v36, v36, v212, v227\n"
    "v_fma_f32 v5, v5, v214, -v228\n"
    "v_fma_f32 v37, v37, v214, v229\n"
    "ds_read_b128 v[212:215], v224 offset:6144\n"
    "v_mfma_f32_32x32x16_bf16 v[16:31], v[108:111], v[132:135], v[16:31]\n"
    "v_mul_f32 v230, v38, v217\n"
    "v_mul_f32 v231, v6, v217\n"
    "v_mul_f32 v232, v39, v219\n"
    "v_mul_f32 v233, v7, v219\n"
    "v_fma_f32 v6, v6, v216, -v230\n"
    "v_fma_f32 v38, v38, v216, v231\n"
    "v_mfma_f32_32x32x16_bf16 v[48:63], v[100:103], v[128:131], v[48:63]\n"
    "v_fma_f32 v7, v7, v218, -v232\n"
    "v_fma_f32 v39, v39, v218, v233\n"
    "ds_read_b128 v[216:219], v224 offset:7168\n"
    "s_waitcnt lgkmcnt(3)\n"
    "v_mul_f32 v226, v40, v205\n"
    "v_mul_f32 v227, v8, v205\n"
    "v_mul_f32 v228, v41, v207\n"
    "v_mul_f32 v229, v9, v207\n"
    "v_mfma_f32_32x32x16_bf16 v[16:31], v[72:75], v[136:139], v[16:31]\n"
    "v_fma_f32 v8, v8, v204, -v226\n"
    "v_fma_f32 v40, v40, v204, v227\n"
    "v_fma_f32 v9, v9, v206, -v228\n"
    "v_fma_f32 v41, v41, v206, v229\n"
    "ds_read_b128 v[204:207], v224 offset:16384\n"
    "s_waitcnt lgkmcnt(3)\n"
    "v_mul_f32 v230, v42, v209\n"
    "v_mul_f32 v231, v10, v209\n"
    "v_mfma_f32_32x32x16_bf16 v[48:63], v[88:91], v[136:139], v[48:63]\n"
    "v_mul_f32 v232, v43, v211\n"
    "v_mul_f32 v233, v11, v211\n"
    "v_fma_f32 v10, v10, v208, -v230\n"
    "v_fma_f32 v42, v42, v208, v231\n"
    "v_fma_f32 v11, v11, v210, -v232\n"
    "v_fma_f32 v43, v43, v210, v233\n"
    "ds_read_b128 v[208:211], v224 offset:17408\n"
    "s_waitcnt lgkmcnt(3)\n"
    "v_mfma_f32_32x32x16_bf16 v[16:31], v[88:91], v[144:147], v[16:31]\n"
    "v_mul_f32 v226, v44, v213\n"
    "v_mul_f32 v227, v12, v213\n"
    "v_mul_f32 v228, v45, v215\n"
    "v_mul_f32 v229, v13, v215\n"
    "v_fma_f32 v12, v12, v212, -v226\n"
    "v_fma_f32 v44, v44, v212, v227\n"
    "v_mfma_f32_32x32x16_bf16 v[48:63], v[72:75], v[140:143], v[48:63]\n"
    "v_fma_f32 v13, v13, v214, -v228\n"
    "v_fma_f32 v45, v45, v214, v229\n"
    "ds_read_b128 v[212:215], v224 offset:18432\n"
    "s_waitcnt lgkmcnt(3)\n"
    "v_mul_f32 v230, v46, v217\n"
    "v_mul_f32 v231, v14, v217\n"
    "v_mul_f32 v232, v47, v219\n"
    "v_mul_f32 v233, v15, v219\n"
    "v_mfma_f32_32x32x16_bf16 v[16:31], v[76:79], v[112:115], v[16:31]\n"
    "v_fma_f32 v14, v14, v216, -v230\n"
    "v_fma_f32 v46, v46, v216, v231\n"
    "v_fma_f32 v15, v15, v218, -v232\n"
    "v_fma_f32 v47, v47, v218, v233\n"
    "ds_read_b128 v[216:219], v224 offset:19456\n"
    "v_cvt_pk_bf16_f32 v64, v0, v1\n"
    "v_mfma_f32_32x32x16_bf16 v[48:63], v[92:95], v[112:115], v[48:63]\n"
    "v_cvt_pk_bf16_f32 v65, v2, v3\n"
    "v_cvt_pk_bf16_f32 v66, v4, v5\n"
    "v_cvt_pk_bf16_f32 v67, v6, v7\n"
    "v_cvt_pk_bf16_f32 v68, v8, v9\n"
    "v_cvt_pk_bf16_f32 v69, v10, v11\n"
    "v_mfma_f32_32x32x16_bf16 v[16:31], v[92:95], v[120:123], v[16:31]\n"
    "v_cvt_pk_bf16_f32 v70, v12, v13\n"
    "v_cvt_pk_bf16_f32 v71, v14, v15\n"
    "v_cvt_pk_bf16_f32 v80, v32, v33\n"
    "v_cvt_pk_bf16_f32 v81, v34, v35\n"
    "v_cvt_pk_bf16_f32 v82, v36, v37\n"
    "v_mfma_f32_32x32x16_bf16 v[48:63], v[76:79], v[116:119], v[48:63]\n"
    "v_cvt_pk_bf16_f32 v83, v38, v39\n"
    "v_cvt_pk_bf16_f32 v84, v40, v41\n"
    "v_cvt_pk_bf16_f32 v85, v42, v43\n"
    "v_cvt_pk_bf16_f32 v86, v44, v45\n"
    "v_cvt_pk_bf16_f32 v87, v46, v47\n"
    "v_mfma_f32_32x32x16_bf16 v[0:15], v[64:67], v[148:151], 0\n"
    "s_waitcnt lgkmcnt(3)\n"
    "s_nop 4\n"
    "v_mul_f32 v226, v48, v205\n"
    "v_mul_f32 v227, v16, v205\n"
    "v_mul_f32 v228, v49, v207\n"
    "v_mul_f32 v229, v17, v207\n"
    "v_fma_f32 v16, v16, v204, -v226\n"
    "v_fma_f32 v48, v48, v204, v227\n"
    "v_fma_f32 v17, v17, v206, -v228\n"
    "v_fma_f32 v49, v49, v206, v229\n"
    "ds_read_b128 v[204:207], v224 offset:20480\n"
    "s_waitcnt lgkmcnt(3)\n"
    "v_mul_f32 v230, v50, v209\n"
    "v_mul_f32 v231, v18, v209\n"
    "v_mfma_f32_32x32x16_bf16 v[32:47], v[80:83], v[148:151], 0\n"
    "v_mul_f32 v232, v51, v211\n"
    "v_mul_f32 v233, v19, v211\n"
    "v_fma_f32 v18, v18, v208, -v230\n"
    "v_fma_f32 v50, v50, v208, v231\n"
    "v_fma_f32 v19, v19, v210, -v232\n"
    "v_fma_f32 v51, v51, v210, v233\n"
    "ds_read_b128 v[208:211], v224 offset:21504\n"
    "s_waitcnt lgkmcnt(3)\n"
    "v_mul_f32 v226, v52, v213\n"
    "v_mul_f32 v227, v20, v213\n"
    "v_mul_f32 v228, v53, v215\n"
    "v_mul_f32 v229, v21, v215\n"
    "v_mfma_f32_32x32x16_bf16 v[0:15], v[80:83], v[164:167], v[0:15]\n"
    "v_fma_f32 v20, v20, v212, -v226\n"
    "v_fma_f32 v52, v52, v212, v227\n"
    "v_fma_f32 v21, v21, v214, -v228\n"
    "v_fma_f32 v53, v53, v214, v229\n"
    "ds_read_b128 v[212:215], v224 offset:22528\n"
    "s_waitcnt lgkmcnt(3)\n"
    "v_mul_f32 v230, v54, v217\n"
    "v_mul_f32 v231, v22, v217\n"
    "v_mul_f32 v232, v55, v219\n"
    "v_mul_f32 v233, v23, v219\n"
    "v_fma_f32 v22, v22, v216, -v230\n"
    "v_fma_f32 v54, v54, v216, v231\n"
    "v_mfma_f32_32x32x16_bf16 v[32:47], v[64:67], v[156:159], v[32:47]\n"
    "v_fma_f32 v23, v23, v218, -v232\n"
    "v_fma_f32 v55, v55, v218, v233\n"
    "ds_read_b128 v[216:219], v224 offset:23552\n"
    "s_waitcnt lgkmcnt(3)\n"
    "v_mul_f32 v226, v56, v205\n"
    "v_mul_f32 v227, v24, v205\n"
    "v_mul_f32 v228, v57, v207\n"
    "v_mul_f32 v229, v25, v207\n"
    "v_fma_f32 v24, v24, v204, -v226\n"
    "v_fma_f32 v56, v56, v204, v227\n"
    "v_fma_f32 v25, v25, v206, -v228\n"
    "v_fma_f32 v57, v57, v206, v229\n"
    "s_waitcnt lgkmcnt(2)\n"
    "v_mfma_f32_32x32x16_bf16 v[0:15], v[68:71], v[152:155], v[0:15]\n"
    "v_mul_f32 v230, v58, v209\n"
    "v_mul_f32 v231, v26, v209\n"
    "v_mul_f32 v232, v59, v211\n"
    "v_mul_f32 v233, v27, v211\n"
    "v_fma_f32 v26, v26, v208, -v230\n"
    "v_fma_f32 v58, v58, v208, v231\n"
    "v_fma_f32 v27, v27, v210, -v232\n"
    "v_fma_f32 v59, v59, v210, v233\n"
    "s_waitcnt lgkmcnt(1)\n"
    "v_mul_f32 v226, v60, v213\n"
    "v_mul_f32 v227, v28, v213\n"
    "v_mfma_f32_32x32x16_bf16 v[32:47], v[84:87], v[152:155], v[32:47]\n"
    "v_mul_f32 v228, v61, v215\n"
    "v_mul_f32 v229, v29, v215\n"
    "v_fma_f32 v28, v28, v212, -v226\n"
    "v_fma_f32 v60, v60, v212, v227\n"
    "v_fma_f32 v29, v29, v214, -v228\n"
    "v_fma_f32 v61, v61, v214, v229\n"
    "s_waitcnt lgkmcnt(0)\n"
    "v_mul_f32 v230, v62, v217\n"
    "v_mul_f32 v231, v30, v217\n"
    "v_mul_f32 v232, v63, v219\n"
    "v_mul_f32 v233, v31, v219\n"
    "v_mfma_f32_32x32x16_bf16 v[0:15], v[84:87], v[168:171], v[0:15]\n"
    "v_fma_f32 v30, v30, v216, -v230\n"
    "v_fma_f32 v62, v62, v216, v231\n"
    "v_fma_f32 v31, v31, v218, -v232\n"
    "v_fma_f32 v63, v63, v218, v233\n"
    "v_cvt_pk_bf16_f32 v72, v16, v17\n"
    "v_cvt_pk_bf16_f32 v73, v18, v19\n"
    "v_cvt_pk_bf16_f32 v74, v20, v21\n"
    "v_cvt_pk_bf16_f32 v75, v22, v23\n"
    "v_cvt_pk_bf16_f32 v76, v24, v25\n"
    "v_cvt_pk_bf16_f32 v77, v26, v27\n"
    "v_mfma_f32_32x32x16_bf16 v[32:47], v[68:71], v[160:163], v[32:47]\n"
    "v_cvt_pk_bf16_f32 v78, v28, v29\n"
    "v_cvt_pk_bf16_f32 v79, v30, v31\n"
    "v_cvt_pk_bf16_f32 v88, v48, v49\n"
    "v_cvt_pk_bf16_f32 v89, v50, v51\n"
    "v_cvt_pk_bf16_f32 v90, v52, v53\n"
    "v_cvt_pk_bf16_f32 v91, v54, v55\n"
    "v_cvt_pk_bf16_f32 v92, v56, v57\n"
    "v_cvt_pk_bf16_f32 v93, v58, v59\n"
    "v_cvt_pk_bf16_f32 v94, v60, v61\n"
    "v_cvt_pk_bf16_f32 v95, v62, v63\n"
    "v_mfma_f32_32x32x16_bf16 v[16:31], v[72:75], v[148:151], 0\n"
    "v_mfma_f32_32x32x16_bf16 v[48:63], v[88:91], v[148:151], 0\n"
    "v_mfma_f32_32x32x16_bf16 v[16:31], v[88:91], v[164:167], v[16:31]\n"
    "v_xor_b32 v120, 0, v225\n"
    "ds_read_b128 v[112:115], v120\n"
    "v_xor_b32 v121, 32, v225\n"
    "ds_read_b128 v[116:119], v121\n"
    "s_waitcnt lgkmcnt(1)\n"
    "v_mul_f32 v0, v0, v112\n"
    "v_mul_f32 v1, v1, v113\n"
    "v_mul_f32 v2, v2, v114\n"
    "v_mul_f32 v3, v3, v115\n"
    "v_mul_f32 v32, v32, v112\n"
    "v_mul_f32 v33, v33, v113\n"
    "v_mul_f32 v34, v34, v114\n"
    "v_mul_f32 v35, v35, v115\n"
    "v_mfma_f32_32x32x16_bf16 v[48:63], v[72:75], v[156:159], v[48:63]\n"
    "v_cvt_pk_bf16_f32 v226, v0, v1\n"
    "v_cvt_pk_bf16_f32 v227, v2, v3\n"
    "ds_write_b64 v242, v[226:227] offset:0\n"
    "v_cvt_pk_bf16_f32 v234, v32, v33\n"
    "v_cvt_pk_bf16_f32 v235, v34, v35\n"
    "ds_write_b64 v242, v[234:235] offset:4128\n"
    "v_xor_b32 v120, 64, v225\n"
    "ds_read_b128 v[112:115], v120\n"
    "s_waitcnt lgkmcnt(3)\n"
    "v_mul_f32 v4, v4, v116\n"
    "v_mul_f32 v5, v5, v117\n"
    "v_mul_f32 v6, v6, v118\n"
    "v_mfma_f32_32x32x16_bf16 v[16:31], v[76:79], v[152:155], v[16:31]\n"
    "v_mul_f32 v7, v7, v119\n"
    "v_mul_f32 v36, v36, v116\n"
    "v_mul_f32 v37, v37, v117\n"
    "v_mul_f32 v38, v38, v118\n"
    "v_mul_f32 v39, v39, v119\n"
    "v_cvt_pk_bf16_f32 v228, v4, v5\n"
    "v_cvt_pk_bf16_f32 v229, v6, v7\n"
    "ds_write_b64 v243, v[228:229] offset:0\n"
    "v_cvt_pk_bf16_f32 v236, v36, v37\n"
    "v_cvt_pk_bf16_f32 v237, v38, v39\n"
    "v_mfma_f32_32x32x16_bf16 v[48:63], v[92:95], v[152:155], v[48:63]\n"
    "ds_write_b64 v243, v[236:237] offset:4128\n"
    "v_xor_b32 v121, 96, v225\n"
    "ds_read_b128 v[116:119], v121\n"
    "s_waitcnt lgkmcnt(3)\n"
    "v_mul_f32 v8, v8, v112\n"
    "v_mul_f32 v9, v9, v113\n"
    "v_mul_f32 v10, v10, v114\n"
    "v_mul_f32 v11, v11, v115\n"
    "v_mul_f32 v40, v40, v112\n"
    "v_mul_f32 v41, v41, v113\n"
    "v_mul_f32 v42, v42, v114\n"
    "v_mul_f32 v43, v43, v115\n"
    "v_mfma_f32_32x32x16_bf16 v[16:31], v[92:95], v[168:171], v[16:31]\n"
    "v_cvt_pk_bf16_f32 v230, v8, v9\n"
    "v_cvt_pk_bf16_f32 v231, v10, v11\n"
    "ds_write_b64 v244, v[230:231] offset:0\n"
    "v_cvt_pk_bf16_f32 v238, v40, v41\n"
    "v_cvt_pk_bf16_f32 v239, v42, v43\n"
    "ds_write_b64 v244, v[238:239] offset:4128\n"
    "s_waitcnt lgkmcnt(2)\n"
    "v_mul_f32 v12, v12, v116\n"
    "v_mul_f32 v13, v13, v117\n"
    "v_mul_f32 v14, v14, v118\n"
    "v_mul_f32 v15, v15, v119\n"
    "v_mfma_f32_32x32x16_bf16 v[48:63], v[76:79], v[160:163], v[48:63]\n"
    "v_mul_f32 v44, v44, v116\n"
    "v_mul_f32 v45, v45, v117\n"
    "v_mul_f32 v46, v46, v118\n"
    "v_mul_f32 v47, v47, v119\n"
    "v_cvt_pk_bf16_f32 v232, v12, v13\n"
    "v_cvt_pk_bf16_f32 v233, v14, v15\n"
    "ds_write_b64 v245, v[232:233] offset:0\n"
    "v_cvt_pk_bf16_f32 v240, v44, v45\n"
    "v_cvt_pk_bf16_f32 v241, v46, v47\n"
    "ds_write_b64 v245, v[240:241] offset:4128\n"
    "v_lshrrev_b32 v204, 5, v223\n"
    "s_lshl_b32 s59, s56, 1\n"
    "v_add_u32 v204, s59, v204\n"
    "v_lshrrev_b32 v205, 2, v204\n"
    "v_and_b32 v206, 3, v204\n"
    "v_lshlrev_b32 v206, 2, v206\n"
    "v_and_b32 v207, 3, v223\n"
    "v_add_u32 v206, v206, v207\n"
    "v_xor_b32 v206, v206, v205\n"
    "v_lshlrev_b32 v206, 3, v206\n"
    "v_lshlrev_b32 v207, 7, v205\n"
    "v_add_u32 v206, v206, v207\n"
    "v_bfe_u32 v207, v223, 4, 1\n"
    "v_lshlrev_b32 v207, 3, v207\n"
    "v_bfe_u32 v208, v223, 2, 2\n"
    "v_add_u32 v207, v207, v208\n"
    "s_movk_i32 s59, 4128\n"
    "v_mul_lo_u32 v207, v207, s59\n"
    "v_add_u32 v207, 18432, v207\n"
    "v_xor_b32 v212, 0, v206\n"
    "v_add_u32 v212, v212, v207\n"
    "v_xor_b32 v213, 32, v206\n"
    "v_add_u32 v213, v213, v207\n"
    "v_xor_b32 v214, 64, v206\n"
    "v_add_u32 v214, v214, v207\n"
    "v_xor_b32 v215, 96, v206\n"
    "v_add_u32 v215, v215, v207\n"
    "v_lshrrev_b32 v208, 3, v204\n"
    "s_mov_b32 s59, 1335296\n"
    "v_mul_lo_u32 v208, v208, s59\n"
    "v_and_b32 v209, 7, v204\n"
    "v_lshlrev_b32 v209, 10, v209\n"
    "v_add_u32 v208, v208, v209\n"
    "v_and_b32 v209, 31, v223\n"
    "v_lshlrev_b32 v209, 4, v209\n"
    "v_add_u32 v208, v208, v209\n"
    "v_xor_b32 v120, 128, v225\n"
    "ds_read_b128 v[112:115], v120\n"
    "v_xor_b32 v121, 160, v225\n"
    "ds_read_b128 v[116:119], v121\n"
    "s_waitcnt lgkmcnt(1)\n"
    "v_mul_f32 v16, v16, v112\n"
    "v_mul_f32 v17, v17, v113\n"
    "v_mul_f32 v18, v18, v114\n"
    "v_mul_f32 v19, v19, v115\n"
    "v_mul_f32 v48, v48, v112\n"
    "v_mul_f32 v49, v49, v113\n"
    "v_mul_f32 v50, v50, v114\n"
    "v_mul_f32 v51, v51, v115\n"
    "v_cvt_pk_bf16_f32 v226, v16, v17\n"
    "v_cvt_pk_bf16_f32 v227, v18, v19\n"
    "ds_write_b64 v246, v[226:227] offset:0\n"
    "v_cvt_pk_bf16_f32 v234, v48, v49\n"
    "v_cvt_pk_bf16_f32 v235, v50, v51\n"
    "ds_write_b64 v246, v[234:235] offset:4128\n"
    "v_xor_b32 v120, 192, v225\n"
    "ds_read_b128 v[112:115], v120\n"
    "s_waitcnt lgkmcnt(3)\n"
    "v_mul_f32 v20, v20, v116\n"
    "v_mul_f32 v21, v21, v117\n"
    "v_mul_f32 v22, v22, v118\n"
    "v_mul_f32 v23, v23, v119\n"
    "v_mul_f32 v52, v52, v116\n"
    "v_mul_f32 v53, v53, v117\n"
    "v_mul_f32 v54, v54, v118\n"
    "v_mul_f32 v55, v55, v119\n"
    "v_cvt_pk_bf16_f32 v228, v20, v21\n"
    "v_cvt_pk_bf16_f32 v229, v22, v23\n"
    "ds_write_b64 v247, v[228:229] offset:0\n"
    "v_cvt_pk_bf16_f32 v236, v52, v53\n"
    "v_cvt_pk_bf16_f32 v237, v54, v55\n"
    "ds_write_b64 v247, v[236:237] offset:4128\n"
    "v_xor_b32 v121, 224, v225\n"
    "ds_read_b128 v[116:119], v121\n"
    "s_waitcnt lgkmcnt(3)\n"
    "v_mul_f32 v24, v24, v112\n"
    "v_mul_f32 v25, v25, v113\n"
    "v_mul_f32 v26, v26, v114\n"
    "v_mul_f32 v27, v27, v115\n"
    "v_mul_f32 v56, v56, v112\n"
    "v_mul_f32 v57, v57, v113\n"
    "v_mul_f32 v58, v58, v114\n"
    "v_mul_f32 v59, v59, v115\n"
    "v_cvt_pk_bf16_f32 v230, v24, v25\n"
    "v_cvt_pk_bf16_f32 v231, v26, v27\n"
    "ds_write_b64 v248, v[230:231] offset:0\n"
    "v_cvt_pk_bf16_f32 v238, v56, v57\n"
    "v_cvt_pk_bf16_f32 v239, v58, v59\n"
    "ds_write_b64 v248, v[238:239] offset:4128\n"
    "s_waitcnt lgkmcnt(2)\n"
    "v_mul_f32 v28, v28, v116\n"
    "v_mul_f32 v29, v29, v117\n"
    "v_mul_f32 v30, v30, v118\n"
    "v_mul_f32 v31, v31, v119\n"
    "v_mul_f32 v60, v60, v116\n"
    "v_mul_f32 v61, v61, v117\n"
    "v_mul_f32 v62, v62, v118\n"
    "v_mul_f32 v63, v63, v119\n"
    "v_cvt_pk_bf16_f32 v232, v28, v29\n"
    "v_cvt_pk_bf16_f32 v233, v30, v31\n"
    "ds_write_b64 v249, v[232:233] offset:0\n"
    "v_cvt_pk_bf16_f32 v240, v60, v61\n"
    "v_cvt_pk_bf16_f32 v241, v62, v63\n"
    "ds_write_b64 v249, v[240:241] offset:4128\n"
    "s_waitcnt lgkmcnt(0)\n"
    "s_barrier\n"
    "ds_read_b64_tr_b16 v[112:113], v212 offset:0\n"
    "ds_read_b64_tr_b16 v[114:115], v212 offset:16512\n"
    "ds_read_b64_tr_b16 v[116:117], v213 offset:512\n"
    "ds_read_b64_tr_b16 v[118:119], v213 offset:17024\n"
    "ds_read_b64_tr_b16 v[120:121], v214 offset:1024\n"
    "ds_read_b64_tr_b16 v[122:123], v214 offset:17536\n"
    "ds_read_b64_tr_b16 v[124:125], v215 offset:1536\n"
    "ds_read_b64_tr_b16 v[126:127], v215 offset:18048\n"
    "ds_read_b64_tr_b16 v[128:129], v212 offset:2048\n"
    "ds_read_b64_tr_b16 v[130:131], v212 offset:18560\n"
    "ds_read_b64_tr_b16 v[132:133], v213 offset:2560\n"
    "ds_read_b64_tr_b16 v[134:135], v213 offset:19072\n"
    "ds_read_b64_tr_b16 v[136:137], v214 offset:3072\n"
    "ds_read_b64_tr_b16 v[138:139], v214 offset:19584\n"
    "ds_read_b64_tr_b16 v[140:141], v215 offset:3584\n"
    "ds_read_b64_tr_b16 v[142:143], v215 offset:20096\n"
    "s_mov_b32 s59, s73\n"
    "s_waitcnt lgkmcnt(14)\n"
    "v_min_u32 v112, 0xfffffffe, v112\n"
    "v_min_u32 v113, 0xfffffffe, v113\n"
    "v_min_u32 v114, 0xfffffffe, v114\n"
    "v_min_u32 v115, 0xfffffffe, v115\n"
    "buffer_store_dwordx4 v[112:115], v208, s[96:99], s59 offen sc1\n"
    "s_add_u32 s59, s59, 2670592\n"
    "s_waitcnt lgkmcnt(12)\n"
    "v_min_u32 v116, 0xfffffffe, v116\n"
    "v_min_u32 v117, 0xfffffffe, v117\n"
    "v_min_u32 v118, 0xfffffffe, v118\n"
    "v_min_u32 v119, 0xfffffffe, v119\n"
    "buffer_store_dwordx4 v[116:119], v208, s[96:99], s59 offen sc1\n"
    "s_add_u32 s59, s59, 2670592\n"
    "s_waitcnt lgkmcnt(10)\n"
    "v_min_u32 v120, 0xfffffffe, v120\n"
    "v_min_u32 v121, 0xfffffffe, v121\n"
    "v_min_u32 v122, 0xfffffffe, v122\n"
    "v_min_u32 v123, 0xfffffffe, v123\n"
    "buffer_store_dwordx4 v[120:123], v208, s[96:99], s59 offen sc1\n"
    "s_add_u32 s59, s59, 2670592\n"
    "s_waitcnt lgkmcnt(8)\n"
    "v_min_u32 v124, 0xfffffffe, v124\n"
    "v_min_u32 v125, 0xfffffffe, v125\n"
    "v_min_u32 v126, 0xfffffffe, v126\n"
    "v_min_u32 v127, 0xfffffffe, v127\n"
    "buffer_store_dwordx4 v[124:127], v208, s[96:99], s59 offen sc1\n"
    "s_add_u32 s59, s59, 2670592\n"
    "s_waitcnt lgkmcnt(6)\n"
    "v_min_u32 v128, 0xfffffffe, v128\n"
    "v_min_u32 v129, 0xfffffffe, v129\n"
    "v_min_u32 v130, 0xfffffffe, v130\n"
    "v_min_u32 v131, 0xfffffffe, v131\n"
    "buffer_store_dwordx4 v[128:131], v208, s[96:99], s59 offen sc1\n"
    "s_add_u32 s59, s59, 2670592\n"
    "s_waitcnt lgkmcnt(4)\n"
    "v_min_u32 v132, 0xfffffffe, v132\n"
    "v_min_u32 v133, 0xfffffffe, v133\n"
    "v_min_u32 v134, 0xfffffffe, v134\n"
    "v_min_u32 v135, 0xfffffffe, v135\n"
    "buffer_store_dwordx4 v[132:135], v208, s[96:99], s59 offen sc1\n"
    "s_add_u32 s59, s59, 2670592\n"
    "s_waitcnt lgkmcnt(2)\n"
    "v_min_u32 v136, 0xfffffffe, v136\n"
    "v_min_u32 v137, 0xfffffffe, v137\n"
    "v_min_u32 v138, 0xfffffffe, v138\n"
    "v_min_u32 v139, 0xfffffffe, v139\n"
    "buffer_store_dwordx4 v[136:139], v208, s[96:99], s59 offen sc1\n"
    "s_add_u32 s59, s59, 2670592\n"
    "s_waitcnt lgkmcnt(0)\n"
    "v_min_u32 v140, 0xfffffffe, v140\n"
    "v_min_u32 v141, 0xfffffffe, v141\n"
    "v_min_u32 v142, 0xfffffffe, v142\n"
    "v_min_u32 v143, 0xfffffffe, v143\n"
    "buffer_store_dwordx4 v[140:143], v208, s[96:99], s59 offen sc1\n"
    "s_barrier\n"
    "L2NOF2%=:\n"
    "s_mul_i32 s59, s77, 1335296\n"
    "s_mov_b64 s[36:37], %0\n"
    "s_add_u32 s36, s36, s59\n"
    "s_addc_u32 s37, s37, 0\n"
    "s_and_b32 s37, s37, 0xffff\n"
    "s_mov_b32 s38, 1335296\n"
    "s_mov_b32 s39, 0x20000\n"
    "s_mov_b64 s[44:45], %0\n"
    "s_add_u32 s44, s44, 29360128\n"
    "s_addc_u32 s45, s45, 0\n"
    "s_and_b32 s45, s45, 0xffff\n"
    "s_mov_b32 s46, 7077888\n"
    "s_mov_b32 s47, 0x20000\n"
    "s_mov_b64 s[48:49], %2\n"
    "s_and_b32 s49, s49, 0xffff\n"
    "s_mov_b32 s50, 2359296\n"
    "s_mov_b32 s51, 0x20000\n"
    "s_mov_b64 s[52:53], %0\n"
    "s_add_u32 s52, s52, 46137344\n"
    "s_addc_u32 s53, s53, 0\n"
    "s_and_b32 s53, s53, 0xffff\n"
    "s_mov_b32 s54, 8192\n"
    "s_mov_b32 s55, 0x20000\n"
    "v_mbcnt_lo_u32_b32 v206, -1, 0\n"
    "v_mbcnt_hi_u32_b32 v206, -1, v206\n"
    "v_lshlrev_b32 v200, 4, v206\n"
    "v_lshlrev_b32 v222, 2, v206\n"
    "s_mov_b32 s70, s63\n"
    "s_sub_u32 s63, s63, 8192\n"
    "s_add_u32 m0, s67, 18432\n"
    "s_add_u32 s64, s64, 9216\n"
    "buffer_load_dwordx4 v200, s[40:43], s64 offen lds\n"
    "s_add_u32 m0, s68, 18432\n"
    "s_add_u32 s65, s65, 9216\n"
    "buffer_load_dwordx4 v200, s[40:43], s65 offen lds\n"
    "s_add_u32 m0, s69, 18432\n"
    "s_add_u32 s66, s66, 9216\n"
    "buffer_load_dword v222, s[40:43], s66 offen lds\n"
    "s_add_u32 m0, s67, 36864\n"
    "s_add_u32 s64, s64, 9216\n"
    "buffer_load_dwordx4 v200, s[40:43], s64 offen lds\n"
    "s_add_u32 m0, s68, 36864\n"
    "s_add_u32 s65, s65, 9216\n"
    "buffer_load_dwordx4 v200, s[40:43], s65 offen lds\n"
    "s_add_u32 m0, s69, 36864\n"
    "s_add_u32 s66, s66, 9216\n"
    "buffer_load_dword v222, s[40:43], s66 offen lds\n"
    "s_add_u32 m0, s67, 55296\n"
    "s_add_u32 s64, s64, 9216\n"
    "buffer_load_dwordx4 v200, s[40:43], s64 offen lds\n"
    "s_add_u32 m0, s68, 55296\n"
    "s_add_u32 s65, s65, 9216\n"
    "buffer_load_dwordx4 v200, s[40:43], s65 offen lds\n"
    "s_add_u32 m0, s69, 55296\n"
    "s_add_u32 s66, s66, 9216\n"
    "buffer_load_dword v222, s[40:43], s66 offen lds\n"
    "s_add_u32 s63, s63, 8192\n"
    "s_mov_b32 s71, 0\n"
    "s_mov_b64 exec, 1\n"
    "L2YW%=:\n"
    "buffer_load_dword v208, v200, s[36:39], s63 offen offset:1024 sc1\n"
    "s_waitcnt vmcnt(0)\n"
    "v_cmp_eq_u32 vcc, -1, v208\n"
    "s_cbranch_vccz L2YG%=\n"
    "s_add_u32 s71, s71, 1\n"
    "s_cmp_lt_u32 s71, 0x40000\n"
    "s_cbranch_scc0 L2YG%=\n"
    "s_sleep 4\n"
    "s_branch L2YW%=\n"
    "L2YG%=:\n"
    "s_mov_b64 exec, -1\n"
    "buffer_load_dwordx4 v[108:111], v200, s[36:39], s63 offen offset:0 sc1\n"
    "buffer_load_dwordx4 v[112:115], v200, s[36:39], s63 offen offset:1024 sc1\n"
    "s_add_u32 s63, s63, 8192\n"
    "buffer_load_dwordx4 v[116:119], v200, s[36:39], s63 offen offset:0 sc1\n"
    "buffer_load_dwordx4 v[120:123], v200, s[36:39], s63 offen offset:1024 sc1\n"
    "s_add_u32 s63, s63, 8192\n"
    "buffer_load_dwordx4 v[124:127], v200, s[36:39], s63 offen offset:0 sc1\n"
    "buffer_load_dwordx4 v[128:131], v200, s[36:39], s63 offen offset:1024 sc1\n"
    "s_add_u32 s63, s63, 8192\n"
    "s_cmp_eq_u32 s58, 0\n"
    "s_cselect_b32 s75, s63, 0x40000000\n"
    "buffer_load_dwordx4 v[132:135], v200, s[36:39], s75 offen offset:0 sc1\n"
    "buffer_load_dwordx4 v[136:139], v200, s[36:39], s75 offen offset:1024 sc1\n"
    "v_and_b32 v207, 15, v206\n"
    "s_lshl_b32 s59, s77, 7\n"
    "s_lshl_b32 s60, s57, 5\n"
    "s_add_u32 s59, s59, s60\n"
    "v_add_u32 v207, s59, v207\n"
    "v_lshlrev_b32 v223, 2, v207\n"
    "s_mul_i32 s59, s58, 9216\n"
    "v_add_u32 v201, s59, v200\n"
    "v_add_u32 v202, 55296, v201\n"
    "v_add_u32 v205, 110592, v201\n"
    "s_barrier\n"
    "ds_read_b128 v[72:75], v201 offset:0\n"
    "ds_read_b128 v[76:79], v201 offset:1024\n"
    "ds_read_b128 v[80:83], v201 offset:2048\n"
    "ds_read_b128 v[84:87], v201 offset:3072\n"
    "ds_read_b128 v[88:91], v201 offset:4096\n"
    "ds_read_b128 v[92:95], v201 offset:5120\n"
    "ds_read_b128 v[96:99], v201 offset:6144\n"
    "ds_read_b128 v[100:103], v201 offset:7168\n"
    "ds_read_b128 v[104:107], v201 offset:8192\n"
    "s_barrier\n"
    "s_waitcnt vmcnt(6)\n"
    "s_mov_b32 s71, 0\n"
    "L2CKA0%=:\n"
    "v_max3_u32 v208, v108, v109, v110\n"
    "v_max3_u32 v208, v208, v111, v112\n"
    "v_max3_u32 v208, v208, v113, v114\n"
    "v_max_u32 v208, v208, v115\n"
    "v_cmp_eq_u32 vcc, -1, v208\n"
    "s_cbranch_vccz L2OKA0%=\n"
    "s_add_u32 s71, s71, 1\n"
    "s_cmp_lt_u32 s71, 0x40000\n"
    "s_cbranch_scc0 L2OKA0%=\n"
    "s_sleep 4\n"
    "s_add_u32 s59, s70, 0\n"
    "buffer_load_dwordx4 v[108:111], v200, s[36:39], s59 offen offset:0 sc1\n"
    "buffer_load_dwordx4 v[112:115], v200, s[36:39], s59 offen offset:1024 sc1\n"
    "s_waitcnt vmcnt(0)\n"
    "s_branch L2CKA0%=\n"
    "L2OKA0%=:\n"
    "s_waitcnt lgkmcnt(8)\n"
    "v_mfma_f32_16x16x32_bf16 v[0:3], v[72:75], v[108:111], 0\n"
    "v_mfma_f32_16x16x32_bf16 v[4:7], v[72:75], v[112:115], 0\n"
    "ds_read_b128 v[72:75], v201 offset:18432\n"
    "s_waitcnt lgkmcnt(8)\n"
    "v_mfma_f32_16x16x32_bf16 v[8:11], v[76:79], v[108:111], 0\n"
    "v_mfma_f32_16x16x32_bf16 v[12:15], v[76:79], v[112:115], 0\n"
    "ds_read_b128 v[76:79], v201 offset:19456\n"
    "s_waitcnt lgkmcnt(8)\n"
    "v_mfma_f32_16x16x32_bf16 v[16:19], v[80:83], v[108:111], 0\n"
    "v_mfma_f32_16x16x32_bf16 v[20:23], v[80:83], v[112:115], 0\n"
    "ds_read_b128 v[80:83], v201 offset:20480\n"
    "s_waitcnt lgkmcnt(8)\n"
    "v_mfma_f32_16x16x32_bf16 v[24:27], v[84:87], v[108:111], 0\n"
    "v_mfma_f32_16x16x32_bf16 v[28:31], v[84:87], v[112:115], 0\n"
    "ds_read_b128 v[84:87], v201 offset:21504\n"
    "s_waitcnt lgkmcnt(8)\n"
    "v_mfma_f32_16x16x32_bf16 v[32:35], v[88:91], v[108:111], 0\n"
    "v_mfma_f32_16x16x32_bf16 v[36:39], v[88:91], v[112:115], 0\n"
    "ds_read_b128 v[88:91], v201 offset:22528\n"
    "s_waitcnt lgkmcnt(8)\n"
    "v_mfma_f32_16x16x32_bf16 v[40:43], v[92:95], v[108:111], 0\n"
    "v_mfma_f32_16x16x32_bf16 v[44:47], v[92:95], v[112:115], 0\n"
    "ds_read_b128 v[92:95], v201 offset:23552\n"
    "s_waitcnt lgkmcnt(8)\n"
    "v_mfma_f32_16x16x32_bf16 v[48:51], v[96:99], v[108:111], 0\n"
    "v_mfma_f32_16x16x32_bf16 v[52:55], v[96:99], v[112:115], 0\n"
    "ds_read_b128 v[96:99], v201 offset:24576\n"
    "s_waitcnt lgkmcnt(8)\n"
    "v_mfma_f32_16x16x32_bf16 v[56:59], v[100:103], v[108:111], 0\n"
    "v_mfma_f32_16x16x32_bf16 v[60:63], v[100:103], v[112:115], 0\n"
    "ds_read_b128 v[100:103], v201 offset:25600\n"
    "s_waitcnt lgkmcnt(8)\n"
    "v_mfma_f32_16x16x32_bf16 v[64:67], v[104:107], v[108:111], 0\n"
    "v_mfma_f32_16x16x32_bf16 v[68:71], v[104:107], v[112:115], 0\n"
    "ds_read_b128 v[104:107], v201 offset:26624\n"
    "s_waitcnt vmcnt(4)\n"
    "s_mov_b32 s71, 0\n"
    "L2CKA1%=:\n"
    "v_max3_u32 v208, v116, v117, v118\n"
    "v_max3_u32 v208, v208, v119, v120\n"
    "v_max3_u32 v208, v208, v121, v122\n"
    "v_max_u32 v208, v208, v123\n"
    "v_cmp_eq_u32 vcc, -1, v208\n"
    "s_cbranch_vccz L2OKA1%=\n"
    "s_add_u32 s71, s71, 1\n"
    "s_cmp_lt_u32 s71, 0x40000\n"
    "s_cbranch_scc0 L2OKA1%=\n"
    "s_sleep 4\n"
    "s_add_u32 s59, s70, 8192\n"
    "buffer_load_dwordx4 v[116:119], v200, s[36:39], s59 offen offset:0 sc1\n"
    "buffer_load_dwordx4 v[120:123], v200, s[36:39], s59 offen offset:1024 sc1\n"
    "s_waitcnt vmcnt(0)\n"
    "s_branch L2CKA1%=\n"
    "L2OKA1%=:\n"
    "s_waitcnt lgkmcnt(8)\n"
    "v_mfma_f32_16x16x32_bf16 v[0:3], v[72:75], v[116:119], v[0:3]\n"
    "v_mfma_f32_16x16x32_bf16 v[4:7], v[72:75], v[120:123], v[4:7]\n"
    "ds_read_b128 v[72:75], v201 offset:36864\n"
    "s_waitcnt lgkmcnt(8)\n"
    "v_mfma_f32_16x16x32_bf16 v[8:11], v[76:79], v[116:119], v[8:11]\n"
    "v_mfma_f32_16x16x32_bf16 v[12:15], v[76:79], v[120:123], v[12:15]\n"
    "ds_read_b128 v[76:79], v201 offset:37888\n"
    "s_waitcnt lgkmcnt(8)\n"
    "v_mfma_f32_16x16x32_bf16 v[16:19], v[80:83], v[116:119], v[16:19]\n"
    "v_mfma_f32_16x16x32_bf16 v[20:23], v[80:83], v[120:123], v[20:23]\n"
    "ds_read_b128 v[80:83], v201 offset:38912\n"
    "s_waitcnt lgkmcnt(8)\n"
    "v_mfma_f32_16x16x32_bf16 v[24:27], v[84:87], v[116:119], v[24:27]\n"
    "v_mfma_f32_16x16x32_bf16 v[28:31], v[84:87], v[120:123], v[28:31]\n"
    "ds_read_b128 v[84:87], v201 offset:39936\n"
    "s_waitcnt lgkmcnt(8)\n"
    "v_mfma_f32_16x16x32_bf16 v[32:35], v[88:91], v[116:119], v[32:35]\n"
    "v_mfma_f32_16x16x32_bf16 v[36:39], v[88:91], v[120:123], v[36:39]\n"
    "ds_read_b128 v[88:91], v201 offset:40960\n"
    "s_waitcnt lgkmcnt(8)\n"
    "v_mfma_f32_16x16x32_bf16 v[40:43], v[92:95], v[116:119], v[40:43]\n"
    "v_mfma_f32_16x16x32_bf16 v[44:47], v[92:95], v[120:123], v[44:47]\n"
    "ds_read_b128 v[92:95], v201 offset:41984\n"
    "s_waitcnt lgkmcnt(8)\n"
    "v_mfma_f32_16x16x32_bf16 v[48:51], v[96:99], v[116:119], v[48:51]\n"
    "v_mfma_f32_16x16x32_bf16 v[52:55], v[96:99], v[120:123], v[52:55]\n"
    "ds_read_b128 v[96:99], v201 offset:43008\n"
    "s_waitcnt lgkmcnt(8)\n"
    "v_mfma_f32_16x16x32_bf16 v[56:59], v[100:103], v[116:119], v[56:59]\n"
    "v_mfma_f32_16x16x32_bf16 v[60:63], v[100:103], v[120:123], v[60:63]\n"
    "ds_read_b128 v[100:103], v201 offset:44032\n"
    "s_waitcnt lgkmcnt(8)\n"
    "v_mfma_f32_16x16x32_bf16 v[64:67], v[104:107], v[116:119], v[64:67]\n"
    "v_mfma_f32_16x16x32_bf16 v[68:71], v[104:107], v[120:123], v[68:71]\n"
    "ds_read_b128 v[104:107], v201 offset:45056\n"
    "s_barrier\n"
    "s_waitcnt vmcnt(2)\n"
    "s_mov_b32 s71, 0\n"
    "L2CKA2%=:\n"
    "v_max3_u32 v208, v124, v125, v126\n"
    "v_max3_u32 v208, v208, v127, v128\n"
    "v_max3_u32 v208, v208, v129, v130\n"
    "v_max_u32 v208, v208, v131\n"
    "v_cmp_eq_u32 vcc, -1, v208\n"
    "s_cbranch_vccz L2OKA2%=\n"
    "s_add_u32 s71, s71, 1\n"
    "s_cmp_lt_u32 s71, 0x40000\n"
    "s_cbranch_scc0 L2OKA2%=\n"
    "s_sleep 4\n"
    "s_add_u32 s59, s70, 16384\n"
    "buffer_load_dwordx4 v[124:127], v200, s[36:39], s59 offen offset:0 sc1\n"
    "buffer_load_dwordx4 v[128:131], v200, s[36:39], s59 offen offset:1024 sc1\n"
    "s_waitcnt vmcnt(0)\n"
    "s_branch L2CKA2%=\n"
    "L2OKA2%=:\n"
    "s_waitcnt lgkmcnt(8)\n"
    "v_mfma_f32_16x16x32_bf16 v[0:3], v[72:75], v[124:127], v[0:3]\n"
    "v_mfma_f32_16x16x32_bf16 v[4:7], v[72:75], v[128:131], v[4:7]\n"
    "ds_read_b128 v[72:75], v202 offset:0\n"
    "s_waitcnt lgkmcnt(8)\n"
    "v_mfma_f32_16x16x32_bf16 v[8:11], v[76:79], v[124:127], v[8:11]\n"
    "v_mfma_f32_16x16x32_bf16 v[12:15], v[76:79], v[128:131], v[12:15]\n"
    "ds_read_b128 v[76:79], v202 offset:1024\n"
    "s_waitcnt lgkmcnt(8)\n"
    "v_mfma_f32_16x16x32_bf16 v[16:19], v[80:83], v[124:127], v[16:19]\n"
    "v_mfma_f32_16x16x32_bf16 v[20:23], v[80:83], v[128:131], v[20:23]\n"
    "ds_read_b128 v[80:83], v202 offset:2048\n"
    "s_waitcnt lgkmcnt(8)\n"
    "v_mfma_f32_16x16x32_bf16 v[24:27], v[84:87], v[124:127], v[24:27]\n"
    "v_mfma_f32_16x16x32_bf16 v[28:31], v[84:87], v[128:131], v[28:31]\n"
    "ds_read_b128 v[84:87], v202 offset:3072\n"
    "s_waitcnt lgkmcnt(8)\n"
    "v_mfma_f32_16x16x32_bf16 v[32:35], v[88:91], v[124:127], v[32:35]\n"
    "v_mfma_f32_16x16x32_bf16 v[36:39], v[88:91], v[128:131], v[36:39]\n"
    "ds_read_b128 v[88:91], v202 offset:4096\n"
    "s_waitcnt lgkmcnt(8)\n"
    "v_mfma_f32_16x16x32_bf16 v[40:43], v[92:95], v[124:127], v[40:43]\n"
    "v_mfma_f32_16x16x32_bf16 v[44:47], v[92:95], v[128:131], v[44:47]\n"
    "ds_read_b128 v[92:95], v202 offset:5120\n"
    "s_waitcnt lgkmcnt(8)\n"
    "v_mfma_f32_16x16x32_bf16 v[48:51], v[96:99], v[124:127], v[48:51]\n"
    "v_mfma_f32_16x16x32_bf16 v[52:55], v[96:99], v[128:131], v[52:55]\n"
    "ds_read_b128 v[96:99], v202 offset:6144\n"
    "s_waitcnt lgkmcnt(8)\n"
    "v_mfma_f32_16x16x32_bf16 v[56:59], v[100:103], v[124:127], v[56:59]\n"
    "v_mfma_f32_16x16x32_bf16 v[60:63], v[100:103], v[128:131], v[60:63]\n"
    "ds_read_b128 v[100:103], v202 offset:7168\n"
    "s_waitcnt lgkmcnt(8)\n"
    "v_mfma_f32_16x16x32_bf16 v[64:67], v[104:107], v[124:127], v[64:67]\n"
    "v_mfma_f32_16x16x32_bf16 v[68:71], v[104:107], v[128:131], v[68:71]\n"
    "ds_read_b128 v[104:107], v202 offset:8192\n"
    "s_waitcnt vmcnt(0)\n"
    "s_mov_b32 s71, 0\n"
    "L2CKA3%=:\n"
    "v_max3_u32 v208, v132, v133, v134\n"
    "v_max3_u32 v208, v208, v135, v136\n"
    "v_max3_u32 v208, v208, v137, v138\n"
    "v_max_u32 v208, v208, v139\n"
    "v_cmp_eq_u32 vcc, -1, v208\n"
    "s_cbranch_vccz L2OKA3%=\n"
    "s_add_u32 s71, s71, 1\n"
    "s_cmp_lt_u32 s71, 0x40000\n"
    "s_cbranch_scc0 L2OKA3%=\n"
    "s_sleep 4\n"
    "s_add_u32 s59, s70, 24576\n"
    "buffer_load_dwordx4 v[132:135], v200, s[36:39], s59 offen offset:0 sc1\n"
    "buffer_load_dwordx4 v[136:139], v200, s[36:39], s59 offen offset:1024 sc1\n"
    "s_waitcnt vmcnt(0)\n"
    "s_branch L2CKA3%=\n"
    "L2OKA3%=:\n"
    "s_waitcnt lgkmcnt(8)\n"
    "v_mfma_f32_16x16x32_bf16 v[0:3], v[72:75], v[132:135], v[0:3]\n"
    "v_mfma_f32_16x16x32_bf16 v[4:7], v[72:75], v[136:139], v[4:7]\n"
    "s_waitcnt lgkmcnt(7)\n"
    "v_mfma_f32_16x16x32_bf16 v[8:11], v[76:79], v[132:135], v[8:11]\n"
    "v_mfma_f32_16x16x32_bf16 v[12:15], v[76:79], v[136:139], v[12:15]\n"
    "s_waitcnt lgkmcnt(6)\n"
    "v_mfma_f32_16x16x32_bf16 v[16:19], v[80:83], v[132:135], v[16:19]\n"
    "v_mfma_f32_16x16x32_bf16 v[20:23], v[80:83], v[136:139], v[20:23]\n"
    "s_waitcnt lgkmcnt(5)\n"
    "v_mfma_f32_16x16x32_bf16 v[24:27], v[84:87], v[132:135], v[24:27]\n"
    "v_mfma_f32_16x16x32_bf16 v[28:31], v[84:87], v[136:139], v[28:31]\n"
    "s_waitcnt lgkmcnt(4)\n"
    "v_mfma_f32_16x16x32_bf16 v[32:35], v[88:91], v[132:135], v[32:35]\n"
    "v_mfma_f32_16x16x32_bf16 v[36:39], v[88:91], v[136:139], v[36:39]\n"
    "s_waitcnt lgkmcnt(3)\n"
    "v_mfma_f32_16x16x32_bf16 v[40:43], v[92:95], v[132:135], v[40:43]\n"
    "v_mfma_f32_16x16x32_bf16 v[44:47], v[92:95], v[136:139], v[44:47]\n"
    "s_waitcnt lgkmcnt(2)\n"
    "v_mfma_f32_16x16x32_bf16 v[48:51], v[96:99], v[132:135], v[48:51]\n"
    "v_mfma_f32_16x16x32_bf16 v[52:55], v[96:99], v[136:139], v[52:55]\n"
    "s_waitcnt lgkmcnt(1)\n"
    "v_mfma_f32_16x16x32_bf16 v[56:59], v[100:103], v[132:135], v[56:59]\n"
    "v_mfma_f32_16x16x32_bf16 v[60:63], v[100:103], v[136:139], v[60:63]\n"
    "s_waitcnt lgkmcnt(0)\n"
    "v_mfma_f32_16x16x32_bf16 v[64:67], v[104:107], v[132:135], v[64:67]\n"
    "v_mfma_f32_16x16x32_bf16 v[68:71], v[104:107], v[136:139], v[68:71]\n"
    "L2DONE%=:\n"
    "s_waitcnt vmcnt(0) lgkmcnt(0)\n"
    "s_nop 7\n"
    "s_mul_i32 s59, s57, 18432\n"
    "s_add_u32 s59, s59, 73728\n"
    "v_add_u32 v201, s59, v200\n"
    "s_cmp_eq_u32 s58, 0\n"
    "s_cbranch_scc0 L2RW1%=\n"
    "ds_write_b128 v201, v[40:43] offset:10240\n"
    "ds_write_b128 v201, v[44:47] offset:11264\n"
    "ds_write_b128 v201, v[48:51] offset:12288\n"
    "ds_write_b128 v201, v[52:55] offset:13312\n"
    "ds_write_b128 v201, v[56:59] offset:14336\n"
    "ds_write_b128 v201, v[60:63] offset:15360\n"
    "ds_write_b128 v201, v[64:67] offset:16384\n"
    "ds_write_b128 v201, v[68:71] offset:17408\n"
    "s_branch L2RWD%=\n"
    "L2RW1%=:\n"
    "ds_write_b128 v201, v[0:3] offset:0\n"
    "ds_write_b128 v201, v[4:7] offset:1024\n"
    "ds_write_b128 v201, v[8:11] offset:2048\n"
    "ds_write_b128 v201, v[12:15] offset:3072\n"
    "ds_write_b128 v201, v[16:19] offset:4096\n"
    "ds_write_b128 v201, v[20:23] offset:5120\n"
    "ds_write_b128 v201, v[24:27] offset:6144\n"
    "ds_write_b128 v201, v[28:31] offset:7168\n"
    "ds_write_b128 v201, v[32:35] offset:8192\n"
    "ds_write_b128 v201, v[36:39] offset:9216\n"
    "L2RWD%=:\n"
    "s_waitcnt lgkmcnt(0)\n"
    "s_barrier\n"
    "s_cmp_eq_u32 s58, 0\n"
    "s_cbranch_scc0 L2K1%=\n"
    "ds_read_b128 v[108:111], v201 offset:0\n"
    "ds_read_b128 v[112:115], v201 offset:1024\n"
    "ds_read_b128 v[116:119], v201 offset:2048\n"
    "ds_read_b128 v[120:123], v201 offset:3072\n"
    "ds_read_b128 v[124:127], v201 offset:4096\n"
    "ds_read_b128 v[128:131], v201 offset:5120\n"
    "ds_read_b128 v[132:135], v201 offset:6144\n"
    "ds_read_b128 v[136:139], v201 offset:7168\n"
    "ds_read_b128 v[140:143], v201 offset:8192\n"
    "ds_read_b128 v[144:147], v201 offset:9216\n"
    "s_waitcnt lgkmcnt(0)\n"
    "v_add_f32 v0, v0, v108\n"
    "v_add_f32 v1, v1, v109\n"
    "v_add_f32 v2, v2, v110\n"
    "v_add_f32 v3, v3, v111\n"
    "v_add_f32 v4, v4, v112\n"
    "v_add_f32 v5, v5, v113\n"
    "v_add_f32 v6, v6, v114\n"
    "v_add_f32 v7, v7, v115\n"
    "v_add_f32 v8, v8, v116\n"
    "v_add_f32 v9, v9, v117\n"
    "v_add_f32 v10, v10, v118\n"
    "v_add_f32 v11, v11, v119\n"
    "v_add_f32 v12, v12, v120\n"
    "v_add_f32 v13, v13, v121\n"
    "v_add_f32 v14, v14, v122\n"
    "v_add_f32 v15, v15, v123\n"
    "v_add_f32 v16, v16, v124\n"
    "v_add_f32 v17, v17, v125\n"
    "v_add_f32 v18, v18, v126\n"
    "v_add_f32 v19, v19, v127\n"
    "v_add_f32 v20, v20, v128\n"
    "v_add_f32 v21, v21, v129\n"
    "v_add_f32 v22, v22, v130\n"
    "v_add_f32 v23, v23, v131\n"
    "v_add_f32 v24, v24, v132\n"
    "v_add_f32 v25, v25, v133\n"
    "v_add_f32 v26, v26, v134\n"
    "v_add_f32 v27, v27, v135\n"
    "v_add_f32 v28, v28, v136\n"
    "v_add_f32 v29, v29, v137\n"
    "v_add_f32 v30, v30, v138\n"
    "v_add_f32 v31, v31, v139\n"
    "v_add_f32 v32, v32, v140\n"
    "v_add_f32 v33, v33, v141\n"
    "v_add_f32 v34, v34, v142\n"
    "v_add_f32 v35, v35, v143\n"
    "v_add_f32 v36, v36, v144\n"
    "v_add_f32 v37, v37, v145\n"
    "v_add_f32 v38, v38, v146\n"
    "v_add_f32 v39, v39, v147\n"
    "s_cmp_eq_u32 s57, s62\n"
    "s_cbranch_scc1 L2OWN0%=\n"
    "s_lshl_b32 s59, s77, 2\n"
    "s_add_u32 s59, s59, s78\n"
    "s_lshl_b32 s59, s59, 2\n"
    "s_add_u32 s59, s59, s57\n"
    "s_mul_i32 s59, s59, 3\n"
    "s_sub_u32 s60, s62, s57\n"
    "s_sub_u32 s60, s60, 1\n"
    "s_and_b32 s60, s60, 3\n"
    "s_add_u32 s59, s59, s60\n"
    "s_mul_i32 s59, s59, 9216\n"
    "v_cvt_pk_bf16_f32 v108, v0, v1\n"
    "v_cvt_pk_bf16_f32 v109, v2, v3\n"
    "v_cvt_pk_bf16_f32 v110, v4, v5\n"
    "v_cvt_pk_bf16_f32 v111, v6, v7\n"
    "v_min_u32 v108, 0xfffffffe, v108\n"
    "v_min_u32 v109, 0xfffffffe, v109\n"
    "v_min_u32 v110, 0xfffffffe, v110\n"
    "v_min_u32 v111, 0xfffffffe, v111\n"
    "s_add_u32 s60, s59, 0\n"
    "buffer_store_dwordx4 v[108:111], v200, s[44:47], s60 offen sc1\n"
    "v_cvt_pk_bf16_f32 v112, v8, v9\n"
    "v_cvt_pk_bf16_f32 v113, v10, v11\n"
    "v_cvt_pk_bf16_f32 v114, v12, v13\n"
    "v_cvt_pk_bf16_f32 v115, v14, v15\n"
    "v_min_u32 v112, 0xfffffffe, v112\n"
    "v_min_u32 v113, 0xfffffffe, v113\n"
    "v_min_u32 v114, 0xfffffffe, v114\n"
    "v_min_u32 v115, 0xfffffffe, v115\n"
    "s_add_u32 s60, s59, 1024\n"
    "buffer_store_dwordx4 v[112:115], v200, s[44:47], s60 offen sc1\n"
    "v_cvt_pk_bf16_f32 v116, v16, v17\n"
    "v_cvt_pk_bf16_f32 v117, v18, v19\n"
    "v_cvt_pk_bf16_f32 v118, v20, v21\n"
    "v_cvt_pk_bf16_f32 v119, v22, v23\n"
    "v_min_u32 v116, 0xfffffffe, v116\n"
    "v_min_u32 v117, 0xfffffffe, v117\n"
    "v_min_u32 v118, 0xfffffffe, v118\n"
    "v_min_u32 v119, 0xfffffffe, v119\n"
    "s_add_u32 s60, s59, 2048\n"
    "buffer_store_dwordx4 v[116:119], v200, s[44:47], s60 offen sc1\n"
    "v_cvt_pk_bf16_f32 v120, v24, v25\n"
    "v_cvt_pk_bf16_f32 v121, v26, v27\n"
    "v_cvt_pk_bf16_f32 v122, v28, v29\n"
    "v_cvt_pk_bf16_f32 v123, v30, v31\n"
    "v_min_u32 v120, 0xfffffffe, v120\n"
    "v_min_u32 v121, 0xfffffffe, v121\n"
    "v_min_u32 v122, 0xfffffffe, v122\n"
    "v_min_u32 v123, 0xfffffffe, v123\n"
    "s_add_u32 s60, s59, 3072\n"
    "buffer_store_dwordx4 v[120:123], v200, s[44:47], s60 offen sc1\n"
    "v_cvt_pk_bf16_f32 v124, v32, v33\n"
    "v_cvt_pk_bf16_f32 v125, v34, v35\n"
    "v_cvt_pk_bf16_f32 v126, v36, v37\n"
    "v_cvt_pk_bf16_f32 v127, v38, v39\n"
    "v_min_u32 v124, 0xfffffffe, v124\n"
    "v_min_u32 v125, 0xfffffffe, v125\n"
    "v_min_u32 v126, 0xfffffffe, v126\n"
    "v_min_u32 v127, 0xfffffffe, v127\n"
    "s_add_u32 s60, s59, 4096\n"
    "buffer_store_dwordx4 v[124:127], v200, s[44:47], s60 offen sc1\n"
    "s_branch L2END%=\n"
    "L2OWN0%=:\n"
    "s_lshl_b32 s59, s77, 2\n"
    "s_add_u32 s59, s59, s78\n"
    "s_lshl_b32 s59, s59, 2\n"
    "s_add_u32 s59, s59, s57\n"
    "s_mul_i32 s79, s59, 27648\n"
    "s_add_u32 s64, s79, 0\n"
    "s_add_u32 s65, s79, 9216\n"
    "s_add_u32 s66, s79, 18432\n"
    "s_mov_b32 s61, 0\n"
    "s_mov_b64 exec, 1\n"
    "L2HINT0%=:\n"
    "s_add_u32 s60, s64, 4096\n"
    "buffer_load_dword v108, v200, s[44:47], s60 offen sc1\n"
    "s_add_u32 s60, s65, 4096\n"
    "buffer_load_dword v109, v200, s[44:47], s60 offen sc1\n"
    "s_add_u32 s60, s66, 4096\n"
    "buffer_load_dword v110, v200, s[44:47], s60 offen sc1\n"
    "s_waitcnt vmcnt(0)\n"
    "v_max3_u32 v206, v108, v109, v110\n"
    "v_cmp_eq_u32 vcc, -1, v206\n"
    "s_cbranch_vccz L2HOK0%=\n"
    "s_add_u32 s61, s61, 1\n"
    "s_cmp_lt_u32 s61, 0x100000\n"
    "s_cbranch_scc0 L2HOK0%=\n"
    "s_sleep 4\n"
    "s_branch L2HINT0%=\n"
    "L2HOK0%=:\n"
    "s_mov_b64 exec, -1\n"
    "L2POLL0%=:\n"
    "s_add_u32 s60, s64, 0\n"
    "buffer_load_dwordx4 v[108:111], v200, s[44:47], s60 offen sc1\n"
    "s_add_u32 s60, s64, 1024\n"
    "buffer_load_dwordx4 v[112:115], v200, s[44:47], s60 offen sc1\n"
    "s_add_u32 s60, s64, 2048\n"
    "buffer_load_dwordx4 v[116:119], v200, s[44:47], s60 offen sc1\n"
    "s_add_u32 s60, s64, 3072\n"
    "buffer_load_dwordx4 v[120:123], v200, s[44:47], s60 offen sc1\n"
    "s_add_u32 s60, s64, 4096\n"
    "buffer_load_dwordx4 v[124:127], v200, s[44:47], s60 offen sc1\n"
    "s_add_u32 s60, s65, 0\n"
    "buffer_load_dwordx4 v[128:131], v200, s[44:47], s60 offen sc1\n"
    "s_add_u32 s60, s65, 1024\n"
    "buffer_load_dwordx4 v[132:135], v200, s[44:47], s60 offen sc1\n"
    "s_add_u32 s60, s65, 2048\n"
    "buffer_load_dwordx4 v[136:139], v200, s[44:47], s60 offen sc1\n"
    "s_add_u32 s60, s65, 3072\n"
    "buffer_load_dwordx4 v[140:143], v200, s[44:47], s60 offen sc1\n"
    "s_add_u32 s60, s65, 4096\n"
    "buffer_load_dwordx4 v[144:147], v200, s[44:47], s60 offen sc1\n"
    "s_add_u32 s60, s66, 0\n"
    "buffer_load_dwordx4 v[148:151], v200, s[44:47], s60 offen sc1\n"
    "s_add_u32 s60, s66, 1024\n"
    "buffer_load_dwordx4 v[152:155], v200, s[44:47], s60 offen sc1\n"
    "s_add_u32 s60, s66, 2048\n"
    "buffer_load_dwordx4 v[156:159], v200, s[44:47], s60 offen sc1\n"
    "s_add_u32 s60, s66, 3072\n"
    "buffer_load_dwordx4 v[160:163], v200, s[44:47], s60 offen sc1\n"
    "s_add_u32 s60, s66, 4096\n"
    "buffer_load_dwordx4 v[164:167], v200, s[44:47], s60 offen sc1\n"
    "s_waitcnt vmcnt(0)\n"
    "v_max_u32 v206, v108, v109\n"
    "v_max3_u32 v206, v206, v110, v111\n"
    "v_max3_u32 v206, v206, v112, v113\n"
    "v_max3_u32 v206, v206, v114, v115\n"
    "v_max3_u32 v206, v206, v116, v117\n"
    "v_max3_u32 v206, v206, v118, v119\n"
    "v_max3_u32 v206, v206, v120, v121\n"
    "v_max3_u32 v206, v206, v122, v123\n"
    "v_max3_u32 v206, v206, v124, v125\n"
    "v_max3_u32 v206, v206, v126, v127\n"
    "v_max3_u32 v206, v206, v128, v129\n"
    "v_max3_u32 v206, v206, v130, v131\n"
    "v_max3_u32 v206, v206, v132, v133\n"
    "v_max3_u32 v206, v206, v134, v135\n"
    "v_max3_u32 v206, v206, v136, v137\n"
    "v_max3_u32 v206, v206, v138, v139\n"
    "v_max3_u32 v206, v206, v140, v141\n"
    "v_max3_u32 v206, v206, v142, v143\n"
    "v_max3_u32 v206, v206, v144, v145\n"
    "v_max3_u32 v206, v206, v146, v147\n"
    "v_max3_u32 v206, v206, v148, v149\n"
    "v_max3_u32 v206, v206, v150, v151\n"
    "v_max3_u32 v206, v206, v152, v153\n"
    "v_max3_u32 v206, v206, v154, v155\n"
    "v_max3_u32 v206, v206, v156, v157\n"
    "v_max3_u32 v206, v206, v158, v159\n"
    "v_max3_u32 v206, v206, v160, v161\n"
    "v_max3_u32 v206, v206, v162, v163\n"
    "v_max3_u32 v206, v206, v164, v165\n"
    "v_max3_u32 v206, v206, v166, v167\n"
    "v_cmp_eq_u32 vcc, -1, v206\n"
    "s_cbranch_vccz L2GOT0%=\n"
    "s_add_u32 s61, s61, 1\n"
    "s_cmp_lt_u32 s61, 0x100000\n"
    "s_cbranch_scc0 L2GOT0%=\n"
    "s_sleep 4\n"
    "s_branch L2POLL0%=\n"
    "L2GOT0%=:\n"
    "v_lshlrev_b32 v207, 16, v108\n"
    "v_and_b32 v208, 0xffff0000, v108\n"
    "v_add_f32 v0, v0, v207\n"
    "v_add_f32 v1, v1, v208\n"
    "v_lshlrev_b32 v207, 16, v109\n"
    "v_and_b32 v208, 0xffff0000, v109\n"
    "v_add_f32 v2, v2, v207\n"
    "v_add_f32 v3, v3, v208\n"
    "v_lshlrev_b32 v207, 16, v110\n"
    "v_and_b32 v208, 0xffff0000, v110\n"
    "v_add_f32 v4, v4, v207\n"
    "v_add_f32 v5, v5, v208\n"
    "v_lshlrev_b32 v207, 16, v111\n"
    "v_and_b32 v208, 0xffff0000, v111\n"
    "v_add_f32 v6, v6, v207\n"
    "v_add_f32 v7, v7, v208\n"
    "v_lshlrev_b32 v207, 16, v112\n"
    "v_and_b32 v208, 0xffff0000, v112\n"
    "v_add_f32 v8, v8, v207\n"
    "v_add_f32 v9, v9, v208\n"
    "v_lshlrev_b32 v207, 16, v113\n"
    "v_and_b32 v208, 0xffff0000, v113\n"
    "v_add_f32 v10, v10, v207\n"
    "v_add_f32 v11, v11, v208\n"
    "v_lshlrev_b32 v207, 16, v114\n"
    "v_and_b32 v208, 0xffff0000, v114\n"
    "v_add_f32 v12, v12, v207\n"
    "v_add_f32 v13, v13, v208\n"
    "v_lshlrev_b32 v207, 16, v115\n"
    "v_and_b32 v208, 0xffff0000, v115\n"
    "v_add_f32 v14, v14, v207\n"
    "v_add_f32 v15, v15, v208\n"
    "v_lshlrev_b32 v207, 16, v116\n"
    "v_and_b32 v208, 0xffff0000, v116\n"
    "v_add_f32 v16, v16, v207\n"
    "v_add_f32 v17, v17, v208\n"
    "v_lshlrev_b32 v207, 16, v117\n"
    "v_and_b32 v208, 0xffff0000, v117\n"
    "v_add_f32 v18, v18, v207\n"
    "v_add_f32 v19, v19, v208\n"
    "v_lshlrev_b32 v207, 16, v118\n"
    "v_and_b32 v208, 0xffff0000, v118\n"
    "v_add_f32 v20, v20, v207\n"
    "v_add_f32 v21, v21, v208\n"
    "v_lshlrev_b32 v207, 16, v119\n"
    "v_and_b32 v208, 0xffff0000, v119\n"
    "v_add_f32 v22, v22, v207\n"
    "v_add_f32 v23, v23, v208\n"
    "v_lshlrev_b32 v207, 16, v120\n"
    "v_and_b32 v208, 0xffff0000, v120\n"
    "v_add_f32 v24, v24, v207\n"
    "v_add_f32 v25, v25, v208\n"
    "v_lshlrev_b32 v207, 16, v121\n"
    "v_and_b32 v208, 0xffff0000, v121\n"
    "v_add_f32 v26, v26, v207\n"
    "v_add_f32 v27, v27, v208\n"
    "v_lshlrev_b32 v207, 16, v122\n"
    "v_and_b32 v208, 0xffff0000, v122\n"
    "v_add_f32 v28, v28, v207\n"
    "v_add_f32 v29, v29, v208\n"
    "v_lshlrev_b32 v207, 16, v123\n"
    "v_and_b32 v208, 0xffff0000, v123\n"
    "v_add_f32 v30, v30, v207\n"
    "v_add_f32 v31, v31, v208\n"
    "v_lshlrev_b32 v207, 16, v124\n"
    "v_and_b32 v208, 0xffff0000, v124\n"
    "v_add_f32 v32, v32, v207\n"
    "v_add_f32 v33, v33, v208\n"
    "v_lshlrev_b32 v207, 16, v125\n"
    "v_and_b32 v208, 0xffff0000, v125\n"
    "v_add_f32 v34, v34, v207\n"
    "v_add_f32 v35, v35, v208\n"
    "v_lshlrev_b32 v207, 16, v126\n"
    "v_and_b32 v208, 0xffff0000, v126\n"
    "v_add_f32 v36, v36, v207\n"
    "v_add_f32 v37, v37, v208\n"
    "v_lshlrev_b32 v207, 16, v127\n"
    "v_and_b32 v208, 0xffff0000, v127\n"
    "v_add_f32 v38, v38, v207\n"
    "v_add_f32 v39, v39, v208\n"
    "v_lshlrev_b32 v207, 16, v128\n"
    "v_and_b32 v208, 0xffff0000, v128\n"
    "v_add_f32 v0, v0, v207\n"
    "v_add_f32 v1, v1, v208\n"
    "v_lshlrev_b32 v207, 16, v129\n"
    "v_and_b32 v208, 0xffff0000, v129\n"
    "v_add_f32 v2, v2, v207\n"
    "v_add_f32 v3, v3, v208\n"
    "v_lshlrev_b32 v207, 16, v130\n"
    "v_and_b32 v208, 0xffff0000, v130\n"
    "v_add_f32 v4, v4, v207\n"
    "v_add_f32 v5, v5, v208\n"
    "v_lshlrev_b32 v207, 16, v131\n"
    "v_and_b32 v208, 0xffff0000, v131\n"
    "v_add_f32 v6, v6, v207\n"
    "v_add_f32 v7, v7, v208\n"
    "v_lshlrev_b32 v207, 16, v132\n"
    "v_and_b32 v208, 0xffff0000, v132\n"
    "v_add_f32 v8, v8, v207\n"
    "v_add_f32 v9, v9, v208\n"
    "v_lshlrev_b32 v207, 16, v133\n"
    "v_and_b32 v208, 0xffff0000, v133\n"
    "v_add_f32 v10, v10, v207\n"
    "v_add_f32 v11, v11, v208\n"
    "v_lshlrev_b32 v207, 16, v134\n"
    "v_and_b32 v208, 0xffff0000, v134\n"
    "v_add_f32 v12, v12, v207\n"
    "v_add_f32 v13, v13, v208\n"
    "v_lshlrev_b32 v207, 16, v135\n"
    "v_and_b32 v208, 0xffff0000, v135\n"
    "v_add_f32 v14, v14, v207\n"
    "v_add_f32 v15, v15, v208\n"
    "v_lshlrev_b32 v207, 16, v136\n"
    "v_and_b32 v208, 0xffff0000, v136\n"
    "v_add_f32 v16, v16, v207\n"
    "v_add_f32 v17, v17, v208\n"
    "v_lshlrev_b32 v207, 16, v137\n"
    "v_and_b32 v208, 0xffff0000, v137\n"
    "v_add_f32 v18, v18, v207\n"
    "v_add_f32 v19, v19, v208\n"
    "v_lshlrev_b32 v207, 16, v138\n"
    "v_and_b32 v208, 0xffff0000, v138\n"
    "v_add_f32 v20, v20, v207\n"
    "v_add_f32 v21, v21, v208\n"
    "v_lshlrev_b32 v207, 16, v139\n"
    "v_and_b32 v208, 0xffff0000, v139\n"
    "v_add_f32 v22, v22, v207\n"
    "v_add_f32 v23, v23, v208\n"
    "v_lshlrev_b32 v207, 16, v140\n"
    "v_and_b32 v208, 0xffff0000, v140\n"
    "v_add_f32 v24, v24, v207\n"
    "v_add_f32 v25, v25, v208\n"
    "v_lshlrev_b32 v207, 16, v141\n"
    "v_and_b32 v208, 0xffff0000, v141\n"
    "v_add_f32 v26, v26, v207\n"
    "v_add_f32 v27, v27, v208\n"
    "v_lshlrev_b32 v207, 16, v142\n"
    "v_and_b32 v208, 0xffff0000, v142\n"
    "v_add_f32 v28, v28, v207\n"
    "v_add_f32 v29, v29, v208\n"
    "v_lshlrev_b32 v207, 16, v143\n"
    "v_and_b32 v208, 0xffff0000, v143\n"
    "v_add_f32 v30, v30, v207\n"
    "v_add_f32 v31, v31, v208\n"
    "v_lshlrev_b32 v207, 16, v144\n"
    "v_and_b32 v208, 0xffff0000, v144\n"
    "v_add_f32 v32, v32, v207\n"
    "v_add_f32 v33, v33, v208\n"
    "v_lshlrev_b32 v207, 16, v145\n"
    "v_and_b32 v208, 0xffff0000, v145\n"
    "v_add_f32 v34, v34, v207\n"
    "v_add_f32 v35, v35, v208\n"
    "v_lshlrev_b32 v207, 16, v146\n"
    "v_and_b32 v208, 0xffff0000, v146\n"
    "v_add_f32 v36, v36, v207\n"
    "v_add_f32 v37, v37, v208\n"
    "v_lshlrev_b32 v207, 16, v147\n"
    "v_and_b32 v208, 0xffff0000, v147\n"
    "v_add_f32 v38, v38, v207\n"
    "v_add_f32 v39, v39, v208\n"
    "v_lshlrev_b32 v207, 16, v148\n"
    "v_and_b32 v208, 0xffff0000, v148\n"
    "v_add_f32 v0, v0, v207\n"
    "v_add_f32 v1, v1, v208\n"
    "v_lshlrev_b32 v207, 16, v149\n"
    "v_and_b32 v208, 0xffff0000, v149\n"
    "v_add_f32 v2, v2, v207\n"
    "v_add_f32 v3, v3, v208\n"
    "v_lshlrev_b32 v207, 16, v150\n"
    "v_and_b32 v208, 0xffff0000, v150\n"
    "v_add_f32 v4, v4, v207\n"
    "v_add_f32 v5, v5, v208\n"
    "v_lshlrev_b32 v207, 16, v151\n"
    "v_and_b32 v208, 0xffff0000, v151\n"
    "v_add_f32 v6, v6, v207\n"
    "v_add_f32 v7, v7, v208\n"
    "v_lshlrev_b32 v207, 16, v152\n"
    "v_and_b32 v208, 0xffff0000, v152\n"
    "v_add_f32 v8, v8, v207\n"
    "v_add_f32 v9, v9, v208\n"
    "v_lshlrev_b32 v207, 16, v153\n"
    "v_and_b32 v208, 0xffff0000, v153\n"
    "v_add_f32 v10, v10, v207\n"
    "v_add_f32 v11, v11, v208\n"
    "v_lshlrev_b32 v207, 16, v154\n"
    "v_and_b32 v208, 0xffff0000, v154\n"
    "v_add_f32 v12, v12, v207\n"
    "v_add_f32 v13, v13, v208\n"
    "v_lshlrev_b32 v207, 16, v155\n"
    "v_and_b32 v208, 0xffff0000, v155\n"
    "v_add_f32 v14, v14, v207\n"
    "v_add_f32 v15, v15, v208\n"
    "v_lshlrev_b32 v207, 16, v156\n"
    "v_and_b32 v208, 0xffff0000, v156\n"
    "v_add_f32 v16, v16, v207\n"
    "v_add_f32 v17, v17, v208\n"
    "v_lshlrev_b32 v207, 16, v157\n"
    "v_and_b32 v208, 0xffff0000, v157\n"
    "v_add_f32 v18, v18, v207\n"
    "v_add_f32 v19, v19, v208\n"
    "v_lshlrev_b32 v207, 16, v158\n"
    "v_and_b32 v208, 0xffff0000, v158\n"
    "v_add_f32 v20, v20, v207\n"
    "v_add_f32 v21, v21, v208\n"
    "v_lshlrev_b32 v207, 16, v159\n"
    "v_and_b32 v208, 0xffff0000, v159\n"
    "v_add_f32 v22, v22, v207\n"
    "v_add_f32 v23, v23, v208\n"
    "v_lshlrev_b32 v207, 16, v160\n"
    "v_and_b32 v208, 0xffff0000, v160\n"
    "v_add_f32 v24, v24, v207\n"
    "v_add_f32 v25, v25, v208\n"
    "v_lshlrev_b32 v207, 16, v161\n"
    "v_and_b32 v208, 0xffff0000, v161\n"
    "v_add_f32 v26, v26, v207\n"
    "v_add_f32 v27, v27, v208\n"
    "v_lshlrev_b32 v207, 16, v162\n"
    "v_and_b32 v208, 0xffff0000, v162\n"
    "v_add_f32 v28, v28, v207\n"
    "v_add_f32 v29, v29, v208\n"
    "v_lshlrev_b32 v207, 16, v163\n"
    "v_and_b32 v208, 0xffff0000, v163\n"
    "v_add_f32 v30, v30, v207\n"
    "v_add_f32 v31, v31, v208\n"
    "v_lshlrev_b32 v207, 16, v164\n"
    "v_and_b32 v208, 0xffff0000, v164\n"
    "v_add_f32 v32, v32, v207\n"
    "v_add_f32 v33, v33, v208\n"
    "v_lshlrev_b32 v207, 16, v165\n"
    "v_and_b32 v208, 0xffff0000, v165\n"
    "v_add_f32 v34, v34, v207\n"
    "v_add_f32 v35, v35, v208\n"
    "v_lshlrev_b32 v207, 16, v166\n"
    "v_and_b32 v208, 0xffff0000, v166\n"
    "v_add_f32 v36, v36, v207\n"
    "v_add_f32 v37, v37, v208\n"
    "v_lshlrev_b32 v207, 16, v167\n"
    "v_and_b32 v208, 0xffff0000, v167\n"
    "v_add_f32 v38, v38, v207\n"
    "v_add_f32 v39, v39, v208\n"
    "v_mbcnt_lo_u32_b32 v206, -1, 0\n"
    "v_mbcnt_hi_u32_b32 v206, -1, v206\n"
    "v_and_b32 v207, 15, v206\n"
    "v_lshrrev_b32 v208, 4, v206\n"
    "s_movk_i32 s59, 1152\n"
    "v_mul_lo_u32 v207, v207, s59\n"
    "v_lshlrev_b32 v208, 3, v208\n"
    "v_add_u32 v207, v207, v208\n"
    "s_lshl_b32 s59, s77, 7\n"
    "s_lshl_b32 s60, s62, 5\n"
    "s_add_u32 s59, s59, s60\n"
    "s_mul_i32 s59, s59, 1152\n"
    "s_mul_i32 s60, s78, 288\n"
    "s_add_u32 s59, s59, s60\n"
    "v_cvt_pk_bf16_f32 v0, v0, v1\n"
    "v_cvt_pk_bf16_f32 v1, v2, v3\n"
    "s_add_u32 s60, s59, 0\n"
    "buffer_store_dwordx2 v[0:1], v207, s[48:51], s60 offen\n"
    "v_cvt_pk_bf16_f32 v4, v4, v5\n"
    "v_cvt_pk_bf16_f32 v5, v6, v7\n"
    "s_add_u32 s60, s59, 18432\n"
    "buffer_store_dwordx2 v[4:5], v207, s[48:51], s60 offen\n"
    "v_cvt_pk_bf16_f32 v8, v8, v9\n"
    "v_cvt_pk_bf16_f32 v9, v10, v11\n"
    "s_add_u32 s60, s59, 32\n"
    "buffer_store_dwordx2 v[8:9], v207, s[48:51], s60 offen\n"
    "v_cvt_pk_bf16_f32 v12, v12, v13\n"
    "v_cvt_pk_bf16_f32 v13, v14, v15\n"
    "s_add_u32 s60, s59, 18464\n"
    "buffer_store_dwordx2 v[12:13], v207, s[48:51], s60 offen\n"
    "v_cvt_pk_bf16_f32 v16, v16, v17\n"
    "v_cvt_pk_bf16_f32 v17, v18, v19\n"
    "s_add_u32 s60, s59, 64\n"
    "buffer_store_dwordx2 v[16:17], v207, s[48:51], s60 offen\n"
    "v_cvt_pk_bf16_f32 v20, v20, v21\n"
    "v_cvt_pk_bf16_f32 v21, v22, v23\n"
    "s_add_u32 s60, s59, 18496\n"
    "buffer_store_dwordx2 v[20:21], v207, s[48:51], s60 offen\n"
    "v_cvt_pk_bf16_f32 v24, v24, v25\n"
    "v_cvt_pk_bf16_f32 v25, v26, v27\n"
    "s_add_u32 s60, s59, 96\n"
    "buffer_store_dwordx2 v[24:25], v207, s[48:51], s60 offen\n"
    "v_cvt_pk_bf16_f32 v28, v28, v29\n"
    "v_cvt_pk_bf16_f32 v29, v30, v31\n"
    "s_add_u32 s60, s59, 18528\n"
    "buffer_store_dwordx2 v[28:29], v207, s[48:51], s60 offen\n"
    "v_cvt_pk_bf16_f32 v32, v32, v33\n"
    "v_cvt_pk_bf16_f32 v33, v34, v35\n"
    "s_add_u32 s60, s59, 128\n"
    "buffer_store_dwordx2 v[32:33], v207, s[48:51], s60 offen\n"
    "v_cvt_pk_bf16_f32 v36, v36, v37\n"
    "v_cvt_pk_bf16_f32 v37, v38, v39\n"
    "s_add_u32 s60, s59, 18560\n"
    "buffer_store_dwordx2 v[36:37], v207, s[48:51], s60 offen\n"
    "s_branch L2END%=\n"
    "L2K1%=:\n"
    "ds_read_b128 v[108:111], v201 offset:10240\n"
    "ds_read_b128 v[112:115], v201 offset:11264\n"
    "ds_read_b128 v[116:119], v201 offset:12288\n"
    "ds_read_b128 v[120:123], v201 offset:13312\n"
    "ds_read_b128 v[124:127], v201 offset:14336\n"
    "ds_read_b128 v[128:131], v201 offset:15360\n"
    "ds_read_b128 v[132:135], v201 offset:16384\n"
    "ds_read_b128 v[136:139], v201 offset:17408\n"
    "s_waitcnt lgkmcnt(0)\n"
    "v_add_f32 v40, v40, v108\n"
    "v_add_f32 v41, v41, v109\n"
    "v_add_f32 v42, v42, v110\n"
    "v_add_f32 v43, v43, v111\n"
    "v_add_f32 v44, v44, v112\n"
    "v_add_f32 v45, v45, v113\n"
    "v_add_f32 v46, v46, v114\n"
    "v_add_f32 v47, v47, v115\n"
    "v_add_f32 v48, v48, v116\n"
    "v_add_f32 v49, v49, v117\n"
    "v_add_f32 v50, v50, v118\n"
    "v_add_f32 v51, v51, v119\n"
    "v_add_f32 v52, v52, v120\n"
    "v_add_f32 v53, v53, v121\n"
    "v_add_f32 v54, v54, v122\n"
    "v_add_f32 v55, v55, v123\n"
    "v_add_f32 v56, v56, v124\n"
    "v_add_f32 v57, v57, v125\n"
    "v_add_f32 v58, v58, v126\n"
    "v_add_f32 v59, v59, v127\n"
    "v_add_f32 v60, v60, v128\n"
    "v_add_f32 v61, v61, v129\n"
    "v_add_f32 v62, v62, v130\n"
    "v_add_f32 v63, v63, v131\n"
    "v_add_f32 v64, v64, v132\n"
    "v_add_f32 v65, v65, v133\n"
    "v_add_f32 v66, v66, v134\n"
    "v_add_f32 v67, v67, v135\n"
    "v_add_f32 v68, v68, v136\n"
    "v_add_f32 v69, v69, v137\n"
    "v_add_f32 v70, v70, v138\n"
    "v_add_f32 v71, v71, v139\n"
    "s_cmp_eq_u32 s57, s62\n"
    "s_cbranch_scc1 L2OWN1%=\n"
    "s_lshl_b32 s59, s77, 2\n"
    "s_add_u32 s59, s59, s78\n"
    "s_lshl_b32 s59, s59, 2\n"
    "s_add_u32 s59, s59, s57\n"
    "s_mul_i32 s59, s59, 3\n"
    "s_sub_u32 s60, s62, s57\n"
    "s_sub_u32 s60, s60, 1\n"
    "s_and_b32 s60, s60, 3\n"
    "s_add_u32 s59, s59, s60\n"
    "s_mul_i32 s59, s59, 9216\n"
    "v_cvt_pk_bf16_f32 v108, v40, v41\n"
    "v_cvt_pk_bf16_f32 v109, v42, v43\n"
    "v_cvt_pk_bf16_f32 v110, v44, v45\n"
    "v_cvt_pk_bf16_f32 v111, v46, v47\n"
    "v_min_u32 v108, 0xfffffffe, v108\n"
    "v_min_u32 v109, 0xfffffffe, v109\n"
    "v_min_u32 v110, 0xfffffffe, v110\n"
    "v_min_u32 v111, 0xfffffffe, v111\n"
    "s_add_u32 s60, s59, 5120\n"
    "buffer_store_dwordx4 v[108:111], v200, s[44:47], s60 offen sc1\n"
    "v_cvt_pk_bf16_f32 v112, v48, v49\n"
    "v_cvt_pk_bf16_f32 v113, v50, v51\n"
    "v_cvt_pk_bf16_f32 v114, v52, v53\n"
    "v_cvt_pk_bf16_f32 v115, v54, v55\n"
    "v_min_u32 v112, 0xfffffffe, v112\n"
    "v_min_u32 v113, 0xfffffffe, v113\n"
    "v_min_u32 v114, 0xfffffffe, v114\n"
    "v_min_u32 v115, 0xfffffffe, v115\n"
    "s_add_u32 s60, s59, 6144\n"
    "buffer_store_dwordx4 v[112:115], v200, s[44:47], s60 offen sc1\n"
    "v_cvt_pk_bf16_f32 v116, v56, v57\n"
    "v_cvt_pk_bf16_f32 v117, v58, v59\n"
    "v_cvt_pk_bf16_f32 v118, v60, v61\n"
    "v_cvt_pk_bf16_f32 v119, v62, v63\n"
    "v_min_u32 v116, 0xfffffffe, v116\n"
    "v_min_u32 v117, 0xfffffffe, v117\n"
    "v_min_u32 v118, 0xfffffffe, v118\n"
    "v_min_u32 v119, 0xfffffffe, v119\n"
    "s_add_u32 s60, s59, 7168\n"
    "buffer_store_dwordx4 v[116:119], v200, s[44:47], s60 offen sc1\n"
    "v_cvt_pk_bf16_f32 v120, v64, v65\n"
    "v_cvt_pk_bf16_f32 v121, v66, v67\n"
    "v_cvt_pk_bf16_f32 v122, v68, v69\n"
    "v_cvt_pk_bf16_f32 v123, v70, v71\n"
    "v_min_u32 v120, 0xfffffffe, v120\n"
    "v_min_u32 v121, 0xfffffffe, v121\n"
    "v_min_u32 v122, 0xfffffffe, v122\n"
    "v_min_u32 v123, 0xfffffffe, v123\n"
    "s_add_u32 s60, s59, 8192\n"
    "buffer_store_dwordx4 v[120:123], v200, s[44:47], s60 offen sc1\n"
    "s_branch L2END%=\n"
    "L2OWN1%=:\n"
    "s_lshl_b32 s59, s77, 2\n"
    "s_add_u32 s59, s59, s78\n"
    "s_lshl_b32 s59, s59, 2\n"
    "s_add_u32 s59, s59, s57\n"
    "s_mul_i32 s79, s59, 27648\n"
    "s_add_u32 s64, s79, 0\n"
    "s_add_u32 s65, s79, 9216\n"
    "s_add_u32 s66, s79, 18432\n"
    "s_mov_b32 s61, 0\n"
    "s_mov_b64 exec, 1\n"
    "L2HINT1%=:\n"
    "s_add_u32 s60, s64, 8192\n"
    "buffer_load_dword v108, v200, s[44:47], s60 offen sc1\n"
    "s_add_u32 s60, s65, 8192\n"
    "buffer_load_dword v109, v200, s[44:47], s60 offen sc1\n"
    "s_add_u32 s60, s66, 8192\n"
    "buffer_load_dword v110, v200, s[44:47], s60 offen sc1\n"
    "s_waitcnt vmcnt(0)\n"
    "v_max3_u32 v206, v108, v109, v110\n"
    "v_cmp_eq_u32 vcc, -1, v206\n"
    "s_cbranch_vccz L2HOK1%=\n"
    "s_add_u32 s61, s61, 1\n"
    "s_cmp_lt_u32 s61, 0x100000\n"
    "s_cbranch_scc0 L2HOK1%=\n"
    "s_sleep 4\n"
    "s_branch L2HINT1%=\n"
    "L2HOK1%=:\n"
    "s_mov_b64 exec, -1\n"
    "L2POLL1%=:\n"
    "s_add_u32 s60, s64, 5120\n"
    "buffer_load_dwordx4 v[108:111], v200, s[44:47], s60 offen sc1\n"
    "s_add_u32 s60, s64, 6144\n"
    "buffer_load_dwordx4 v[112:115], v200, s[44:47], s60 offen sc1\n"
    "s_add_u32 s60, s64, 7168\n"
    "buffer_load_dwordx4 v[116:119], v200, s[44:47], s60 offen sc1\n"
    "s_add_u32 s60, s64, 8192\n"
    "buffer_load_dwordx4 v[120:123], v200, s[44:47], s60 offen sc1\n"
    "s_add_u32 s60, s65, 5120\n"
    "buffer_load_dwordx4 v[124:127], v200, s[44:47], s60 offen sc1\n"
    "s_add_u32 s60, s65, 6144\n"
    "buffer_load_dwordx4 v[128:131], v200, s[44:47], s60 offen sc1\n"
    "s_add_u32 s60, s65, 7168\n"
    "buffer_load_dwordx4 v[132:135], v200, s[44:47], s60 offen sc1\n"
    "s_add_u32 s60, s65, 8192\n"
    "buffer_load_dwordx4 v[136:139], v200, s[44:47], s60 offen sc1\n"
    "s_add_u32 s60, s66, 5120\n"
    "buffer_load_dwordx4 v[140:143], v200, s[44:47], s60 offen sc1\n"
    "s_add_u32 s60, s66, 6144\n"
    "buffer_load_dwordx4 v[144:147], v200, s[44:47], s60 offen sc1\n"
    "s_add_u32 s60, s66, 7168\n"
    "buffer_load_dwordx4 v[148:151], v200, s[44:47], s60 offen sc1\n"
    "s_add_u32 s60, s66, 8192\n"
    "buffer_load_dwordx4 v[152:155], v200, s[44:47], s60 offen sc1\n"
    "s_waitcnt vmcnt(0)\n"
    "v_max_u32 v206, v108, v109\n"
    "v_max3_u32 v206, v206, v110, v111\n"
    "v_max3_u32 v206, v206, v112, v113\n"
    "v_max3_u32 v206, v206, v114, v115\n"
    "v_max3_u32 v206, v206, v116, v117\n"
    "v_max3_u32 v206, v206, v118, v119\n"
    "v_max3_u32 v206, v206, v120, v121\n"
    "v_max3_u32 v206, v206, v122, v123\n"
    "v_max3_u32 v206, v206, v124, v125\n"
    "v_max3_u32 v206, v206, v126, v127\n"
    "v_max3_u32 v206, v206, v128, v129\n"
    "v_max3_u32 v206, v206, v130, v131\n"
    "v_max3_u32 v206, v206, v132, v133\n"
    "v_max3_u32 v206, v206, v134, v135\n"
    "v_max3_u32 v206, v206, v136, v137\n"
    "v_max3_u32 v206, v206, v138, v139\n"
    "v_max3_u32 v206, v206, v140, v141\n"
    "v_max3_u32 v206, v206, v142, v143\n"
    "v_max3_u32 v206, v206, v144, v145\n"
    "v_max3_u32 v206, v206, v146, v147\n"
    "v_max3_u32 v206, v206, v148, v149\n"
    "v_max3_u32 v206, v206, v150, v151\n"
    "v_max3_u32 v206, v206, v152, v153\n"
    "v_max3_u32 v206, v206, v154, v155\n"
    "v_cmp_eq_u32 vcc, -1, v206\n"
    "s_cbranch_vccz L2GOT1%=\n"
    "s_add_u32 s61, s61, 1\n"
    "s_cmp_lt_u32 s61, 0x100000\n"
    "s_cbranch_scc0 L2GOT1%=\n"
    "s_sleep 4\n"
    "s_branch L2POLL1%=\n"
    "L2GOT1%=:\n"
    "v_lshlrev_b32 v207, 16, v108\n"
    "v_and_b32 v208, 0xffff0000, v108\n"
    "v_add_f32 v40, v40, v207\n"
    "v_add_f32 v41, v41, v208\n"
    "v_lshlrev_b32 v207, 16, v109\n"
    "v_and_b32 v208, 0xffff0000, v109\n"
    "v_add_f32 v42, v42, v207\n"
    "v_add_f32 v43, v43, v208\n"
    "v_lshlrev_b32 v207, 16, v110\n"
    "v_and_b32 v208, 0xffff0000, v110\n"
    "v_add_f32 v44, v44, v207\n"
    "v_add_f32 v45, v45, v208\n"
    "v_lshlrev_b32 v207, 16, v111\n"
    "v_and_b32 v208, 0xffff0000, v111\n"
    "v_add_f32 v46, v46, v207\n"
    "v_add_f32 v47, v47, v208\n"
    "v_lshlrev_b32 v207, 16, v112\n"
    "v_and_b32 v208, 0xffff0000, v112\n"
    "v_add_f32 v48, v48, v207\n"
    "v_add_f32 v49, v49, v208\n"
    "v_lshlrev_b32 v207, 16, v113\n"
    "v_and_b32 v208, 0xffff0000, v113\n"
    "v_add_f32 v50, v50, v207\n"
    "v_add_f32 v51, v51, v208\n"
    "v_lshlrev_b32 v207, 16, v114\n"
    "v_and_b32 v208, 0xffff0000, v114\n"
    "v_add_f32 v52, v52, v207\n"
    "v_add_f32 v53, v53, v208\n"
    "v_lshlrev_b32 v207, 16, v115\n"
    "v_and_b32 v208, 0xffff0000, v115\n"
    "v_add_f32 v54, v54, v207\n"
    "v_add_f32 v55, v55, v208\n"
    "v_lshlrev_b32 v207, 16, v116\n"
    "v_and_b32 v208, 0xffff0000, v116\n"
    "v_add_f32 v56, v56, v207\n"
    "v_add_f32 v57, v57, v208\n"
    "v_lshlrev_b32 v207, 16, v117\n"
    "v_and_b32 v208, 0xffff0000, v117\n"
    "v_add_f32 v58, v58, v207\n"
    "v_add_f32 v59, v59, v208\n"
    "v_lshlrev_b32 v207, 16, v118\n"
    "v_and_b32 v208, 0xffff0000, v118\n"
    "v_add_f32 v60, v60, v207\n"
    "v_add_f32 v61, v61, v208\n"
    "v_lshlrev_b32 v207, 16, v119\n"
    "v_and_b32 v208, 0xffff0000, v119\n"
    "v_add_f32 v62, v62, v207\n"
    "v_add_f32 v63, v63, v208\n"
    "v_lshlrev_b32 v207, 16, v120\n"
    "v_and_b32 v208, 0xffff0000, v120\n"
    "v_add_f32 v64, v64, v207\n"
    "v_add_f32 v65, v65, v208\n"
    "v_lshlrev_b32 v207, 16, v121\n"
    "v_and_b32 v208, 0xffff0000, v121\n"
    "v_add_f32 v66, v66, v207\n"
    "v_add_f32 v67, v67, v208\n"
    "v_lshlrev_b32 v207, 16, v122\n"
    "v_and_b32 v208, 0xffff0000, v122\n"
    "v_add_f32 v68, v68, v207\n"
    "v_add_f32 v69, v69, v208\n"
    "v_lshlrev_b32 v207, 16, v123\n"
    "v_and_b32 v208, 0xffff0000, v123\n"
    "v_add_f32 v70, v70, v207\n"
    "v_add_f32 v71, v71, v208\n"
    "v_lshlrev_b32 v207, 16, v124\n"
    "v_and_b32 v208, 0xffff0000, v124\n"
    "v_add_f32 v40, v40, v207\n"
    "v_add_f32 v41, v41, v208\n"
    "v_lshlrev_b32 v207, 16, v125\n"
    "v_and_b32 v208, 0xffff0000, v125\n"
    "v_add_f32 v42, v42, v207\n"
    "v_add_f32 v43, v43, v208\n"
    "v_lshlrev_b32 v207, 16, v126\n"
    "v_and_b32 v208, 0xffff0000, v126\n"
    "v_add_f32 v44, v44, v207\n"
    "v_add_f32 v45, v45, v208\n"
    "v_lshlrev_b32 v207, 16, v127\n"
    "v_and_b32 v208, 0xffff0000, v127\n"
    "v_add_f32 v46, v46, v207\n"
    "v_add_f32 v47, v47, v208\n"
    "v_lshlrev_b32 v207, 16, v128\n"
    "v_and_b32 v208, 0xffff0000, v128\n"
    "v_add_f32 v48, v48, v207\n"
    "v_add_f32 v49, v49, v208\n"
    "v_lshlrev_b32 v207, 16, v129\n"
    "v_and_b32 v208, 0xffff0000, v129\n"
    "v_add_f32 v50, v50, v207\n"
    "v_add_f32 v51, v51, v208\n"
    "v_lshlrev_b32 v207, 16, v130\n"
    "v_and_b32 v208, 0xffff0000, v130\n"
    "v_add_f32 v52, v52, v207\n"
    "v_add_f32 v53, v53, v208\n"
    "v_lshlrev_b32 v207, 16, v131\n"
    "v_and_b32 v208, 0xffff0000, v131\n"
    "v_add_f32 v54, v54, v207\n"
    "v_add_f32 v55, v55, v208\n"
    "v_lshlrev_b32 v207, 16, v132\n"
    "v_and_b32 v208, 0xffff0000, v132\n"
    "v_add_f32 v56, v56, v207\n"
    "v_add_f32 v57, v57, v208\n"
    "v_lshlrev_b32 v207, 16, v133\n"
    "v_and_b32 v208, 0xffff0000, v133\n"
    "v_add_f32 v58, v58, v207\n"
    "v_add_f32 v59, v59, v208\n"
    "v_lshlrev_b32 v207, 16, v134\n"
    "v_and_b32 v208, 0xffff0000, v134\n"
    "v_add_f32 v60, v60, v207\n"
    "v_add_f32 v61, v61, v208\n"
    "v_lshlrev_b32 v207, 16, v135\n"
    "v_and_b32 v208, 0xffff0000, v135\n"
    "v_add_f32 v62, v62, v207\n"
    "v_add_f32 v63, v63, v208\n"
    "v_lshlrev_b32 v207, 16, v136\n"
    "v_and_b32 v208, 0xffff0000, v136\n"
    "v_add_f32 v64, v64, v207\n"
    "v_add_f32 v65, v65, v208\n"
    "v_lshlrev_b32 v207, 16, v137\n"
    "v_and_b32 v208, 0xffff0000, v137\n"
    "v_add_f32 v66, v66, v207\n"
    "v_add_f32 v67, v67, v208\n"
    "v_lshlrev_b32 v207, 16, v138\n"
    "v_and_b32 v208, 0xffff0000, v138\n"
    "v_add_f32 v68, v68, v207\n"
    "v_add_f32 v69, v69, v208\n"
    "v_lshlrev_b32 v207, 16, v139\n"
    "v_and_b32 v208, 0xffff0000, v139\n"
    "v_add_f32 v70, v70, v207\n"
    "v_add_f32 v71, v71, v208\n"
    "v_lshlrev_b32 v207, 16, v140\n"
    "v_and_b32 v208, 0xffff0000, v140\n"
    "v_add_f32 v40, v40, v207\n"
    "v_add_f32 v41, v41, v208\n"
    "v_lshlrev_b32 v207, 16, v141\n"
    "v_and_b32 v208, 0xffff0000, v141\n"
    "v_add_f32 v42, v42, v207\n"
    "v_add_f32 v43, v43, v208\n"
    "v_lshlrev_b32 v207, 16, v142\n"
    "v_and_b32 v208, 0xffff0000, v142\n"
    "v_add_f32 v44, v44, v207\n"
    "v_add_f32 v45, v45, v208\n"
    "v_lshlrev_b32 v207, 16, v143\n"
    "v_and_b32 v208, 0xffff0000, v143\n"
    "v_add_f32 v46, v46, v207\n"
    "v_add_f32 v47, v47, v208\n"
    "v_lshlrev_b32 v207, 16, v144\n"
    "v_and_b32 v208, 0xffff0000, v144\n"
    "v_add_f32 v48, v48, v207\n"
    "v_add_f32 v49, v49, v208\n"
    "v_lshlrev_b32 v207, 16, v145\n"
    "v_and_b32 v208, 0xffff0000, v145\n"
    "v_add_f32 v50, v50, v207\n"
    "v_add_f32 v51, v51, v208\n"
    "v_lshlrev_b32 v207, 16, v146\n"
    "v_and_b32 v208, 0xffff0000, v146\n"
    "v_add_f32 v52, v52, v207\n"
    "v_add_f32 v53, v53, v208\n"
    "v_lshlrev_b32 v207, 16, v147\n"
    "v_and_b32 v208, 0xffff0000, v147\n"
    "v_add_f32 v54, v54, v207\n"
    "v_add_f32 v55, v55, v208\n"
    "v_lshlrev_b32 v207, 16, v148\n"
    "v_and_b32 v208, 0xffff0000, v148\n"
    "v_add_f32 v56, v56, v207\n"
    "v_add_f32 v57, v57, v208\n"
    "v_lshlrev_b32 v207, 16, v149\n"
    "v_and_b32 v208, 0xffff0000, v149\n"
    "v_add_f32 v58, v58, v207\n"
    "v_add_f32 v59, v59, v208\n"
    "v_lshlrev_b32 v207, 16, v150\n"
    "v_and_b32 v208, 0xffff0000, v150\n"
    "v_add_f32 v60, v60, v207\n"
    "v_add_f32 v61, v61, v208\n"
    "v_lshlrev_b32 v207, 16, v151\n"
    "v_and_b32 v208, 0xffff0000, v151\n"
    "v_add_f32 v62, v62, v207\n"
    "v_add_f32 v63, v63, v208\n"
    "v_lshlrev_b32 v207, 16, v152\n"
    "v_and_b32 v208, 0xffff0000, v152\n"
    "v_add_f32 v64, v64, v207\n"
    "v_add_f32 v65, v65, v208\n"
    "v_lshlrev_b32 v207, 16, v153\n"
    "v_and_b32 v208, 0xffff0000, v153\n"
    "v_add_f32 v66, v66, v207\n"
    "v_add_f32 v67, v67, v208\n"
    "v_lshlrev_b32 v207, 16, v154\n"
    "v_and_b32 v208, 0xffff0000, v154\n"
    "v_add_f32 v68, v68, v207\n"
    "v_add_f32 v69, v69, v208\n"
    "v_lshlrev_b32 v207, 16, v155\n"
    "v_and_b32 v208, 0xffff0000, v155\n"
    "v_add_f32 v70, v70, v207\n"
    "v_add_f32 v71, v71, v208\n"
    "v_mbcnt_lo_u32_b32 v206, -1, 0\n"
    "v_mbcnt_hi_u32_b32 v206, -1, v206\n"
    "v_and_b32 v207, 15, v206\n"
    "v_lshrrev_b32 v208, 4, v206\n"
    "s_movk_i32 s59, 1152\n"
    "v_mul_lo_u32 v207, v207, s59\n"
    "v_lshlrev_b32 v208, 3, v208\n"
    "v_add_u32 v207, v207, v208\n"
    "s_lshl_b32 s59, s77, 7\n"
    "s_lshl_b32 s60, s62, 5\n"
    "s_add_u32 s59, s59, s60\n"
    "s_mul_i32 s59, s59, 1152\n"
    "s_mul_i32 s60, s78, 288\n"
    "s_add_u32 s59, s59, s60\n"
    "v_cvt_pk_bf16_f32 v40, v40, v41\n"
    "v_cvt_pk_bf16_f32 v41, v42, v43\n"
    "s_add_u32 s60, s59, 160\n"
    "buffer_store_dwordx2 v[40:41], v207, s[48:51], s60 offen\n"
    "v_cvt_pk_bf16_f32 v44, v44, v45\n"
    "v_cvt_pk_bf16_f32 v45, v46, v47\n"
    "s_add_u32 s60, s59, 18592\n"
    "buffer_store_dwordx2 v[44:45], v207, s[48:51], s60 offen\n"
    "v_cvt_pk_bf16_f32 v48, v48, v49\n"
    "v_cvt_pk_bf16_f32 v49, v50, v51\n"
    "s_add_u32 s60, s59, 192\n"
    "buffer_store_dwordx2 v[48:49], v207, s[48:51], s60 offen\n"
    "v_cvt_pk_bf16_f32 v52, v52, v53\n"
    "v_cvt_pk_bf16_f32 v53, v54, v55\n"
    "s_add_u32 s60, s59, 18624\n"
    "buffer_store_dwordx2 v[52:53], v207, s[48:51], s60 offen\n"
    "v_cvt_pk_bf16_f32 v56, v56, v57\n"
    "v_cvt_pk_bf16_f32 v57, v58, v59\n"
    "s_add_u32 s60, s59, 224\n"
    "buffer_store_dwordx2 v[56:57], v207, s[48:51], s60 offen\n"
    "v_cvt_pk_bf16_f32 v60, v60, v61\n"
    "v_cvt_pk_bf16_f32 v61, v62, v63\n"
    "s_add_u32 s60, s59, 18656\n"
    "buffer_store_dwordx2 v[60:61], v207, s[48:51], s60 offen\n"
    "v_cvt_pk_bf16_f32 v64, v64, v65\n"
    "v_cvt_pk_bf16_f32 v65, v66, v67\n"
    "s_add_u32 s60, s59, 256\n"
    "buffer_store_dwordx2 v[64:65], v207, s[48:51], s60 offen\n"
    "v_cvt_pk_bf16_f32 v68, v68, v69\n"
    "v_cvt_pk_bf16_f32 v69, v70, v71\n"
    "s_add_u32 s60, s59, 18688\n"
    "buffer_store_dwordx2 v[68:69], v207, s[48:51], s60 offen\n"
    "s_branch L2END%=\n"
    "L2END%=:\n"
    "L2_CODE_END:\n"
    "s_waitcnt vmcnt(0)\n"
    :: "s"(ws),"s"(Wp),"s"(out),"s"(TB),"s"(TWp),"s"(hoff),"s"(a3),"s"(nr),"s"(d[0]),"s"(d[1]),"s"(d[2]),"s"(m[0]),"s"(m[1]),"s"(m[2]),"s"(wrec),"s"(mt),"s"(nt),"s"(kq),"s"(w),"s"(bb),"s"(GTp)
    : "memory","scc","vcc","v0","v1","v2","v3","v4","v5","v6","v7","v8","v9","v10","v11","v12","v13","v14","v15","v16","v17","v18","v19","v20","v21","v22","v23","v24","v25","v26","v27","v28","v29","v30","v31","v32","v33","v34","v35","v36","v37","v38","v39","v40","v41","v42","v43","v44","v45","v46","v47","v48","v49","v50","v51","v52","v53","v54","v55","v56","v57","v58","v59","v60","v61","v62","v63","v64","v65","v66","v67","v68","v69","v70","v71","v72","v73","v74","v75","v76","v77","v78","v79","v80","v81","v82","v83","v84","v85","v86","v87","v88","v89","v90","v91","v92","v93","v94","v95","v96","v97","v98","v99","v100","v101","v102","v103","v104","v105","v106","v107","v108","v109","v110","v111","v112","v113","v114","v115","v116","v117","v118","v119","v120","v121","v122","v123","v124","v125","v126","v127","v128","v129","v130","v131","v132","v133","v134","v135","v136","v137","v138","v139","v140","v141","v142","v143","v144","v145","v146","v147","v148","v149","v150","v151","v152","v153","v154","v155","v156","v157","v158","v159","v160","v161","v162","v163","v164","v165","v166","v167","v168","v169","v170","v171","v172","v173","v174","v175","v176","v177","v178","v179","v180","v181","v182","v183","v184","v185","v186","v187","v188","v189","v190","v191","v192","v193","v194","v195","v196","v197","v198","v199","v200","v201","v202","v203","v204","v205","v206","v207","v208","v209","v210","v211","v212","v213","v214","v215","v216","v217","v218","v219","v220","v221","v222","v223","v224","v225","v226","v227","v228","v229","v230","v231","v232","v233","v234","v235","v236","v237","v238","v239","v240","v241","v242","v243","v244","v245","v246","v247","v248","v249","s36","s37","s38","s39","s40","s41","s42","s43","s44","s45","s46","s47","s48","s49","s50","s51","s52","s53","s54","s55","s56","s57","s58","s59","s60","s61","s62","s63","s64","s65","s66","s67","s68","s69","s70","s71","s72","s73","s74","s75","s76","s77","s78","s79","s80","s81","s82","s83","s84","s85","s86","s87","s88","s89","s90","s91","s92","s93","s94","s95","s96","s97","s98","s99");

}

extern "C" void kernel_launch(void* const* d_in, const int* in_sizes, int n_in, void* d_out, int out_size, void* d_ws, size_t ws_size, hipStream_t stream){
  (void)hipFuncSetAttribute((const void*)l1_kernel, hipFuncAttributeMaxDynamicSharedMemorySize, 163840);
  (void)hipFuncSetAttribute((const void*)l2_kernel, hipFuncAttributeMaxDynamicSharedMemorySize, 163840);
  d_ws=(char*)d_ws+0;
  l1_kernel<<<256,512,163840,stream>>>((const u16*)d_in[0],(const u16*)d_in[4],(const u16*)d_in[5],(const u16*)d_in[6],(const u16*)d_in[1],(const u16*)d_in[2],(char*)d_ws,(const u16*)d_in[7]);
  l2_kernel<<<256,512,163840,stream>>>((char*)d_ws,d_out);
}
```
